```python
import torch

def _softmax(x, axis=-1):
    return torch.softmax(x, dim=axis)
def _gelu(x):
    return torch.nn.functional.gelu(x, approximate="tanh")
def _silu(x):
    return torch.nn.functional.silu(x)
def _sigmoid(x):
    return torch.sigmoid(x)
def _layernorm(x, axis=-1, eps=1e-5):
    m = x.mean(dim=axis, keepdim=True)
    v = x.var(dim=axis, keepdim=True, unbiased=False)
    return (x - m) / torch.sqrt(v + eps)
def _scatter_add(tgt, idx, src, axis=0):
    return tgt.clone().index_add_(axis, idx, src)
def _segment_sum(src, seg, n):
    out = torch.zeros((n,) + src.shape[1:], dtype=src.dtype)
    return out.index_add_(0, seg, src)
def _pad(x, pw):
    flat = tuple(v for pair in reversed(pw) for v in pair)
    return torch.nn.functional.pad(x, flat)

kernel_name = 'standalone__glu_gated_geglu__prompt'

def setup_inputs():

    _w = (lambda a: (lambda t: t.to(torch.bfloat16) if t.is_floating_point() else t)(torch.as_tensor(a)))
    B, S, Dm, Df = 1, 2048, 640, 1728
    return {
        "x": _w((lambda *s: torch.randn(*s, dtype=torch.bfloat16) * 0.05)(B, S, Dm)),
        "w_gate": _w((lambda *s: torch.randn(*s, dtype=torch.bfloat16) * 0.05)(Dm, Df) * (Dm ** -0.5)),
        "w_up": _w((lambda *s: torch.randn(*s, dtype=torch.bfloat16) * 0.05)(Dm, Df) * (Dm ** -0.5)),
        "w_down": _w((lambda *s: torch.randn(*s, dtype=torch.bfloat16) * 0.05)(Df, Dm) * (Df ** -0.5)),
    }

def reference(x, w_gate, w_up, w_down):
    g = torch.einsum("bsd,df->bsf", x, w_gate)
    u = torch.einsum("bsd,df->bsf", x, w_up)
    h = _gelu(g) * u
    return torch.einsum("bsf,fd->bsd", h, w_down)
```

```cpp
#include <hip/hip_runtime.h>
#include <hip/hip_bf16.h>

#define LAS __attribute__((address_space(3)))
typedef __bf16 bf16x8 __attribute__((ext_vector_type(8)));
typedef __bf16 bf16x4 __attribute__((ext_vector_type(4)));
typedef __bf16 bf16x2 __attribute__((ext_vector_type(2)));
typedef float f32x4 __attribute__((ext_vector_type(4)));
typedef float f32x2 __attribute__((ext_vector_type(2)));
typedef unsigned u32x4 __attribute__((ext_vector_type(4)));
typedef unsigned u32x2 __attribute__((ext_vector_type(2)));

constexpr int M_ = 2048, D_ = 640, F_ = 1728;

#define WAIT_V(n) asm volatile("s_waitcnt vmcnt(" #n ")" ::: "memory")
#define WAIT_L(n) asm volatile("s_waitcnt lgkmcnt(" #n ")" ::: "memory")
#define BAR() __builtin_amdgcn_s_barrier()

#define ST(i)

__device__ __forceinline__ bf16x8 cat8(bf16x4 a, bf16x4 b) { return __builtin_shufflevector(a, b, 0, 1, 2, 3, 4, 5, 6, 7); }
__device__ __forceinline__ bf16x4 trd(LAS char* p) { return __builtin_amdgcn_ds_read_tr16_b64_v4bf16((LAS bf16x4*)p); }
__device__ __forceinline__ unsigned pk(float a, float b) { bf16x2 v = {(__bf16)a, (__bf16)b}; return __builtin_bit_cast(unsigned, v); }
__device__ __forceinline__ float geglu(float g, float u) {
    const float c1 = -2.302208198f, c2 = -0.1029432397f;
    const float a = g * __builtin_fmaf(g * g, c2, c1);
    const float e = __builtin_amdgcn_exp2f(a);
    return g * u * __builtin_amdgcn_rcpf(1.0f + e);
}
__device__ __forceinline__ u32x4 mksrd(const void* p, unsigned bytes) {
    const unsigned long long a = (unsigned long long)p;
    return (u32x4){(unsigned)a, (unsigned)(a >> 32) & 0xffffu, bytes, 0x00020000u};
}
__device__ __forceinline__ void dma(u32x4 r, LAS char* dst, unsigned voff, unsigned soff) {
    asm volatile("s_mov_b32 m0, %0\n\ts_nop 0\n\tbuffer_load_dwordx4 %1, %2, %3 offen lds" :: "s"((unsigned)(size_t)dst), "v"(voff), "s"(r), "s"(soff) : "memory", "m0");
}

constexpr int P1_STAGE = 49152;
__global__ __launch_bounds__(512, 2) void k_up(const void* __restrict__ x, const void* __restrict__ wg, const void* __restrict__ wu,
                                               void* __restrict__ h, unsigned long long* stamps) {
    extern __shared__ __attribute__((aligned(16))) char smem_[];
    LAS char* smem = (LAS char*)smem_;
    const int tid = threadIdx.x, lane = tid & 63, w = __builtin_amdgcn_readfirstlane(tid >> 6);
    const int xcd = blockIdx.x & 7, j = blockIdx.x >> 3;
    const int fgrp = xcd & 3, mgrp = xcd >> 2;
    const int fs = fgrp * 7 + (j >> 2), pm = mgrp * 4 + (j & 3);
    if (fs >= 27 || j >= 28) return;
    ST(0);
    const int row0 = pm * 256, f0 = fs * 64;
    const u32x4 rx = mksrd(x, M_ * D_ * 2);
    const u32x4 rg = mksrd(wg, D_ * F_ * 2);
    const u32x4 ru = mksrd(wu, D_ * F_ * 2);
    const unsigned vx = (lane >> 3) * 1280 + (((lane & 7) ^ ((lane >> 3) & 6)) * 16);
    const unsigned gsw = ((lane >> 4) & 1) | ((w & 1) << 1);
    const unsigned vw = (lane >> 3) * 3456 + (((lane & 7) ^ (gsw << 1)) * 16);
    const unsigned sx0 = (row0 + 32 * w) * 1280, sw0 = 8 * w * 3456 + f0 * 2;
    auto stage = [&](int kt, int slot) {
        LAS char* b = smem + slot * P1_STAGE;
#pragma unroll
        for (int i = 0; i < 4; ++i) dma(rx, b + w * 4096 + i * 1024, vx, sx0 + i * 10240 + kt * 128);
        dma(rg, b + 32768 + w * 1024, vw, sw0 + kt * 64 * 3456);
        dma(ru, b + 40960 + w * 1024, vw, sw0 + kt * 64 * 3456);
    };
    const int wm = w >> 1, wn = w & 1;
    const int q4 = lane >> 4;
    const int ax0 = (wm * 64 + (lane & 15)) * 128 + ((q4 ^ (lane & 6)) * 16);
    const int tq = (lane & 15) >> 2, tp = lane & 3;
    const int tsw = (((tq >> 1) & 1) | ((q4 & 1) << 1)) << 1;
    const int aw0 = 32768 + (8 * q4 + tq) * 128 + (((wn * 4 + (tp >> 1)) ^ tsw) * 16) + 8 * (tp & 1);
    f32x4 ag[4][2] = {}, au[4][2] = {};
    stage(0, 0); stage(1, 1);
#pragma unroll 1
    for (int kt = 0; kt < 10; ++kt) {
        if (kt < 9) WAIT_V(6); else WAIT_V(0);
        BAR();
        if (kt == 0) ST(1);
        if (kt + 2 < 10) stage(kt + 2, (kt + 2) % 3);
        LAS char* b = smem + (kt % 3) * P1_STAGE;
#pragma unroll
        for (int ks = 0; ks < 2; ++ks) {
            bf16x8 xf[4], gf[2], uf[2];
#pragma unroll
            for (int n = 0; n < 2; ++n) {
                LAS char* p = b + (aw0 ^ (n * 32)) + ks * 4096;
                gf[n] = cat8(trd(p), trd(p + 512));
                uf[n] = cat8(trd(p + 8192), trd(p + 8192 + 512));
            }
#pragma unroll
            for (int m = 0; m < 4; ++m) xf[m] = *(LAS bf16x8*)(b + (ax0 ^ (ks * 64)) + m * 2048);
#pragma unroll
            for (int m = 0; m < 4; ++m)
#pragma unroll
                for (int n = 0; n < 2; ++n) {
                    ag[m][n] = __builtin_amdgcn_mfma_f32_16x16x32_bf16(gf[n], xf[m], ag[m][n], 0, 0, 0);
                    au[m][n] = __builtin_amdgcn_mfma_f32_16x16x32_bf16(uf[n], xf[m], au[m][n], 0, 0, 0);
                }
        }
    }
    ST(2);
    const int fo = f0 + wn * 32 + (q4 & 1) * 16 + (q4 >> 1) * 8;
#pragma unroll
    for (int m = 0; m < 4; ++m) {
        unsigned d[2][2];
#pragma unroll
        for (int n = 0; n < 2; ++n) {
            d[n][0] = pk(geglu(ag[m][n][0], au[m][n][0]), geglu(ag[m][n][1], au[m][n][1]));
            d[n][1] = pk(geglu(ag[m][n][2], au[m][n][2]), geglu(ag[m][n][3], au[m][n][3]));
        }
        auto s0 = __builtin_amdgcn_permlane16_swap(d[0][0], d[1][0], false, false);
        auto s1 = __builtin_amdgcn_permlane16_swap(d[0][1], d[1][1], false, false);
        u32x4 v = {s0[0], s1[0], s0[1], s1[1]};
        const int tok = row0 + wm * 64 + m * 16 + (lane & 15);
        *(u32x4*)((char*)h + (size_t)tok * (F_ * 2) + fo * 2) = v;
    }
    ST(3);
}

constexpr int P2_STAGE = 20480;
constexpr int P2_NS = 7;
__global__ __launch_bounds__(256, 1) void k_down(const void* __restrict__ h, const void* __restrict__ wd, void* __restrict__ out, unsigned long long* stamps) {
    extern __shared__ __attribute__((aligned(16))) char smem_[];
    LAS char* smem = (LAS char*)smem_;
    const int tid = threadIdx.x, lane = tid & 63, w = __builtin_amdgcn_readfirstlane(tid >> 6);
    const int xcd = blockIdx.x & 7, j = blockIdx.x >> 3;
    const int pm = (xcd >> 1) * 8 + (j & 7), pn = (xcd & 1) * 4 + (j >> 3);
    ST(8);
    const int row0 = pm * 64, nb = pn * 160, nb0 = nb & ~63;
    const u32x4 rh = mksrd(h, M_ * F_ * 2);
    const u32x4 rw = mksrd(wd, F_ * D_ * 2);
    const unsigned vh = (lane >> 3) * 3456 + (((lane & 7) ^ ((lane >> 3) & 6)) * 16);
    unsigned vw[3];
#pragma unroll
    for (int i = 0; i < 3; ++i) { const int c = (w * 3 + i) * 64 + lane, r = c / 12, cc = c % 12; vw[i] = r * 1280 + ((cc ^ (((r >> 3) & 1) << 1)) * 16) + nb0; }
    const unsigned sh0 = (row0 + 16 * w) * 3456;
    auto stage = [&](int kt, int slot) {
        LAS char* b = smem + slot * P2_STAGE;
#pragma unroll
        for (int i = 0; i < 2; ++i) dma(rh, b + w * 2048 + i * 1024, vh, sh0 + i * 8 * 3456 + kt * 128);
#pragma unroll
        for (int i = 0; i < 3; ++i) dma(rw, b + 8192 + (w * 3 + i) * 1024, vw[i], kt * 64 * 1280);
    };
    const int wm = w & 1, wk = w >> 1;
    const int q4 = lane >> 4;
    const int ah0 = (wm * 32 + (lane & 15)) * 128 + (((wk * 4 + q4) ^ (lane & 6)) * 16);
    const int tq = (lane & 15) >> 2, tp = lane & 3;
    const int cb = (nb - nb0) >> 4;
    int aw[5];
#pragma unroll
    for (int n = 0; n < 5; ++n) aw[n] = 8192 + (wk * 32 + 8 * q4 + tq) * 192 + (((cb + n * 2 + (tp >> 1)) ^ ((q4 & 1) << 1)) * 16) + 8 * (tp & 1);
    f32x4 acc[2][5] = {};
#pragma unroll
    for (int s = 0; s < P2_NS - 1; ++s) stage(s, s);
    int slot = 0, pslot = P2_NS - 1;
#pragma unroll 1
    for (int kt = 0; kt < 27; ++kt) {
        if (kt <= 27 - P2_NS + 1 - 1) WAIT_V(25); else WAIT_V(0);
        BAR();
        if (kt == 0) ST(9);
        if (kt + P2_NS - 1 < 27) stage(kt + P2_NS - 1, pslot);
        LAS char* b = smem + slot * P2_STAGE;
        bf16x8 hf[2], wf[5];
#pragma unroll
        for (int n = 0; n < 5; ++n) wf[n] = cat8(trd(b + aw[n]), trd(b + aw[n] + 4 * 192));
#pragma unroll
        for (int m = 0; m < 2; ++m) hf[m] = *(LAS bf16x8*)(b + ah0 + m * 2048);
#pragma unroll
        for (int m = 0; m < 2; ++m)
#pragma unroll
            for (int n = 0; n < 5; ++n) acc[m][n] = __builtin_amdgcn_mfma_f32_16x16x32_bf16(wf[n], hf[m], acc[m][n], 0, 0, 0);
        pslot = slot; slot = slot + 1 == P2_NS ? 0 : slot + 1;
    }
    ST(10);
    BAR();
    LAS f32x4* ex = (LAS f32x4*)smem;
#pragma unroll
    for (int n = 0; n < 5; ++n) ex[(w * 5 + n) * 64 + lane] = wk ? acc[0][n] : acc[1][n];
    WAIT_L(0); BAR();
    const int tok = row0 + wm * 32 + wk * 16 + (lane & 15);
#pragma unroll
    for (int n = 0; n < 5; ++n) {
        const f32x4 o = ex[((w ^ 2) * 5 + n) * 64 + lane] + (wk ? acc[1][n] : acc[0][n]);
        u32x2 v = {pk(o[0], o[1]), pk(o[2], o[3])};
        *(u32x2*)((char*)out + (size_t)tok * (D_ * 2) + nb + n * 32 + q4 * 8) = v;
    }
    ST(11);
}

extern "C" void kernel_launch(void* const* d_in, const int* in_sizes, int n_in, void* d_out, int out_size, void* d_ws, size_t ws_size, hipStream_t stream) {
    (void)in_sizes; (void)n_in; (void)out_size; (void)ws_size;
    char* ws = (char*)d_ws;
    void* h = ws;
    unsigned long long* stamps = (unsigned long long*)(ws + (16 << 20));
    hipFuncSetAttribute((const void*)k_up, hipFuncAttributeMaxDynamicSharedMemorySize, 3 * P1_STAGE);
    hipFuncSetAttribute((const void*)k_down, hipFuncAttributeMaxDynamicSharedMemorySize, P2_NS * P2_STAGE);
    k_up<<<256, 512, 3 * P1_STAGE, stream>>>(d_in[0], d_in[1], d_in[2], h, stamps);
    k_down<<<256, 256, P2_NS * P2_STAGE, stream>>>(h, d_in[3], d_out, stamps);
}
```

```cpp
#include <hip/hip_runtime.h>
#include <hip/hip_bf16.h>

#define LAS __attribute__((address_space(3)))
#define GAS __attribute__((address_space(1)))
typedef __bf16 bf16x2 __attribute__((ext_vector_type(2)));
typedef float f32x2 __attribute__((ext_vector_type(2)));
typedef float f32x4 __attribute__((ext_vector_type(4)));
typedef unsigned u32x4 __attribute__((ext_vector_type(4)));
typedef unsigned long long u64;

constexpr int M_ = 2048, D_ = 640, F_ = 1728;
constexpr unsigned XROW = D_ * 2, WROW = F_ * 2;
constexpr int P1_SLOT = 49152;
constexpr int WD_SLOTS = 12, WD_SLOT = 8192, H_BASE = 98304, H_SLOTS = 5, H_SLOT = 12288;
constexpr int MISC = H_BASE + H_SLOTS * H_SLOT;
constexpr int LDS_BYTES = 160 * 1024;
constexpr size_t WS_FLAGS = 8u << 20;
constexpr u64 LCG_A = 6364136223846793005ull, LCG_C = 1442695040888963407ull;

#define WAIT_V(n) asm volatile("s_waitcnt vmcnt(" #n ")" ::: "memory")
#define WAIT_VI(n) asm volatile("s_waitcnt vmcnt(%0)" :: "i"(n) : "memory")
#define WAIT_L(n) asm volatile("s_waitcnt lgkmcnt(" #n ")" ::: "memory")
#define BAR() asm volatile("s_barrier" ::: "memory")
#define DI __device__ __forceinline__

#define ST(i)

#define FCLOB "v160","v161","v162","v163","v164","v165","v166","v167","v168","v169","v170","v171","v172","v173","v174","v175","v176","v177","v178","v179","v180","v181","v182","v183","v184","v185","v186","v187","v188","v189","v190","v191", \
  "v192","v193","v194","v195","v196","v197","v198","v199","v200","v201","v202","v203","v204","v205","v206","v207","v208","v209","v210","v211","v212","v213","v214","v215","v216","v217","v218","v219","v220","v221","v222","v223", \
  "v224","v225","v226","v227","v228","v229","v230","v231","v232","v233","v234","v235","v236","v237","v238","v239","v240","v241","v242","v243","v244","v245","v246","v247","v248","v249","v250","v251","v252","v253","v254","v255"
#define OWN_FRAGS() asm volatile("" ::: FCLOB)
constexpr int FR = 160;

DI unsigned rfl(unsigned v) { return __builtin_amdgcn_readfirstlane(v); }
DI u32x4 mksrd(const void* p, unsigned bytes) {
    const u64 a = (u64)p;
    return (u32x4){(unsigned)a, (unsigned)(a >> 32) & 0xffffu, bytes, 0x00020000u};
}
#define DMA_HEAD "s_mov_b32 m0, %0\n\ts_nop 0\n\t"
DI void dma4(unsigned dst, u32x4 r, unsigned soff, unsigned v0, unsigned v1, unsigned v2, unsigned v3) {
    asm volatile(DMA_HEAD "buffer_load_dwordx4 %3, %1, %2 offen lds\n\tbuffer_load_dwordx4 %4, %1, %2 offen offset:1024 lds\n\t"
                 "buffer_load_dwordx4 %5, %1, %2 offen offset:2048 lds\n\tbuffer_load_dwordx4 %6, %1, %2 offen offset:3072 lds"
                 :: "s"(dst), "s"(r), "s"(soff), "v"(v0), "v"(v1), "v"(v2), "v"(v3) : "memory", "m0");
}
DI void dma2(unsigned dst, u32x4 r, unsigned soff, unsigned v0, unsigned v1) {
    asm volatile(DMA_HEAD "buffer_load_dwordx4 %3, %1, %2 offen lds\n\tbuffer_load_dwordx4 %4, %1, %2 offen offset:1024 lds"
                 :: "s"(dst), "s"(r), "s"(soff), "v"(v0), "v"(v1) : "memory", "m0");
}
DI void dma22(unsigned dst, u32x4 ra, u32x4 rb, unsigned soff, unsigned v0, unsigned v1) {
    asm volatile(DMA_HEAD "buffer_load_dwordx4 %4, %1, %3 offen lds\n\tbuffer_load_dwordx4 %5, %1, %3 offen offset:1024 lds\n\t"
                 "buffer_load_dwordx4 %4, %2, %3 offen offset:2048 lds\n\tbuffer_load_dwordx4 %5, %2, %3 offen offset:3072 lds"
                 :: "s"(dst), "s"(ra), "s"(rb), "s"(soff), "v"(v0), "v"(v1) : "memory", "m0");
}
DI void dma4_sc1(unsigned dst, u32x4 r, unsigned soff, unsigned v0, unsigned v1, unsigned v2, unsigned v3) {
    asm volatile(DMA_HEAD "buffer_load_dwordx4 %3, %1, %2 offen sc1 lds\n\tbuffer_load_dwordx4 %4, %1, %2 offen offset:1024 sc1 lds\n\t"
                 "buffer_load_dwordx4 %5, %1, %2 offen offset:2048 sc1 lds\n\tbuffer_load_dwordx4 %6, %1, %2 offen offset:3072 sc1 lds"
                 :: "s"(dst), "s"(r), "s"(soff), "v"(v0), "v"(v1), "v"(v2), "v"(v3) : "memory", "m0");
}
DI void dma2_sc1(unsigned dst, u32x4 r, unsigned soff, unsigned v0, unsigned v1) {
    asm volatile(DMA_HEAD "buffer_load_dwordx4 %3, %1, %2 offen sc1 lds\n\tbuffer_load_dwordx4 %4, %1, %2 offen offset:1024 sc1 lds"
                 :: "s"(dst), "s"(r), "s"(soff), "v"(v0), "v"(v1) : "memory", "m0");
}
template <int R, int OFF> DI void rd128(unsigned addr) { asm volatile("ds_read_b128 v[%c1:%c2], %0 offset:%c3" :: "v"(addr), "i"(FR + R), "i"(FR + R + 3), "i"(OFF)); }
template <int R, int OFF> DI void rdtr(unsigned addr) { asm volatile("ds_read_b64_tr_b16 v[%c1:%c2], %0 offset:%c3" :: "v"(addr), "i"(FR + R), "i"(FR + R + 1), "i"(OFF)); }
template <int RA, int RB> DI void mfma(f32x4& acc) {
    asm volatile("v_mfma_f32_16x16x32_bf16 %0, v[%c1:%c2], v[%c3:%c4], %0" : "+v"(acc) : "i"(FR + RA), "i"(FR + RA + 3), "i"(FR + RB), "i"(FR + RB + 3));
}
DI unsigned pk(float a, float b) { bf16x2 v = {(__bf16)a, (__bf16)b}; return __builtin_bit_cast(unsigned, v); }
DI unsigned geglu2(f32x2 g, f32x2 u) {
    const f32x2 c1 = {-2.302208198f, -2.302208198f}, c2 = {-0.1029432397f, -0.1029432397f};
    const f32x2 a = g * __builtin_elementwise_fma(g * g, c2, c1);
    const f32x2 d = (f32x2){__builtin_amdgcn_exp2f(a.x), __builtin_amdgcn_exp2f(a.y)} + 1.0f;
    const f32x2 r = (g * u) * (f32x2){__builtin_amdgcn_rcpf(d.x), __builtin_amdgcn_rcpf(d.y)};
    return pk(r.x, r.y);
}

template <int S, int KS, int I> DI void p1_read(unsigned ax, unsigned aw0, unsigned aw1) {
    constexpr int B = 48 * S;
    if constexpr (I < 8) { constexpr int n = I >> 2, up = (I >> 1) & 1, t = I & 1;
        rdtr<B + 32 + 8 * up + 4 * n + 2 * t, 32768 + KS * 8192 + up * 2048 + t * 512>(n ? aw1 : aw0); }
    else rd128<B + 4 * (I - 8), (I - 8) * 2048>(ax);
}
template <int S, int I> DI void p1_mfma(f32x4 (&ag)[8][2], f32x4 (&au)[8][2]) {
    constexpr int B = 48 * S, m = I >> 2, n = (I >> 1) & 1, up = I & 1;
    if constexpr (up) mfma<B + 40 + 4 * n, B + 4 * m>(au[m][n]); else mfma<B + 32 + 4 * n, B + 4 * m>(ag[m][n]);
}
template <int SR, int KS, int I> struct P1Stage {
    static DI void run(f32x4 (&ag)[8][2], f32x4 (&au)[8][2], unsigned ax, unsigned aw0, unsigned aw1) {
        p1_mfma<SR ^ 1, I>(ag, au);
        if constexpr (I % 3 != 2 && (I / 3) * 2 + I % 3 < 16) p1_read<SR, KS, (I / 3) * 2 + I % 3>(ax, aw0, aw1);
        if constexpr (I + 1 < 32) P1Stage<SR, KS, I + 1>::run(ag, au, ax, aw0, aw1);
    }
};
template <int S, int KS, int I> DI void p1_reads_only(unsigned ax, unsigned aw0, unsigned aw1) {
    p1_read<S, KS, I>(ax, aw0, aw1);
    if constexpr (I + 1 < 16) p1_reads_only<S, KS, I + 1>(ax, aw0, aw1);
}

template <int S, int I> DI void p2_read(unsigned ah0, unsigned ah1, unsigned aw0, unsigned aw1) {
    constexpr int ks = I / 7, r = I % 7, B = 40 * S + 20 * ks;
    if constexpr (r < 4) { constexpr int n = r >> 1, t = r & 1; rdtr<B + 12 + 4 * n + 2 * t, ks * 4096 + t * 512>(n ? aw1 : aw0); }
    else rd128<B + 4 * (r - 4), (r - 4) * 2048>(ks ? ah1 : ah0);
}
template <int S, int I> DI void p2_mfma(f32x4 (&acc)[3][2]) {
    constexpr int ks = I / 6, m = (I % 6) >> 1, n = I & 1, B = 40 * S + 20 * ks;
    mfma<B + 12 + 4 * n, B + 4 * m>(acc[m][n]);
}
template <int SR, bool MM, int I> struct P2Stage {
    static DI void run(f32x4 (&acc)[3][2], unsigned ah0, unsigned ah1, unsigned aw0, unsigned aw1) {
        if constexpr (MM) p2_mfma<SR ^ 1, I>(acc);
        if constexpr (I < 7) { p2_read<SR, 2 * I>(ah0, ah1, aw0, aw1); p2_read<SR, 2 * I + 1>(ah0, ah1, aw0, aw1); }
        if constexpr (I + 1 < 12) P2Stage<SR, MM, I + 1>::run(acc, ah0, ah1, aw0, aw1);
    }
};
template <int S, int I> DI void p2_mfma_only(f32x4 (&acc)[3][2]) { p2_mfma<S, I>(acc); if constexpr (I + 1 < 12) p2_mfma_only<S, I + 1>(acc); }

template <int NH> DI void h_stream(u32x4 rh, unsigned so, unsigned lh0, int tr, unsigned v0, unsigned v1, unsigned v2, unsigned v3) {
    auto tile = [&](unsigned slot) {
        dma4_sc1(lh0 + slot, rh, so, v0, v1, v2, v3);
        if constexpr (NH == 6) dma2_sc1(lh0 + slot + 4096, rh, so + 32 * WROW, v0, v1);
        so += 128; if (++tr == 27) { tr = 0; so -= 27 * 128; }
    };
#pragma unroll 1
    for (int s = 0; s < H_SLOTS; ++s) tile(s * H_SLOT);
    WAIT_VI(4 * NH); BAR();
    WAIT_VI(3 * NH); BAR();
    unsigned hs = 0;
#pragma unroll 1
    for (int t = 2; t < 24; ++t) {
        WAIT_VI(2 * NH); BAR();
        tile(hs); hs = hs == (H_SLOTS - 1) * H_SLOT ? 0 : hs + H_SLOT;
    }
    WAIT_VI(2 * NH); BAR();
    WAIT_VI(NH); BAR();
    WAIT_V(0); BAR();
}

__global__ __launch_bounds__(512, 2) void geglu_mlp(const void* __restrict__ x, const void* __restrict__ wg, const void* __restrict__ wu,
                                                    const void* __restrict__ wd, void* __restrict__ out, char* __restrict__ ws) {
    extern __shared__ __attribute__((aligned(16))) char smem_[];
    LAS char* smem = (LAS char*)smem_;
    const unsigned lds0 = (unsigned)(size_t)smem;
    const int lane = threadIdx.x & 63, w = rfl(threadIdx.x >> 6);
    const int xcd = blockIdx.x & 7, j = blockIdx.x >> 3;
    const int fs = (xcd & 3) * 7 + (j >> 2), pm = (xcd >> 2) * 4 + (j & 3);
    const bool prod = j < 28 && fs < 27, cons = j < 30;
    const int mt = j / 10, nt = j - mt * 10;
    const int row2 = xcd * 256 + mt * 96, ntok = mt == 2 ? 64 : 96;
    const int rot = (nt * 27) / 10;
    GAS u64* flags = (GAS u64*)(ws + WS_FLAGS);
    GAS u64* epoch = flags + 256;
    void* h = ws;
    const int spare = j >= 28 ? xcd * 4 + (j - 28) : 32 + (xcd >> 2) * 4 + (j & 3);
    const int myslot = prod ? pm * 32 + fs : (spare / 5) * 32 + 27 + spare % 5;

    if (w < 4) {
        if (w == 0) ST(0);
        const u64 e0 = __hip_atomic_load(epoch, __ATOMIC_RELAXED, __HIP_MEMORY_SCOPE_AGENT);
        const int wm = w >> 1, wn = w & 1, q4 = lane >> 4;
        const int tq = (lane & 15) >> 2, tp = lane & 3;
        const unsigned tsw = (((tq >> 1) & 1) | ((q4 & 1) << 1)) << 1;
        const unsigned awc = (((wn * 4 + (tp >> 1)) ^ tsw) * 16) + 8 * (tp & 1);
        const unsigned xsw = (q4 ^ (lane & 6)) * 16;
        if (prod) {
            f32x4 ag[8][2], au[8][2];
#pragma unroll
            for (int m = 0; m < 8; ++m)
#pragma unroll
                for (int n = 0; n < 2; ++n) { ag[m][n] = (f32x4){0.f, 0.f, 0.f, 0.f}; au[m][n] = (f32x4){0.f, 0.f, 0.f, 0.f}; }
            if (threadIdx.x == 0) *(LAS unsigned*)(smem + MISC) = 0u;
            const unsigned axb = lds0 + (wm * 128 + (lane & 15)) * 128 + xsw;
            const unsigned awb = lds0 + (q4 >> 1) * 4096 + (8 * (q4 & 1) + tq) * 128 + awc;
            WAIT_L(0);
            unsigned sl = 2 * P1_SLOT;
            OWN_FRAGS();
            BAR();
            if (w == 0) ST(1);
            p1_reads_only<0, 0, 0>(axb + sl, awb + sl, (awb ^ 32) + sl);
            WAIT_L(0);
#pragma unroll 1
            for (int t = 0; t < 10; ++t) {
                OWN_FRAGS();
                P1Stage<1, 1, 0>::run(ag, au, (axb ^ 64) + sl, awb + sl, (awb ^ 32) + sl);
                WAIT_L(0);
                BAR();
                sl = sl == 2 * P1_SLOT ? 0 : sl + P1_SLOT;
                P1Stage<0, 0, 0>::run(ag, au, axb + sl, awb + sl, (awb ^ 32) + sl);
                WAIT_L(0);
            }
            asm volatile("s_nop 7\n\ts_nop 7" ::: "memory");
            __builtin_amdgcn_sched_barrier(0);
            if (w == 0) ST(2);
            const __amdgpu_buffer_rsrc_t rh = __builtin_amdgcn_make_buffer_rsrc(h, 0, M_ * F_ * 2, 0x00020000);
            const unsigned ho = (pm * 256 + wm * 128 + (lane & 15)) * WROW + (fs * 64 + wn * 32 + (q4 & 1) * 16 + (q4 >> 1) * 8) * 2;
#pragma unroll
            for (int m = 0; m < 8; ++m) {
                unsigned d[2][2];
#pragma unroll
                for (int n = 0; n < 2; ++n) {
                    d[n][0] = geglu2(ag[m][n].xy, au[m][n].xy);
                    d[n][1] = geglu2(ag[m][n].zw, au[m][n].zw);
                }
                const auto s0 = __builtin_amdgcn_permlane16_swap(d[0][0], d[1][0], false, false);
                const auto s1 = __builtin_amdgcn_permlane16_swap(d[0][1], d[1][1], false, false);
                const u32x4 v = {s0[0], s1[0], s0[1], s1[1]};
                __builtin_amdgcn_raw_buffer_store_b128(v, rh, ho + m * 16 * WROW, 0, 16);
            }
            if (w == 0) ST(3);
            WAIT_V(0);
            if (w == 0) ST(4);
            unsigned old = 0;
            if (lane == 0) old = __hip_atomic_fetch_add((LAS unsigned*)(smem + MISC), 1u, __ATOMIC_RELAXED, __HIP_MEMORY_SCOPE_WORKGROUP);
            if (rfl(old) == 3 && lane == 0) __hip_atomic_store(flags + myslot, e0 * LCG_A + LCG_C, __ATOMIC_RELAXED, __HIP_MEMORY_SCOPE_AGENT);
        } else if (w == 0 && lane == 0) {
            __hip_atomic_store(flags + myslot, e0 * LCG_A + LCG_C, __ATOMIC_RELAXED, __HIP_MEMORY_SCOPE_AGENT);
        }
        if (cons) {
            f32x4 acc[3][2];
#pragma unroll
            for (int m = 0; m < 3; ++m) { acc[m][0] = (f32x4){0.f, 0.f, 0.f, 0.f}; acc[m][1] = (f32x4){0.f, 0.f, 0.f, 0.f}; }
            const int tw = ntok >> 1;
            const unsigned ahb = lds0 + H_BASE + (wm * tw + (lane & 15)) * 128 + xsw;
            const unsigned awd = lds0 + (8 * q4 + tq) * 128 + awc;
            unsigned hs = 0, wsl = 0;
            OWN_FRAGS();
            BAR();
            if (w == 0) ST(8);
            P2Stage<0, false, 0>::run(acc, ahb, ahb ^ 64, awd, awd ^ 32);
#pragma unroll 1
            for (int t = 1; t < 27; t += 2) {
                OWN_FRAGS();
                hs = hs == (H_SLOTS - 1) * H_SLOT ? 0 : hs + H_SLOT; wsl = wsl == (WD_SLOTS - 1) * WD_SLOT ? 0 : wsl + WD_SLOT;
                { const unsigned a0 = ahb + hs, a1 = (ahb ^ 64) + hs, b0 = awd + wsl, b1 = (awd ^ 32) + wsl;
                  BAR(); WAIT_L(0);
                  P2Stage<1, true, 0>::run(acc, a0, a1, b0, b1); }
                hs = hs == (H_SLOTS - 1) * H_SLOT ? 0 : hs + H_SLOT; wsl = wsl == (WD_SLOTS - 1) * WD_SLOT ? 0 : wsl + WD_SLOT;
                { const unsigned a0 = ahb + hs, a1 = (ahb ^ 64) + hs, b0 = awd + wsl, b1 = (awd ^ 32) + wsl;
                  BAR(); WAIT_L(0);
                  P2Stage<0, true, 0>::run(acc, a0, a1, b0, b1); }
            }
            WAIT_L(0);
            p2_mfma_only<0, 0>(acc);
            asm volatile("s_nop 7\n\ts_nop 7" ::: "memory");
            __builtin_amdgcn_sched_barrier(0);
            if (w == 0) ST(9);
            const __amdgpu_buffer_rsrc_t ro = __builtin_amdgcn_make_buffer_rsrc(out, 0, M_ * D_ * 2, 0x00020000);
            const unsigned oo = (row2 + wm * tw + (lane & 15)) * XROW + (nt * 64 + wn * 32 + (q4 & 1) * 16 + (q4 >> 1) * 8) * 2;
#pragma unroll
            for (int m = 0; m < 3; ++m) {
                const auto s0 = __builtin_amdgcn_permlane16_swap(pk(acc[m][0][0], acc[m][0][1]), pk(acc[m][1][0], acc[m][1][1]), false, false);
                const auto s1 = __builtin_amdgcn_permlane16_swap(pk(acc[m][0][2], acc[m][0][3]), pk(acc[m][1][2], acc[m][1][3]), false, false);
                const u32x4 v = {s0[0], s1[0], s0[1], s1[1]};
                if (m * 16 < tw) __builtin_amdgcn_raw_buffer_store_b128(v, ro, oo + m * 16 * XROW, 0, 0);
            }
            if (w == 0) ST(10);
        }
    } else {
        const int lw = w - 4;
        u64 e0;
        asm volatile("global_load_dwordx2 %0, %1, off sc1" : "=&v"(e0) : "v"(epoch) : "memory");
        const unsigned r8 = lane >> 3, c8 = lane & 7;
        const unsigned swx = (c8 ^ (r8 & 6)) * 16;
        const unsigned sww0 = (c8 ^ (((r8 >> 1) & 1) << 1)) * 16;
        const unsigned sww1 = (c8 ^ ((((r8 >> 1) & 1) | 2) << 1)) * 16;
        const u32x4 rwd = mksrd(wd, F_ * D_ * 2);
        constexpr unsigned DX = 8 * XROW - 1024, DW = 8 * WROW - 1024;
        const unsigned vd0 = r8 * XROW + sww0, vd1 = r8 * XROW + sww1 + DX, vd2 = vd0 + 2 * DX, vd3 = vd1 + 2 * DX;
        auto wd_batch = [&](int b) {
            int tr = 6 * b + rot; if (tr >= 27) tr -= 27;
            unsigned so = (tr * 64 + lw * 16) * XROW + nt * 128, ld = lds0 + 6 * b * WD_SLOT + lw * 2048;
#pragma unroll 1
            for (int t = 0; t < 6; ++t) {
                dma2(ld, rwd, so, vd0, vd1);
                ld += WD_SLOT; so += 64 * XROW; if (++tr == 27) { tr = 0; so -= 27 * 64 * XROW; }
            }
        };
        if (prod) {
            const u32x4 rx = mksrd(x, M_ * D_ * 2), rg = mksrd(wg, D_ * F_ * 2), ru = mksrd((const char*)wu - 2048, D_ * F_ * 2 + 2048);
            const unsigned vx0 = r8 * XROW + swx, vx1 = vx0 + DX, vx2 = vx0 + 2 * DX, vx3 = vx0 + 3 * DX;
            const unsigned vg0 = r8 * WROW + sww0, vg1 = r8 * WROW + sww1 + DW;
#define ROT1 1
            int kt = ROT1 ? ((j >> 2) + 3 * (j & 3)) % 10 : 0;
            unsigned sx = (pm * 256 + lw * 64) * XROW + kt * 128, sw = (kt * 64 + lw * 16) * WROW + fs * 128;
            auto stage = [&](unsigned slot) {
                const unsigned b = lds0 + slot;
                dma22(b + 32768 + lw * 4096, rg, ru, sw, vg0, vg1);
                dma4(b + lw * 8192, rx, sx, vx0, vx1, vx2, vx3);
                dma4(b + lw * 8192 + 4096, rx, sx + 32 * XROW, vx0, vx1, vx2, vx3);
                sx += 128; sw += 64 * WROW; if (++kt == 10) { kt = 0; sx -= 1280; sw -= 640 * WROW; }
            };
            stage(2 * P1_SLOT); stage(0); stage(P1_SLOT);
            WAIT_V(24); BAR();
            WAIT_V(12); BAR();
            unsigned sl = 2 * P1_SLOT;
#pragma unroll 1
            for (int t = 1; t < 8; ++t) {
                stage(sl);
                sl = sl == 2 * P1_SLOT ? 0 : sl + P1_SLOT;
                WAIT_V(12); BAR();
            }
            wd_batch(0);
            WAIT_V(12); BAR();
            wd_batch(1);
            BAR();
        } else if (cons) {
            wd_batch(0); wd_batch(1);
        }
        if (cons) {
            if (lw < 2) {
                unsigned dummy = 0;
#pragma unroll 1
                for (int t = WD_SLOTS + mt * 2 + lw; t < 27; t += 6) {
                    const int tr = t + rot >= 27 ? t + rot - 27 : t + rot;
                    asm volatile("buffer_load_dword %0, %1, %2, %3 offen" : "+v"(dummy) : "v"(lane * XROW), "s"(rwd), "s"((unsigned)(tr * 64 * XROW + nt * 128)) : "memory");
                }
                WAIT_V(0);
                asm volatile("" :: "v"(dummy));
                if (lw == 0) ST(11);
                int tr = WD_SLOTS + rot; if (tr >= 27) tr -= 27;
                unsigned so = (tr * 64 + lw * 32) * XROW + nt * 128, ld = 0;
                BAR(); BAR();
#pragma unroll 1
                for (int t = 2; t < 12; ++t) {
                    BAR();
                    dma4(lds0 + ld + lw * 4096, rwd, so, vd0, vd1, vd2, vd3);
                    ld += WD_SLOT; so += 64 * XROW; if (++tr == 27) { tr = 0; so -= 27 * 64 * XROW; }
                }
#pragma unroll 1
                for (int t = 12; t < 17; ++t) {
                    WAIT_V(36); BAR();
                    dma4(lds0 + ld + lw * 4096, rwd, so, vd0, vd1, vd2, vd3);
                    ld = ld == (WD_SLOTS - 1) * WD_SLOT ? 0 : ld + WD_SLOT; so += 64 * XROW; if (++tr == 27) { tr = 0; so -= 27 * 64 * XROW; }
                }
                WAIT_V(36); BAR();
                WAIT_V(32); BAR(); WAIT_V(28); BAR(); WAIT_V(24); BAR(); WAIT_V(20); BAR(); WAIT_V(16); BAR();
                WAIT_V(12); BAR(); WAIT_V(8); BAR(); WAIT_V(4); BAR(); WAIT_V(0); BAR();
            } else {
                WAIT_V(0);
                asm volatile("" : "+v"(e0));
                const u64 tag = e0 * LCG_A + LCG_C;
                GAS u64* pf = flags + xcd * 32 + (lane & 31);
                const u64 t0 = __builtin_amdgcn_s_memrealtime();
                for (;;) {
                    const u64 v = __hip_atomic_load(pf, __ATOMIC_RELAXED, __HIP_MEMORY_SCOPE_AGENT);
                    if (__all(v == tag)) break;
                    if (__builtin_amdgcn_s_memrealtime() - t0 > 5000000ull) break;
                    __builtin_amdgcn_s_sleep(1);
                }
                if (lw == 2) ST(5);
                const u32x4 rh = mksrd(h, M_ * F_ * 2);
                const unsigned v0 = r8 * WROW + swx, v1 = v0 + DW, v2 = v0 + 2 * DW, v3 = v0 + 3 * DW;
                const int hl = lw - 2;
                if (ntok == 96) h_stream<6>(rh, (row2 + hl * 48) * WROW + rot * 128, lds0 + H_BASE + hl * 6144, rot, v0, v1, v2, v3);
                else h_stream<4>(rh, (row2 + hl * 32) * WROW + rot * 128, lds0 + H_BASE + hl * 4096, rot, v0, v1, v2, v3);
            }
        }
        if (blockIdx.x == 0 && lw == 0) {
            WAIT_V(0);
            asm volatile("" : "+v"(e0));
            const u64 tag = e0 * LCG_A + LCG_C;
            const u64 t0 = __builtin_amdgcn_s_memrealtime();
            for (;;) {
                bool ok = true;
#pragma unroll
                for (int i = 0; i < 4; ++i) ok &= __hip_atomic_load(flags + i * 64 + lane, __ATOMIC_RELAXED, __HIP_MEMORY_SCOPE_AGENT) == tag;
                if (__all(ok)) break;
                if (__builtin_amdgcn_s_memrealtime() - t0 > 5000000ull) break;
                __builtin_amdgcn_s_sleep(1);
            }
            if (lane == 0) __hip_atomic_store(epoch, tag, __ATOMIC_RELAXED, __HIP_MEMORY_SCOPE_AGENT);
        }
    }
}

extern "C" void kernel_launch(void* const* d_in, const int* in_sizes, int n_in, void* d_out, int out_size, void* d_ws, size_t ws_size, hipStream_t stream) {
    (void)in_sizes; (void)n_in; (void)out_size; (void)ws_size;
    (void)hipFuncSetAttribute((const void*)geglu_mlp, hipFuncAttributeMaxDynamicSharedMemorySize, LDS_BYTES);
    geglu_mlp<<<256, 512, LDS_BYTES, stream>>>(d_in[0], d_in[1], d_in[2], d_in[3], d_out, (char*)d_ws);
}
```

```cpp
#define CODEPF
#define CODEPF_BACK 256
#define CODEPF_N2 44
#define TLBPF
#include <hip/hip_runtime.h>
#include <hip/hip_bf16.h>

#define LAS __attribute__((address_space(3)))
#define GAS __attribute__((address_space(1)))
typedef __bf16 bf16x2 __attribute__((ext_vector_type(2)));
typedef float f32x2 __attribute__((ext_vector_type(2)));
typedef float f32x4 __attribute__((ext_vector_type(4)));
typedef unsigned u32x4 __attribute__((ext_vector_type(4)));
typedef unsigned long long u64;

#define OUTAUX 0
constexpr int M_ = 2048, D_ = 640, F_ = 1728;
constexpr unsigned XROW = D_ * 2, WROW = F_ * 2;
constexpr int P1_SLOT = 49152;
constexpr int WD_SLOTS = 12, WD_SLOT = 8192, H_BASE = 98304, H_SLOTS = 5, H_SLOT = 12288;
constexpr int MISC = H_BASE + H_SLOTS * H_SLOT;
constexpr int LDS_BYTES = 160 * 1024;
constexpr size_t WS_FLAGS = 8u << 20;
constexpr u64 LCG_A = 6364136223846793005ull, LCG_C = 1442695040888963407ull;

#define WAIT_V(n) asm volatile("s_waitcnt vmcnt(" #n ")" ::: "memory")
#define WAIT_VI(n) asm volatile("s_waitcnt vmcnt(%0)" :: "i"(n) : "memory")
#define WAIT_L(n) asm volatile("s_waitcnt lgkmcnt(" #n ")" ::: "memory")
#define BAR() asm volatile("s_barrier" ::: "memory")
#define DI __device__ __forceinline__

#define ST(i)

#define FCLOB "v160","v161","v162","v163","v164","v165","v166","v167","v168","v169","v170","v171","v172","v173","v174","v175","v176","v177","v178","v179","v180","v181","v182","v183","v184","v185","v186","v187","v188","v189","v190","v191", \
  "v192","v193","v194","v195","v196","v197","v198","v199","v200","v201","v202","v203","v204","v205","v206","v207","v208","v209","v210","v211","v212","v213","v214","v215","v216","v217","v218","v219","v220","v221","v222","v223", \
  "v224","v225","v226","v227","v228","v229","v230","v231","v232","v233","v234","v235","v236","v237","v238","v239","v240","v241","v242","v243","v244","v245","v246","v247","v248","v249","v250","v251","v252","v253","v254","v255"
#define OWN_FRAGS() asm volatile("" ::: FCLOB)
constexpr int FR = 160;

DI unsigned rfl(unsigned v) { return __builtin_amdgcn_readfirstlane(v); }
DI u32x4 mksrd(const void* p, unsigned bytes) {
    const u64 a = (u64)p;
    return (u32x4){(unsigned)a, (unsigned)(a >> 32) & 0xffffu, bytes, 0x00020000u};
}
#define DMA_HEAD "s_mov_b32 m0, %0\n\ts_nop 0\n\t"
DI void dma4(unsigned dst, u32x4 r, unsigned soff, unsigned v0, unsigned v1, unsigned v2, unsigned v3) {
    asm volatile(DMA_HEAD "buffer_load_dwordx4 %3, %1, %2 offen lds\n\tbuffer_load_dwordx4 %4, %1, %2 offen offset:1024 lds\n\t"
                 "buffer_load_dwordx4 %5, %1, %2 offen offset:2048 lds\n\tbuffer_load_dwordx4 %6, %1, %2 offen offset:3072 lds"
                 :: "s"(dst), "s"(r), "s"(soff), "v"(v0), "v"(v1), "v"(v2), "v"(v3) : "memory", "m0");
}
DI void dma2(unsigned dst, u32x4 r, unsigned soff, unsigned v0, unsigned v1) {
    asm volatile(DMA_HEAD "buffer_load_dwordx4 %3, %1, %2 offen lds\n\tbuffer_load_dwordx4 %4, %1, %2 offen offset:1024 lds"
                 :: "s"(dst), "s"(r), "s"(soff), "v"(v0), "v"(v1) : "memory", "m0");
}
DI void dma22(unsigned dst, u32x4 ra, u32x4 rb, unsigned soff, unsigned v0, unsigned v1) {
    asm volatile(DMA_HEAD "buffer_load_dwordx4 %4, %1, %3 offen lds\n\tbuffer_load_dwordx4 %5, %1, %3 offen offset:1024 lds\n\t"
                 "buffer_load_dwordx4 %4, %2, %3 offen offset:2048 lds\n\tbuffer_load_dwordx4 %5, %2, %3 offen offset:3072 lds"
                 :: "s"(dst), "s"(ra), "s"(rb), "s"(soff), "v"(v0), "v"(v1) : "memory", "m0");
}
DI void dma4_sc1(unsigned dst, u32x4 r, unsigned soff, unsigned v0, unsigned v1, unsigned v2, unsigned v3) {
    asm volatile(DMA_HEAD "buffer_load_dwordx4 %3, %1, %2 offen sc1 lds\n\tbuffer_load_dwordx4 %4, %1, %2 offen offset:1024 sc1 lds\n\t"
                 "buffer_load_dwordx4 %5, %1, %2 offen offset:2048 sc1 lds\n\tbuffer_load_dwordx4 %6, %1, %2 offen offset:3072 sc1 lds"
                 :: "s"(dst), "s"(r), "s"(soff), "v"(v0), "v"(v1), "v"(v2), "v"(v3) : "memory", "m0");
}
DI void dma2_sc1(unsigned dst, u32x4 r, unsigned soff, unsigned v0, unsigned v1) {
    asm volatile(DMA_HEAD "buffer_load_dwordx4 %3, %1, %2 offen sc1 lds\n\tbuffer_load_dwordx4 %4, %1, %2 offen offset:1024 sc1 lds"
                 :: "s"(dst), "s"(r), "s"(soff), "v"(v0), "v"(v1) : "memory", "m0");
}
template <int R, int OFF> DI void rd128(unsigned addr) { asm volatile("ds_read_b128 v[%c1:%c2], %0 offset:%c3" :: "v"(addr), "i"(FR + R), "i"(FR + R + 3), "i"(OFF)); }
template <int R, int OFF> DI void rdtr(unsigned addr) { asm volatile("ds_read_b64_tr_b16 v[%c1:%c2], %0 offset:%c3" :: "v"(addr), "i"(FR + R), "i"(FR + R + 1), "i"(OFF)); }
template <int RA, int RB> DI void mfma(f32x4& acc) {
    asm volatile("v_mfma_f32_16x16x32_bf16 %0, v[%c1:%c2], v[%c3:%c4], %0" : "+v"(acc) : "i"(FR + RA), "i"(FR + RA + 3), "i"(FR + RB), "i"(FR + RB + 3));
}
DI unsigned pk(float a, float b) { bf16x2 v = {(__bf16)a, (__bf16)b}; return __builtin_bit_cast(unsigned, v); }
DI unsigned geglu2(f32x2 g, f32x2 u) {
    const f32x2 c1 = {-2.302208198f, -2.302208198f}, c2 = {-0.1029432397f, -0.1029432397f};
    const f32x2 a = g * __builtin_elementwise_fma(g * g, c2, c1);
    const f32x2 d = (f32x2){__builtin_amdgcn_exp2f(a.x), __builtin_amdgcn_exp2f(a.y)} + 1.0f;
    const f32x2 r = (g * u) * (f32x2){__builtin_amdgcn_rcpf(d.x), __builtin_amdgcn_rcpf(d.y)};
    return pk(r.x, r.y);
}

template <int S, int KS, int I> DI void p1_read(unsigned ax, unsigned aw0, unsigned aw1) {
    constexpr int B = 48 * S;
    if constexpr (I < 8) { constexpr int n = I >> 2, up = (I >> 1) & 1, t = I & 1;
        rdtr<B + 32 + 8 * up + 4 * n + 2 * t, 32768 + KS * 8192 + up * 2048 + t * 512>(n ? aw1 : aw0); }
    else rd128<B + 4 * (I - 8), (I - 8) * 2048>(ax);
}
template <int S, int I> DI void p1_mfma(f32x4 (&ag)[8][2], f32x4 (&au)[8][2]) {
    constexpr int B = 48 * S, m = I >> 2, n = (I >> 1) & 1, up = I & 1;
    if constexpr (up) mfma<B + 40 + 4 * n, B + 4 * m>(au[m][n]); else mfma<B + 32 + 4 * n, B + 4 * m>(ag[m][n]);
}
template <int SR, int KS, int I> struct P1Stage {
    static DI void run(f32x4 (&ag)[8][2], f32x4 (&au)[8][2], unsigned ax, unsigned aw0, unsigned aw1) {
        p1_mfma<SR ^ 1, I>(ag, au);
        if constexpr (I % 3 != 2 && (I / 3) * 2 + I % 3 < 16) p1_read<SR, KS, (I / 3) * 2 + I % 3>(ax, aw0, aw1);
        if constexpr (I + 1 < 32) P1Stage<SR, KS, I + 1>::run(ag, au, ax, aw0, aw1);
    }
};
template <int S, int KS, int I> DI void p1_reads_only(unsigned ax, unsigned aw0, unsigned aw1) {
    p1_read<S, KS, I>(ax, aw0, aw1);
    if constexpr (I + 1 < 16) p1_reads_only<S, KS, I + 1>(ax, aw0, aw1);
}

template <int S, int I> DI void p2_read(unsigned ah0, unsigned ah1, unsigned aw0, unsigned aw1) {
    constexpr int ks = I / 7, r = I % 7, B = 40 * S + 20 * ks;
    if constexpr (r < 4) { constexpr int n = r >> 1, t = r & 1; rdtr<B + 12 + 4 * n + 2 * t, ks * 4096 + t * 512>(n ? aw1 : aw0); }
    else rd128<B + 4 * (r - 4), (r - 4) * 2048>(ks ? ah1 : ah0);
}
template <int S, int I> DI void p2_mfma(f32x4 (&acc)[3][2]) {
    constexpr int ks = I / 6, m = (I % 6) >> 1, n = I & 1, B = 40 * S + 20 * ks;
    mfma<B + 12 + 4 * n, B + 4 * m>(acc[m][n]);
}
template <int SR, bool MM, int I> struct P2Stage {
    static DI void run(f32x4 (&acc)[3][2], unsigned ah0, unsigned ah1, unsigned aw0, unsigned aw1) {
        if constexpr (MM) p2_mfma<SR ^ 1, I>(acc);
        if constexpr (I < 7) { p2_read<SR, 2 * I>(ah0, ah1, aw0, aw1); p2_read<SR, 2 * I + 1>(ah0, ah1, aw0, aw1); }
        if constexpr (I + 1 < 12) P2Stage<SR, MM, I + 1>::run(acc, ah0, ah1, aw0, aw1);
    }
};
template <int S, int I> DI void p2_mfma_only(f32x4 (&acc)[3][2]) { p2_mfma<S, I>(acc); if constexpr (I + 1 < 12) p2_mfma_only<S, I + 1>(acc); }

template <int NH> DI void h_stream(u32x4 rh, unsigned so, unsigned lh0, int tr, unsigned v0, unsigned v1, unsigned v2, unsigned v3) {
    auto tile = [&](unsigned slot) {
        dma4_sc1(lh0 + slot, rh, so, v0, v1, v2, v3);
        if constexpr (NH == 6) dma2_sc1(lh0 + slot + 4096, rh, so + 32 * WROW, v0, v1);
        so += 128; if (++tr == 27) { tr = 0; so -= 27 * 128; }
    };
#pragma unroll 1
    for (int s = 0; s < H_SLOTS; ++s) tile(s * H_SLOT);
    WAIT_VI(4 * NH); BAR();
    WAIT_VI(3 * NH); BAR();
    unsigned hs = 0;
#pragma unroll 1
    for (int t = 2; t < 24; ++t) {
        WAIT_VI(2 * NH); BAR();
        tile(hs); hs = hs == (H_SLOTS - 1) * H_SLOT ? 0 : hs + H_SLOT;
    }
    WAIT_VI(2 * NH); BAR();
    WAIT_VI(NH); BAR();
    WAIT_V(0); BAR();
}

__global__ __launch_bounds__(512, 2) void geglu_mlp(const void* __restrict__ x, const void* __restrict__ wg, const void* __restrict__ wu,
                                                    const void* __restrict__ wd, void* __restrict__ out, char* __restrict__ ws) {
    extern __shared__ __attribute__((aligned(16))) char smem_[];
    LAS char* smem = (LAS char*)smem_;
    const unsigned lds0 = (unsigned)(size_t)smem;
    const int lane = threadIdx.x & 63, w = rfl(threadIdx.x >> 6);
    const int xcd = blockIdx.x & 7, j = blockIdx.x >> 3;
    const int fs = (xcd & 3) * 7 + (j >> 2), pm = (xcd >> 2) * 4 + (j & 3);
    const bool prod = j < 28 && fs < 27, cons = j < 30;
    const int mt = j / 10, nt = j - mt * 10;
    const int row2 = xcd * 256 + mt * 96, ntok = mt == 2 ? 64 : 96;
    const int rot = (nt * 27) / 10;
    GAS u64* flags = (GAS u64*)(ws + WS_FLAGS);
    GAS u64* epoch = flags + 256;
    void* h = ws;
    const int spare = j >= 28 ? xcd * 4 + (j - 28) : 32 + (xcd >> 2) * 4 + (j & 3);
    const int myslot = prod ? pm * 32 + fs : (spare / 5) * 32 + 27 + spare % 5;

    if (w < 4) {
        if (w == 0) ST(0);
        const u64 e0 = __hip_atomic_load(epoch, __ATOMIC_RELAXED, __HIP_MEMORY_SCOPE_AGENT);
        const int wm = w >> 1, wn = w & 1, q4 = lane >> 4;
        const int tq = (lane & 15) >> 2, tp = lane & 3;
        const unsigned tsw = (((tq >> 1) & 1) | ((q4 & 1) << 1)) << 1;
        const unsigned awc = (((wn * 4 + (tp >> 1)) ^ tsw) * 16) + 8 * (tp & 1);
        const unsigned xsw = (q4 ^ (lane & 6)) * 16;
        if (prod) {
            f32x4 ag[8][2], au[8][2];
#pragma unroll
            for (int m = 0; m < 8; ++m)
#pragma unroll
                for (int n = 0; n < 2; ++n) { ag[m][n] = (f32x4){0.f, 0.f, 0.f, 0.f}; au[m][n] = (f32x4){0.f, 0.f, 0.f, 0.f}; }
            if (threadIdx.x == 0) *(LAS unsigned*)(smem + MISC) = 0u;
            const unsigned axb = lds0 + (wm * 128 + (lane & 15)) * 128 + xsw;
            const unsigned awb = lds0 + (q4 >> 1) * 4096 + (8 * (q4 & 1) + tq) * 128 + awc;
            WAIT_L(0);
            unsigned sl = 2 * P1_SLOT;
            OWN_FRAGS();
            BAR();
            if (w == 0) ST(1);
            p1_reads_only<0, 0, 0>(axb + sl, awb + sl, (awb ^ 32) + sl);
            WAIT_L(0);
#pragma unroll 1
            for (int t = 0; t < 10; ++t) {
                OWN_FRAGS();
                P1Stage<1, 1, 0>::run(ag, au, (axb ^ 64) + sl, awb + sl, (awb ^ 32) + sl);
                WAIT_L(0);
                BAR();
                sl = sl == 2 * P1_SLOT ? 0 : sl + P1_SLOT;
                P1Stage<0, 0, 0>::run(ag, au, axb + sl, awb + sl, (awb ^ 32) + sl);
                WAIT_L(0);
            }
            asm volatile("s_nop 7\n\ts_nop 7" ::: "memory");
            __builtin_amdgcn_sched_barrier(0);
            if (w == 0) ST(2);
            const __amdgpu_buffer_rsrc_t rh = __builtin_amdgcn_make_buffer_rsrc(h, 0, 8u << 20, 0x00020000);
            const unsigned ho = (pm >> 1) * (2u << 20) + ((pm & 1) * 256 + wm * 128 + (lane & 15)) * WROW + (fs * 64 + wn * 32 + (q4 & 1) * 16 + (q4 >> 1) * 8) * 2;
#pragma unroll
            for (int m = 0; m < 8; ++m) {
                unsigned d[2][2];
#pragma unroll
                for (int n = 0; n < 2; ++n) {
                    d[n][0] = geglu2(ag[m][n].xy, au[m][n].xy);
                    d[n][1] = geglu2(ag[m][n].zw, au[m][n].zw);
                }
                const auto s0 = __builtin_amdgcn_permlane16_swap(d[0][0], d[1][0], false, false);
                const auto s1 = __builtin_amdgcn_permlane16_swap(d[0][1], d[1][1], false, false);
                const u32x4 v = {s0[0], s1[0], s0[1], s1[1]};
                __builtin_amdgcn_raw_buffer_store_b128(v, rh, ho + m * 16 * WROW, 0, 16);
            }
            if (w == 0) ST(3);
            WAIT_V(0);
            if (w == 0) ST(4);
            unsigned old = 0;
            if (lane == 0) old = __hip_atomic_fetch_add((LAS unsigned*)(smem + MISC), 1u, __ATOMIC_RELAXED, __HIP_MEMORY_SCOPE_WORKGROUP);
            if (rfl(old) == 3 && lane == 0) __hip_atomic_store(flags + myslot, e0 * LCG_A + LCG_C, __ATOMIC_RELAXED, __HIP_MEMORY_SCOPE_AGENT);
        } else if (w == 0 && lane == 0) {
            __hip_atomic_store(flags + myslot, e0 * LCG_A + LCG_C, __ATOMIC_RELAXED, __HIP_MEMORY_SCOPE_AGENT);
        }
        if (cons) {
            f32x4 acc[3][2];
#pragma unroll
            for (int m = 0; m < 3; ++m) { acc[m][0] = (f32x4){0.f, 0.f, 0.f, 0.f}; acc[m][1] = (f32x4){0.f, 0.f, 0.f, 0.f}; }
            const int tw = ntok >> 1;
            const unsigned ahb = lds0 + H_BASE + (wm * tw + (lane & 15)) * 128 + xsw;
            const unsigned awd = lds0 + (8 * q4 + tq) * 128 + awc;
            unsigned hs = 0, wsl = 0;
            OWN_FRAGS();
            BAR();
            if (w == 0) ST(8);
            P2Stage<0, false, 0>::run(acc, ahb, ahb ^ 64, awd, awd ^ 32);
#pragma unroll 1
            for (int t = 1; t < 27; t += 2) {
                OWN_FRAGS();
                hs = hs == (H_SLOTS - 1) * H_SLOT ? 0 : hs + H_SLOT; wsl = wsl == (WD_SLOTS - 1) * WD_SLOT ? 0 : wsl + WD_SLOT;
                { const unsigned a0 = ahb + hs, a1 = (ahb ^ 64) + hs, b0 = awd + wsl, b1 = (awd ^ 32) + wsl;
                  BAR(); WAIT_L(0);
                  P2Stage<1, true, 0>::run(acc, a0, a1, b0, b1); }
                hs = hs == (H_SLOTS - 1) * H_SLOT ? 0 : hs + H_SLOT; wsl = wsl == (WD_SLOTS - 1) * WD_SLOT ? 0 : wsl + WD_SLOT;
                { const unsigned a0 = ahb + hs, a1 = (ahb ^ 64) + hs, b0 = awd + wsl, b1 = (awd ^ 32) + wsl;
                  BAR(); WAIT_L(0);
                  P2Stage<0, true, 0>::run(acc, a0, a1, b0, b1); }
            }
            WAIT_L(0);
            p2_mfma_only<0, 0>(acc);
            asm volatile("s_nop 7\n\ts_nop 7" ::: "memory");
            __builtin_amdgcn_sched_barrier(0);
            if (w == 0) ST(9);
            const __amdgpu_buffer_rsrc_t ro = __builtin_amdgcn_make_buffer_rsrc(out, 0, M_ * D_ * 2, 0x00020000);
            const unsigned oo = (row2 + wm * tw + (lane & 15)) * XROW + (nt * 64 + wn * 32 + (q4 & 1) * 16 + (q4 >> 1) * 8) * 2;
#pragma unroll
            for (int m = 0; m < 3; ++m) {
                const auto s0 = __builtin_amdgcn_permlane16_swap(pk(acc[m][0][0], acc[m][0][1]), pk(acc[m][1][0], acc[m][1][1]), false, false);
                const auto s1 = __builtin_amdgcn_permlane16_swap(pk(acc[m][0][2], acc[m][0][3]), pk(acc[m][1][2], acc[m][1][3]), false, false);
                const u32x4 v = {s0[0], s1[0], s0[1], s1[1]};
                if (m * 16 < tw) __builtin_amdgcn_raw_buffer_store_b128(v, ro, oo + m * 16 * XROW, 0, OUTAUX);
            }
            if (w == 0) ST(10);
        }
    } else {
        const int lw = w - 4;
        unsigned cpf0 = 0, cpf1 = 0;
        if (lw == 3) {
            u64 pc; asm volatile("s_getpc_b64 %0" : "=s"(pc));
            const u64 base = pc & ~127ull;
            const char* a = (const char*)base - CODEPF_BACK + lane * 128;
            asm volatile("global_load_dword %0, %1, off" : "=&v"(cpf0) : "v"(a) : "memory");
            const char* b = a + 8192;
            if (lane < CODEPF_N2) asm volatile("global_load_dword %0, %1, off" : "=&v"(cpf1) : "v"(b) : "memory");
        }
        unsigned tpf = 0;
        if (lw == 2) {
            const char* p = lane == 0 ? (const char*)wd : lane == 1 ? (const char*)wd + (F_ * D_ * 2 - 4) : lane == 2 ? (const char*)out : lane == 3 ? (const char*)out + (M_ * D_ * 2 - 4)
                          : (const char*)ws + (size_t)(lane - 4) * (2u << 20) + (2u << 20) - 128;
            if (lane < 8) asm volatile("global_load_dword %0, %1, off" : "=&v"(tpf) : "v"(p) : "memory");
        }
        u64 e0;
        asm volatile("global_load_dwordx2 %0, %1, off sc1" : "=&v"(e0) : "v"(epoch) : "memory");
        const unsigned r8 = lane >> 3, c8 = lane & 7;
        const unsigned swx = (c8 ^ (r8 & 6)) * 16;
        const unsigned sww0 = (c8 ^ (((r8 >> 1) & 1) << 1)) * 16;
        const unsigned sww1 = (c8 ^ ((((r8 >> 1) & 1) | 2) << 1)) * 16;
        const u32x4 rwd = mksrd(wd, F_ * D_ * 2);
        constexpr unsigned DX = 8 * XROW - 1024, DW = 8 * WROW - 1024;
        const unsigned vd0 = r8 * XROW + sww0, vd1 = r8 * XROW + sww1 + DX, vd2 = vd0 + 2 * DX, vd3 = vd1 + 2 * DX;
        auto wd_batch = [&](int b) {
            int tr = 6 * b + rot; if (tr >= 27) tr -= 27;
            unsigned so = (tr * 64 + lw * 16) * XROW + nt * 128, ld = lds0 + 6 * b * WD_SLOT + lw * 2048;
#pragma unroll 1
            for (int t = 0; t < 6; ++t) {
                dma2(ld, rwd, so, vd0, vd1);
                ld += WD_SLOT; so += 64 * XROW; if (++tr == 27) { tr = 0; so -= 27 * 64 * XROW; }
            }
        };
        if (prod) {
            const u32x4 rx = mksrd(x, M_ * D_ * 2), rg = mksrd(wg, D_ * F_ * 2), ru = mksrd((const char*)wu - 2048, D_ * F_ * 2 + 2048);
            const unsigned vx0 = r8 * XROW + swx, vx1 = vx0 + DX, vx2 = vx0 + 2 * DX, vx3 = vx0 + 3 * DX;
            const unsigned vg0 = r8 * WROW + sww0, vg1 = r8 * WROW + sww1 + DW;
#define ROT1 1
            int kt = ROT1 ? ((j >> 2) + 3 * (j & 3)) % 10 : 0;
            unsigned sx = (pm * 256 + lw * 64) * XROW + kt * 128, sw = (kt * 64 + lw * 16) * WROW + fs * 128;
            auto stage = [&](unsigned slot) {
                const unsigned b = lds0 + slot;
                dma22(b + 32768 + lw * 4096, rg, ru, sw, vg0, vg1);
                dma4(b + lw * 8192, rx, sx, vx0, vx1, vx2, vx3);
                dma4(b + lw * 8192 + 4096, rx, sx + 32 * XROW, vx0, vx1, vx2, vx3);
                sx += 128; sw += 64 * WROW; if (++kt == 10) { kt = 0; sx -= 1280; sw -= 640 * WROW; }
            };
            stage(2 * P1_SLOT); stage(0); stage(P1_SLOT);
            WAIT_V(24);
            asm volatile("" :: "v"(cpf0), "v"(cpf1));
            asm volatile("" :: "v"(tpf));
            BAR();
            WAIT_V(12); BAR();
            unsigned sl = 2 * P1_SLOT;
#pragma unroll 1
            for (int t = 1; t < 8; ++t) {
                stage(sl);
                sl = sl == 2 * P1_SLOT ? 0 : sl + P1_SLOT;
                WAIT_V(12); BAR();
            }
            wd_batch(0);
            WAIT_V(12); BAR();
            wd_batch(1);
            BAR();
        } else {
            if (cons) { wd_batch(0); wd_batch(1); }
            WAIT_V(0);
            asm volatile("" :: "v"(cpf0), "v"(cpf1));
            asm volatile("" :: "v"(tpf));
        }
        if (cons) {
            if (lw < 2) {
                unsigned dummy = 0;
#pragma unroll 1
                for (int t = WD_SLOTS + mt * 2 + lw; t < 27; t += 6) {
                    const int tr = t + rot >= 27 ? t + rot - 27 : t + rot;
                    asm volatile("buffer_load_dword %0, %1, %2, %3 offen" : "+v"(dummy) : "v"(lane * XROW), "s"(rwd), "s"((unsigned)(tr * 64 * XROW + nt * 128)) : "memory");
                }
                WAIT_V(0);
                asm volatile("" :: "v"(dummy));
                if (lw == 0) ST(11);
                int tr = WD_SLOTS + rot; if (tr >= 27) tr -= 27;
                unsigned so = (tr * 64 + lw * 32) * XROW + nt * 128, ld = 0;
                BAR(); BAR();
#pragma unroll 1
                for (int t = 2; t < 12; ++t) {
                    BAR();
                    dma4(lds0 + ld + lw * 4096, rwd, so, vd0, vd1, vd2, vd3);
                    ld += WD_SLOT; so += 64 * XROW; if (++tr == 27) { tr = 0; so -= 27 * 64 * XROW; }
                }
#pragma unroll 1
                for (int t = 12; t < 17; ++t) {
                    WAIT_V(36); BAR();
                    dma4(lds0 + ld + lw * 4096, rwd, so, vd0, vd1, vd2, vd3);
                    ld = ld == (WD_SLOTS - 1) * WD_SLOT ? 0 : ld + WD_SLOT; so += 64 * XROW; if (++tr == 27) { tr = 0; so -= 27 * 64 * XROW; }
                }
                WAIT_V(36); BAR();
                WAIT_V(32); BAR(); WAIT_V(28); BAR(); WAIT_V(24); BAR(); WAIT_V(20); BAR(); WAIT_V(16); BAR();
                WAIT_V(12); BAR(); WAIT_V(8); BAR(); WAIT_V(4); BAR(); WAIT_V(0); BAR();
            } else {
                WAIT_V(0);
                asm volatile("" : "+v"(e0));
                const u64 tag = e0 * LCG_A + LCG_C;
                GAS u64* pf = flags + xcd * 32 + (lane & 31);
                const u64 t0 = __builtin_amdgcn_s_memrealtime();
                for (;;) {
                    const u64 v = __hip_atomic_load(pf, __ATOMIC_RELAXED, __HIP_MEMORY_SCOPE_AGENT);
                    if (__all(v == tag)) break;
                    if (__builtin_amdgcn_s_memrealtime() - t0 > 5000000ull) break;
                    __builtin_amdgcn_s_sleep(1);
                }
                if (lw == 2) ST(5);
                const u32x4 rh = mksrd(h, 8u << 20);
                const unsigned v0 = r8 * WROW + swx, v1 = v0 + DW, v2 = v0 + 2 * DW, v3 = v0 + 3 * DW;
                const int hl = lw - 2;
                if (ntok == 96) h_stream<6>(rh, (xcd >> 1) * (2u << 20) + ((xcd & 1) * 256 + mt * 96 + hl * 48) * WROW + rot * 128, lds0 + H_BASE + hl * 6144, rot, v0, v1, v2, v3);
                else h_stream<4>(rh, (xcd >> 1) * (2u << 20) + ((xcd & 1) * 256 + mt * 96 + hl * 32) * WROW + rot * 128, lds0 + H_BASE + hl * 4096, rot, v0, v1, v2, v3);
            }
        }
        if (blockIdx.x == 0 && lw == 0) {
            WAIT_V(0);
            asm volatile("" : "+v"(e0));
            const u64 tag = e0 * LCG_A + LCG_C;
            const u64 t0 = __builtin_amdgcn_s_memrealtime();
            for (;;) {
                bool ok = true;
#pragma unroll
                for (int i = 0; i < 4; ++i) ok &= __hip_atomic_load(flags + i * 64 + lane, __ATOMIC_RELAXED, __HIP_MEMORY_SCOPE_AGENT) == tag;
                if (__all(ok)) break;
                if (__builtin_amdgcn_s_memrealtime() - t0 > 5000000ull) break;
                __builtin_amdgcn_s_sleep(1);
            }
            if (lane == 0) __hip_atomic_store(epoch, tag, __ATOMIC_RELAXED, __HIP_MEMORY_SCOPE_AGENT);
        }
    }
}

extern "C" void kernel_launch(void* const* d_in, const int* in_sizes, int n_in, void* d_out, int out_size, void* d_ws, size_t ws_size, hipStream_t stream) {
    (void)in_sizes; (void)n_in; (void)out_size; (void)ws_size;
    (void)hipFuncSetAttribute((const void*)geglu_mlp, hipFuncAttributeMaxDynamicSharedMemorySize, LDS_BYTES);
    geglu_mlp<<<256, 512, LDS_BYTES, stream>>>(d_in[0], d_in[1], d_in[2], d_in[3], d_out, (char*)d_ws);
}
```

```cpp
#define PROBE_A 4
#define PROBE_B 8
#define CODEPF
#define CODEPF_BACK 256
#define CODEPF_N2 44
#define TLBPF
#include <hip/hip_runtime.h>
#include <hip/hip_bf16.h>

#define LAS __attribute__((address_space(3)))
#define GAS __attribute__((address_space(1)))
typedef __bf16 bf16x2 __attribute__((ext_vector_type(2)));
typedef float f32x2 __attribute__((ext_vector_type(2)));
typedef float f32x4 __attribute__((ext_vector_type(4)));
typedef unsigned u32x4 __attribute__((ext_vector_type(4)));
typedef unsigned long long u64;

#define OUTAUX 0
constexpr int M_ = 2048, D_ = 640, F_ = 1728;
constexpr unsigned XROW = D_ * 2, WROW = F_ * 2;
constexpr int P1_SLOT = 49152;
constexpr int WD_SLOTS = 12, WD_SLOT = 8192, H_BASE = 98304, H_SLOTS = 5, H_SLOT = 12288;
constexpr int MISC = H_BASE + H_SLOTS * H_SLOT;
constexpr int LDS_BYTES = 160 * 1024;
constexpr size_t WS_FLAGS = 8u << 20;
constexpr u64 LCG_A = 6364136223846793005ull, LCG_C = 1442695040888963407ull;

#define WAIT_V(n) asm volatile("s_waitcnt vmcnt(" #n ")" ::: "memory")
#define WAIT_VI(n) asm volatile("s_waitcnt vmcnt(%0)" :: "i"(n) : "memory")
#define WAIT_L(n) asm volatile("s_waitcnt lgkmcnt(" #n ")" ::: "memory")
#define BAR() asm volatile("s_barrier" ::: "memory")
#define DI __device__ __forceinline__

#define ST(i) do { if ((i) == PROBE_A) tpa = __builtin_amdgcn_s_memrealtime(); if ((i) == PROBE_B) tpb = __builtin_amdgcn_s_memrealtime(); } while (0)

#define FCLOB "v160","v161","v162","v163","v164","v165","v166","v167","v168","v169","v170","v171","v172","v173","v174","v175","v176","v177","v178","v179","v180","v181","v182","v183","v184","v185","v186","v187","v188","v189","v190","v191", \
  "v192","v193","v194","v195","v196","v197","v198","v199","v200","v201","v202","v203","v204","v205","v206","v207","v208","v209","v210","v211","v212","v213","v214","v215","v216","v217","v218","v219","v220","v221","v222","v223", \
  "v224","v225","v226","v227","v228","v229","v230","v231","v232","v233","v234","v235","v236","v237","v238","v239","v240","v241","v242","v243","v244","v245","v246","v247","v248","v249","v250","v251","v252","v253","v254","v255"
#define OWN_FRAGS() asm volatile("" ::: FCLOB)
constexpr int FR = 160;

DI unsigned rfl(unsigned v) { return __builtin_amdgcn_readfirstlane(v); }
DI u32x4 mksrd(const void* p, unsigned bytes) {
    const u64 a = (u64)p;
    return (u32x4){(unsigned)a, (unsigned)(a >> 32) & 0xffffu, bytes, 0x00020000u};
}
#define DMA_HEAD "s_mov_b32 m0, %0\n\ts_nop 0\n\t"
DI void dma4(unsigned dst, u32x4 r, unsigned soff, unsigned v0, unsigned v1, unsigned v2, unsigned v3) {
    asm volatile(DMA_HEAD "buffer_load_dwordx4 %3, %1, %2 offen lds\n\tbuffer_load_dwordx4 %4, %1, %2 offen offset:1024 lds\n\t"
                 "buffer_load_dwordx4 %5, %1, %2 offen offset:2048 lds\n\tbuffer_load_dwordx4 %6, %1, %2 offen offset:3072 lds"
                 :: "s"(dst), "s"(r), "s"(soff), "v"(v0), "v"(v1), "v"(v2), "v"(v3) : "memory", "m0");
}
DI void dma2(unsigned dst, u32x4 r, unsigned soff, unsigned v0, unsigned v1) {
    asm volatile(DMA_HEAD "buffer_load_dwordx4 %3, %1, %2 offen lds\n\tbuffer_load_dwordx4 %4, %1, %2 offen offset:1024 lds"
                 :: "s"(dst), "s"(r), "s"(soff), "v"(v0), "v"(v1) : "memory", "m0");
}
DI void dma22(unsigned dst, u32x4 ra, u32x4 rb, unsigned soff, unsigned v0, unsigned v1) {
    asm volatile(DMA_HEAD "buffer_load_dwordx4 %4, %1, %3 offen lds\n\tbuffer_load_dwordx4 %5, %1, %3 offen offset:1024 lds\n\t"
                 "buffer_load_dwordx4 %4, %2, %3 offen offset:2048 lds\n\tbuffer_load_dwordx4 %5, %2, %3 offen offset:3072 lds"
                 :: "s"(dst), "s"(ra), "s"(rb), "s"(soff), "v"(v0), "v"(v1) : "memory", "m0");
}
DI void dma4_sc1(unsigned dst, u32x4 r, unsigned soff, unsigned v0, unsigned v1, unsigned v2, unsigned v3) {
    asm volatile(DMA_HEAD "buffer_load_dwordx4 %3, %1, %2 offen sc1 lds\n\tbuffer_load_dwordx4 %4, %1, %2 offen offset:1024 sc1 lds\n\t"
                 "buffer_load_dwordx4 %5, %1, %2 offen offset:2048 sc1 lds\n\tbuffer_load_dwordx4 %6, %1, %2 offen offset:3072 sc1 lds"
                 :: "s"(dst), "s"(r), "s"(soff), "v"(v0), "v"(v1), "v"(v2), "v"(v3) : "memory", "m0");
}
DI void dma2_sc1(unsigned dst, u32x4 r, unsigned soff, unsigned v0, unsigned v1) {
    asm volatile(DMA_HEAD "buffer_load_dwordx4 %3, %1, %2 offen sc1 lds\n\tbuffer_load_dwordx4 %4, %1, %2 offen offset:1024 sc1 lds"
                 :: "s"(dst), "s"(r), "s"(soff), "v"(v0), "v"(v1) : "memory", "m0");
}
template <int R, int OFF> DI void rd128(unsigned addr) { asm volatile("ds_read_b128 v[%c1:%c2], %0 offset:%c3" :: "v"(addr), "i"(FR + R), "i"(FR + R + 3), "i"(OFF)); }
template <int R, int OFF> DI void rdtr(unsigned addr) { asm volatile("ds_read_b64_tr_b16 v[%c1:%c2], %0 offset:%c3" :: "v"(addr), "i"(FR + R), "i"(FR + R + 1), "i"(OFF)); }
template <int RA, int RB> DI void mfma(f32x4& acc) {
    asm volatile("v_mfma_f32_16x16x32_bf16 %0, v[%c1:%c2], v[%c3:%c4], %0" : "+v"(acc) : "i"(FR + RA), "i"(FR + RA + 3), "i"(FR + RB), "i"(FR + RB + 3));
}
DI unsigned pk(float a, float b) { bf16x2 v = {(__bf16)a, (__bf16)b}; return __builtin_bit_cast(unsigned, v); }
DI unsigned geglu2(f32x2 g, f32x2 u) {
    const f32x2 c1 = {-2.302208198f, -2.302208198f}, c2 = {-0.1029432397f, -0.1029432397f};
    const f32x2 a = g * __builtin_elementwise_fma(g * g, c2, c1);
    const f32x2 d = (f32x2){__builtin_amdgcn_exp2f(a.x), __builtin_amdgcn_exp2f(a.y)} + 1.0f;
    const f32x2 r = (g * u) * (f32x2){__builtin_amdgcn_rcpf(d.x), __builtin_amdgcn_rcpf(d.y)};
    return pk(r.x, r.y);
}

template <int S, int KS, int I> DI void p1_read(unsigned ax, unsigned aw0, unsigned aw1) {
    constexpr int B = 48 * S;
    if constexpr (I < 8) { constexpr int n = I >> 2, up = (I >> 1) & 1, t = I & 1;
        rdtr<B + 32 + 8 * up + 4 * n + 2 * t, 32768 + KS * 8192 + up * 2048 + t * 512>(n ? aw1 : aw0); }
    else rd128<B + 4 * (I - 8), (I - 8) * 2048>(ax);
}
template <int S, int I> DI void p1_mfma(f32x4 (&ag)[8][2], f32x4 (&au)[8][2]) {
    constexpr int B = 48 * S, m = I >> 2, n = (I >> 1) & 1, up = I & 1;
    if constexpr (up) mfma<B + 40 + 4 * n, B + 4 * m>(au[m][n]); else mfma<B + 32 + 4 * n, B + 4 * m>(ag[m][n]);
}
template <int SR, int KS, int I> struct P1Stage {
    static DI void run(f32x4 (&ag)[8][2], f32x4 (&au)[8][2], unsigned ax, unsigned aw0, unsigned aw1) {
        p1_mfma<SR ^ 1, I>(ag, au);
        if constexpr (I % 3 != 2 && (I / 3) * 2 + I % 3 < 16) p1_read<SR, KS, (I / 3) * 2 + I % 3>(ax, aw0, aw1);
        if constexpr (I + 1 < 32) P1Stage<SR, KS, I + 1>::run(ag, au, ax, aw0, aw1);
    }
};
template <int S, int KS, int I> DI void p1_reads_only(unsigned ax, unsigned aw0, unsigned aw1) {
    p1_read<S, KS, I>(ax, aw0, aw1);
    if constexpr (I + 1 < 16) p1_reads_only<S, KS, I + 1>(ax, aw0, aw1);
}

template <int S, int I> DI void p2_read(unsigned ah0, unsigned ah1, unsigned aw0, unsigned aw1) {
    constexpr int ks = I / 7, r = I % 7, B = 40 * S + 20 * ks;
    if constexpr (r < 4) { constexpr int n = r >> 1, t = r & 1; rdtr<B + 12 + 4 * n + 2 * t, ks * 4096 + t * 512>(n ? aw1 : aw0); }
    else rd128<B + 4 * (r - 4), (r - 4) * 2048>(ks ? ah1 : ah0);
}
template <int S, int I> DI void p2_mfma(f32x4 (&acc)[3][2]) {
    constexpr int ks = I / 6, m = (I % 6) >> 1, n = I & 1, B = 40 * S + 20 * ks;
    mfma<B + 12 + 4 * n, B + 4 * m>(acc[m][n]);
}
template <int SR, bool MM, int I> struct P2Stage {
    static DI void run(f32x4 (&acc)[3][2], unsigned ah0, unsigned ah1, unsigned aw0, unsigned aw1) {
        if constexpr (MM) p2_mfma<SR ^ 1, I>(acc);
        if constexpr (I < 7) { p2_read<SR, 2 * I>(ah0, ah1, aw0, aw1); p2_read<SR, 2 * I + 1>(ah0, ah1, aw0, aw1); }
        if constexpr (I + 1 < 12) P2Stage<SR, MM, I + 1>::run(acc, ah0, ah1, aw0, aw1);
    }
};
template <int S, int I> DI void p2_mfma_only(f32x4 (&acc)[3][2]) { p2_mfma<S, I>(acc); if constexpr (I + 1 < 12) p2_mfma_only<S, I + 1>(acc); }

template <int NH> DI void h_stream(u32x4 rh, unsigned so, unsigned lh0, int tr, unsigned v0, unsigned v1, unsigned v2, unsigned v3) {
    auto tile = [&](unsigned slot) {
        dma4_sc1(lh0 + slot, rh, so, v0, v1, v2, v3);
        if constexpr (NH == 6) dma2_sc1(lh0 + slot + 4096, rh, so + 32 * WROW, v0, v1);
        so += 128; if (++tr == 27) { tr = 0; so -= 27 * 128; }
    };
#pragma unroll 1
    for (int s = 0; s < H_SLOTS; ++s) tile(s * H_SLOT);
    WAIT_VI(4 * NH); BAR();
    WAIT_VI(3 * NH); BAR();
    unsigned hs = 0;
#pragma unroll 1
    for (int t = 2; t < 24; ++t) {
        WAIT_VI(2 * NH); BAR();
        tile(hs); hs = hs == (H_SLOTS - 1) * H_SLOT ? 0 : hs + H_SLOT;
    }
    WAIT_VI(2 * NH); BAR();
    WAIT_VI(NH); BAR();
    WAIT_V(0); BAR();
}

__global__ __launch_bounds__(512, 2) void geglu_mlp(const void* __restrict__ x, const void* __restrict__ wg, const void* __restrict__ wu,
                                                    const void* __restrict__ wd, void* __restrict__ out, char* __restrict__ ws) {
    extern __shared__ __attribute__((aligned(16))) char smem_[];
    LAS char* smem = (LAS char*)smem_;
    const unsigned lds0 = (unsigned)(size_t)smem;
    const int lane = threadIdx.x & 63, w = rfl(threadIdx.x >> 6);
    const int xcd = blockIdx.x & 7, j = blockIdx.x >> 3;
    const int fs = (xcd & 3) * 7 + (j >> 2), pm = (xcd >> 2) * 4 + (j & 3);
    const bool prod = j < 28 && fs < 27, cons = j < 30;
    const int mt = j / 10, nt = j - mt * 10;
    const int row2 = xcd * 256 + mt * 96, ntok = mt == 2 ? 64 : 96;
    const int rot = (nt * 27) / 10;
    GAS u64* flags = (GAS u64*)(ws + WS_FLAGS);
    GAS u64* epoch = flags + 256;
    void* h = ws;
    const int spare = j >= 28 ? xcd * 4 + (j - 28) : 32 + (xcd >> 2) * 4 + (j & 3);
    const int myslot = prod ? pm * 32 + fs : (spare / 5) * 32 + 27 + spare % 5;

    u64 tpa = 0, tpb = 0;
    if (w < 4) {
        if (w == 0) ST(0);
        const u64 e0 = __hip_atomic_load(epoch, __ATOMIC_RELAXED, __HIP_MEMORY_SCOPE_AGENT);
        const int wm = w >> 1, wn = w & 1, q4 = lane >> 4;
        const int tq = (lane & 15) >> 2, tp = lane & 3;
        const unsigned tsw = (((tq >> 1) & 1) | ((q4 & 1) << 1)) << 1;
        const unsigned awc = (((wn * 4 + (tp >> 1)) ^ tsw) * 16) + 8 * (tp & 1);
        const unsigned xsw = (q4 ^ (lane & 6)) * 16;
        if (prod) {
            f32x4 ag[8][2], au[8][2];
#pragma unroll
            for (int m = 0; m < 8; ++m)
#pragma unroll
                for (int n = 0; n < 2; ++n) { ag[m][n] = (f32x4){0.f, 0.f, 0.f, 0.f}; au[m][n] = (f32x4){0.f, 0.f, 0.f, 0.f}; }
            if (threadIdx.x == 0) *(LAS unsigned*)(smem + MISC) = 0u;
            const unsigned axb = lds0 + (wm * 128 + (lane & 15)) * 128 + xsw;
            const unsigned awb = lds0 + (q4 >> 1) * 4096 + (8 * (q4 & 1) + tq) * 128 + awc;
            WAIT_L(0);
            unsigned sl = 2 * P1_SLOT;
            OWN_FRAGS();
            BAR();
            if (w == 0) ST(1);
            p1_reads_only<0, 0, 0>(axb + sl, awb + sl, (awb ^ 32) + sl);
            WAIT_L(0);
#pragma unroll 1
            for (int t = 0; t < 10; ++t) {
                OWN_FRAGS();
                P1Stage<1, 1, 0>::run(ag, au, (axb ^ 64) + sl, awb + sl, (awb ^ 32) + sl);
                WAIT_L(0);
                BAR();
                sl = sl == 2 * P1_SLOT ? 0 : sl + P1_SLOT;
                P1Stage<0, 0, 0>::run(ag, au, axb + sl, awb + sl, (awb ^ 32) + sl);
                WAIT_L(0);
            }
            asm volatile("s_nop 7\n\ts_nop 7" ::: "memory");
            __builtin_amdgcn_sched_barrier(0);
            if (w == 0) ST(2);
            const __amdgpu_buffer_rsrc_t rh = __builtin_amdgcn_make_buffer_rsrc(h, 0, 8u << 20, 0x00020000);
            const unsigned ho = (pm >> 1) * (2u << 20) + ((pm & 1) * 256 + wm * 128 + (lane & 15)) * WROW + (fs * 64 + wn * 32 + (q4 & 1) * 16 + (q4 >> 1) * 8) * 2;
#pragma unroll
            for (int m = 0; m < 8; ++m) {
                unsigned d[2][2];
#pragma unroll
                for (int n = 0; n < 2; ++n) {
                    d[n][0] = geglu2(ag[m][n].xy, au[m][n].xy);
                    d[n][1] = geglu2(ag[m][n].zw, au[m][n].zw);
                }
                const auto s0 = __builtin_amdgcn_permlane16_swap(d[0][0], d[1][0], false, false);
                const auto s1 = __builtin_amdgcn_permlane16_swap(d[0][1], d[1][1], false, false);
                const u32x4 v = {s0[0], s1[0], s0[1], s1[1]};
                __builtin_amdgcn_raw_buffer_store_b128(v, rh, ho + m * 16 * WROW, 0, 16);
            }
            if (w == 0) ST(3);
            WAIT_V(0);
            if (w == 0) ST(4);
            unsigned old = 0;
            if (lane == 0) old = __hip_atomic_fetch_add((LAS unsigned*)(smem + MISC), 1u, __ATOMIC_RELAXED, __HIP_MEMORY_SCOPE_WORKGROUP);
            if (rfl(old) == 3 && lane == 0) __hip_atomic_store(flags + myslot, e0 * LCG_A + LCG_C, __ATOMIC_RELAXED, __HIP_MEMORY_SCOPE_AGENT);
        } else if (w == 0 && lane == 0) {
            __hip_atomic_store(flags + myslot, e0 * LCG_A + LCG_C, __ATOMIC_RELAXED, __HIP_MEMORY_SCOPE_AGENT);
        }
        if (cons) {
            f32x4 acc[3][2];
#pragma unroll
            for (int m = 0; m < 3; ++m) { acc[m][0] = (f32x4){0.f, 0.f, 0.f, 0.f}; acc[m][1] = (f32x4){0.f, 0.f, 0.f, 0.f}; }
            const int tw = ntok >> 1;
            const unsigned ahb = lds0 + H_BASE + (wm * tw + (lane & 15)) * 128 + xsw;
            const unsigned awd = lds0 + (8 * q4 + tq) * 128 + awc;
            unsigned hs = 0, wsl = 0;
            OWN_FRAGS();
            BAR();
            if (w == 0) ST(8);
            P2Stage<0, false, 0>::run(acc, ahb, ahb ^ 64, awd, awd ^ 32);
#pragma unroll 1
            for (int t = 1; t < 27; t += 2) {
                OWN_FRAGS();
                hs = hs == (H_SLOTS - 1) * H_SLOT ? 0 : hs + H_SLOT; wsl = wsl == (WD_SLOTS - 1) * WD_SLOT ? 0 : wsl + WD_SLOT;
                { const unsigned a0 = ahb + hs, a1 = (ahb ^ 64) + hs, b0 = awd + wsl, b1 = (awd ^ 32) + wsl;
                  BAR(); WAIT_L(0);
                  P2Stage<1, true, 0>::run(acc, a0, a1, b0, b1); }
                hs = hs == (H_SLOTS - 1) * H_SLOT ? 0 : hs + H_SLOT; wsl = wsl == (WD_SLOTS - 1) * WD_SLOT ? 0 : wsl + WD_SLOT;
                { const unsigned a0 = ahb + hs, a1 = (ahb ^ 64) + hs, b0 = awd + wsl, b1 = (awd ^ 32) + wsl;
                  BAR(); WAIT_L(0);
                  P2Stage<0, true, 0>::run(acc, a0, a1, b0, b1); }
            }
            WAIT_L(0);
            p2_mfma_only<0, 0>(acc);
            asm volatile("s_nop 7\n\ts_nop 7" ::: "memory");
            __builtin_amdgcn_sched_barrier(0);
            if (w == 0) ST(9);
            const __amdgpu_buffer_rsrc_t ro = __builtin_amdgcn_make_buffer_rsrc(out, 0, M_ * D_ * 2, 0x00020000);
            const unsigned oo = (row2 + wm * tw + (lane & 15)) * XROW + (nt * 64 + wn * 32 + (q4 & 1) * 16 + (q4 >> 1) * 8) * 2;
#pragma unroll
            for (int m = 0; m < 3; ++m) {
                const auto s0 = __builtin_amdgcn_permlane16_swap(pk(acc[m][0][0], acc[m][0][1]), pk(acc[m][1][0], acc[m][1][1]), false, false);
                const auto s1 = __builtin_amdgcn_permlane16_swap(pk(acc[m][0][2], acc[m][0][3]), pk(acc[m][1][2], acc[m][1][3]), false, false);
                const u32x4 v = {s0[0], s1[0], s0[1], s1[1]};
                if (m * 16 < tw) __builtin_amdgcn_raw_buffer_store_b128(v, ro, oo + m * 16 * XROW, 0, OUTAUX);
            }
            if (w == 0) ST(10);
            if (w == 0 && blockIdx.x == 9) { const u64 tend = __builtin_amdgcn_s_memrealtime() + 4 * (tpb - tpa); while (__builtin_amdgcn_s_memrealtime() < tend) __builtin_amdgcn_s_sleep(1); }
        }
    } else {
        const int lw = w - 4;
        unsigned cpf0 = 0, cpf1 = 0;
        if (lw == 3) {
            u64 pc; asm volatile("s_getpc_b64 %0" : "=s"(pc));
            const u64 base = pc & ~127ull;
            const char* a = (const char*)base - CODEPF_BACK + lane * 128;
            asm volatile("global_load_dword %0, %1, off" : "=&v"(cpf0) : "v"(a) : "memory");
            const char* b = a + 8192;
            if (lane < CODEPF_N2) asm volatile("global_load_dword %0, %1, off" : "=&v"(cpf1) : "v"(b) : "memory");
        }
        unsigned tpf = 0;
        if (lw == 2) {
            const char* p = lane == 0 ? (const char*)wd : lane == 1 ? (const char*)wd + (F_ * D_ * 2 - 4) : lane == 2 ? (const char*)out : lane == 3 ? (const char*)out + (M_ * D_ * 2 - 4)
                          : (const char*)ws + (size_t)(lane - 4) * (2u << 20) + (2u << 20) - 128;
            if (lane < 8) asm volatile("global_load_dword %0, %1, off" : "=&v"(tpf) : "v"(p) : "memory");
        }
        u64 e0;
        asm volatile("global_load_dwordx2 %0, %1, off sc1" : "=&v"(e0) : "v"(epoch) : "memory");
        const unsigned r8 = lane >> 3, c8 = lane & 7;
        const unsigned swx = (c8 ^ (r8 & 6)) * 16;
        const unsigned sww0 = (c8 ^ (((r8 >> 1) & 1) << 1)) * 16;
        const unsigned sww1 = (c8 ^ ((((r8 >> 1) & 1) | 2) << 1)) * 16;
        const u32x4 rwd = mksrd(wd, F_ * D_ * 2);
        constexpr unsigned DX = 8 * XROW - 1024, DW = 8 * WROW - 1024;
        const unsigned vd0 = r8 * XROW + sww0, vd1 = r8 * XROW + sww1 + DX, vd2 = vd0 + 2 * DX, vd3 = vd1 + 2 * DX;
        auto wd_batch = [&](int b) {
            int tr = 6 * b + rot; if (tr >= 27) tr -= 27;
            unsigned so = (tr * 64 + lw * 16) * XROW + nt * 128, ld = lds0 + 6 * b * WD_SLOT + lw * 2048;
#pragma unroll 1
            for (int t = 0; t < 6; ++t) {
                dma2(ld, rwd, so, vd0, vd1);
                ld += WD_SLOT; so += 64 * XROW; if (++tr == 27) { tr = 0; so -= 27 * 64 * XROW; }
            }
        };
        if (prod) {
            const u32x4 rx = mksrd(x, M_ * D_ * 2), rg = mksrd(wg, D_ * F_ * 2), ru = mksrd((const char*)wu - 2048, D_ * F_ * 2 + 2048);
            const unsigned vx0 = r8 * XROW + swx, vx1 = vx0 + DX, vx2 = vx0 + 2 * DX, vx3 = vx0 + 3 * DX;
            const unsigned vg0 = r8 * WROW + sww0, vg1 = r8 * WROW + sww1 + DW;
#define ROT1 1
            int kt = ROT1 ? ((j >> 2) + 3 * (j & 3)) % 10 : 0;
            unsigned sx = (pm * 256 + lw * 64) * XROW + kt * 128, sw = (kt * 64 + lw * 16) * WROW + fs * 128;
            auto stage = [&](unsigned slot) {
                const unsigned b = lds0 + slot;
                dma22(b + 32768 + lw * 4096, rg, ru, sw, vg0, vg1);
                dma4(b + lw * 8192, rx, sx, vx0, vx1, vx2, vx3);
                dma4(b + lw * 8192 + 4096, rx, sx + 32 * XROW, vx0, vx1, vx2, vx3);
                sx += 128; sw += 64 * WROW; if (++kt == 10) { kt = 0; sx -= 1280; sw -= 640 * WROW; }
            };
            stage(2 * P1_SLOT); stage(0); stage(P1_SLOT);
            WAIT_V(24);
            asm volatile("" :: "v"(cpf0), "v"(cpf1));
            asm volatile("" :: "v"(tpf));
            BAR();
            WAIT_V(12); BAR();
            unsigned sl = 2 * P1_SLOT;
#pragma unroll 1
            for (int t = 1; t < 8; ++t) {
                stage(sl);
                sl = sl == 2 * P1_SLOT ? 0 : sl + P1_SLOT;
                WAIT_V(12); BAR();
            }
            wd_batch(0);
            WAIT_V(12); BAR();
            wd_batch(1);
            BAR();
        } else {
            if (cons) { wd_batch(0); wd_batch(1); }
            WAIT_V(0);
            asm volatile("" :: "v"(cpf0), "v"(cpf1));
            asm volatile("" :: "v"(tpf));
        }
        if (cons) {
            if (lw < 2) {
                unsigned dummy = 0;
#pragma unroll 1
                for (int t = WD_SLOTS + mt * 2 + lw; t < 27; t += 6) {
                    const int tr = t + rot >= 27 ? t + rot - 27 : t + rot;
                    asm volatile("buffer_load_dword %0, %1, %2, %3 offen" : "+v"(dummy) : "v"(lane * XROW), "s"(rwd), "s"((unsigned)(tr * 64 * XROW + nt * 128)) : "memory");
                }
                WAIT_V(0);
                asm volatile("" :: "v"(dummy));
                if (lw == 0) ST(11);
                int tr = WD_SLOTS + rot; if (tr >= 27) tr -= 27;
                unsigned so = (tr * 64 + lw * 32) * XROW + nt * 128, ld = 0;
                BAR(); BAR();
#pragma unroll 1
                for (int t = 2; t < 12; ++t) {
                    BAR();
                    dma4(lds0 + ld + lw * 4096, rwd, so, vd0, vd1, vd2, vd3);
                    ld += WD_SLOT; so += 64 * XROW; if (++tr == 27) { tr = 0; so -= 27 * 64 * XROW; }
                }
#pragma unroll 1
                for (int t = 12; t < 17; ++t) {
                    WAIT_V(36); BAR();
                    dma4(lds0 + ld + lw * 4096, rwd, so, vd0, vd1, vd2, vd3);
                    ld = ld == (WD_SLOTS - 1) * WD_SLOT ? 0 : ld + WD_SLOT; so += 64 * XROW; if (++tr == 27) { tr = 0; so -= 27 * 64 * XROW; }
                }
                WAIT_V(36); BAR();
                WAIT_V(32); BAR(); WAIT_V(28); BAR(); WAIT_V(24); BAR(); WAIT_V(20); BAR(); WAIT_V(16); BAR();
                WAIT_V(12); BAR(); WAIT_V(8); BAR(); WAIT_V(4); BAR(); WAIT_V(0); BAR();
            } else {
                WAIT_V(0);
                asm volatile("" : "+v"(e0));
                const u64 tag = e0 * LCG_A + LCG_C;
                GAS u64* pf = flags + xcd * 32 + (lane & 31);
                const u64 t0 = __builtin_amdgcn_s_memrealtime();
                for (;;) {
                    const u64 v = __hip_atomic_load(pf, __ATOMIC_RELAXED, __HIP_MEMORY_SCOPE_AGENT);
                    if (__all(v == tag)) break;
                    if (__builtin_amdgcn_s_memrealtime() - t0 > 5000000ull) break;
                    __builtin_amdgcn_s_sleep(1);
                }
                if (lw == 2) ST(5);
                const u32x4 rh = mksrd(h, 8u << 20);
                const unsigned v0 = r8 * WROW + swx, v1 = v0 + DW, v2 = v0 + 2 * DW, v3 = v0 + 3 * DW;
                const int hl = lw - 2;
                if (ntok == 96) h_stream<6>(rh, (xcd >> 1) * (2u << 20) + ((xcd & 1) * 256 + mt * 96 + hl * 48) * WROW + rot * 128, lds0 + H_BASE + hl * 6144, rot, v0, v1, v2, v3);
                else h_stream<4>(rh, (xcd >> 1) * (2u << 20) + ((xcd & 1) * 256 + mt * 96 + hl * 32) * WROW + rot * 128, lds0 + H_BASE + hl * 4096, rot, v0, v1, v2, v3);
            }
        }
        if (blockIdx.x == 0 && lw == 0) {
            WAIT_V(0);
            asm volatile("" : "+v"(e0));
            const u64 tag = e0 * LCG_A + LCG_C;
            const u64 t0 = __builtin_amdgcn_s_memrealtime();
            for (;;) {
                bool ok = true;
#pragma unroll
                for (int i = 0; i < 4; ++i) ok &= __hip_atomic_load(flags + i * 64 + lane, __ATOMIC_RELAXED, __HIP_MEMORY_SCOPE_AGENT) == tag;
                if (__all(ok)) break;
                if (__builtin_amdgcn_s_memrealtime() - t0 > 5000000ull) break;
                __builtin_amdgcn_s_sleep(1);
            }
            if (lane == 0) __hip_atomic_store(epoch, tag, __ATOMIC_RELAXED, __HIP_MEMORY_SCOPE_AGENT);
        }
    }
}

extern "C" void kernel_launch(void* const* d_in, const int* in_sizes, int n_in, void* d_out, int out_size, void* d_ws, size_t ws_size, hipStream_t stream) {
    (void)in_sizes; (void)n_in; (void)out_size; (void)ws_size;
    (void)hipFuncSetAttribute((const void*)geglu_mlp, hipFuncAttributeMaxDynamicSharedMemorySize, LDS_BYTES);
    geglu_mlp<<<256, 512, LDS_BYTES, stream>>>(d_in[0], d_in[1], d_in[2], d_in[3], d_out, (char*)d_ws);
}
```

```cpp
#define PROBE_A 99
#define PROBE_B 0
#define CODEPF
#define CODEPF_BACK 256
#define CODEPF_N2 44
#define TLBPF
#include <hip/hip_runtime.h>
#include <hip/hip_bf16.h>

#define LAS __attribute__((address_space(3)))
#define GAS __attribute__((address_space(1)))
typedef __bf16 bf16x2 __attribute__((ext_vector_type(2)));
typedef float f32x2 __attribute__((ext_vector_type(2)));
typedef float f32x4 __attribute__((ext_vector_type(4)));
typedef unsigned u32x4 __attribute__((ext_vector_type(4)));
typedef unsigned long long u64;

#define OUTAUX 0
constexpr int M_ = 2048, D_ = 640, F_ = 1728;
constexpr unsigned XROW = D_ * 2, WROW = F_ * 2;
constexpr int P1_SLOT = 49152;
constexpr int WD_SLOTS = 12, WD_SLOT = 8192, H_BASE = 98304, H_SLOTS = 5, H_SLOT = 12288;
constexpr int MISC = H_BASE + H_SLOTS * H_SLOT;
constexpr int LDS_BYTES = 160 * 1024;
constexpr size_t WS_FLAGS = 8u << 20;
constexpr u64 LCG_A = 6364136223846793005ull, LCG_C = 1442695040888963407ull;

#define WAIT_V(n) asm volatile("s_waitcnt vmcnt(" #n ")" ::: "memory")
#define WAIT_VI(n) asm volatile("s_waitcnt vmcnt(%0)" :: "i"(n) : "memory")
#define WAIT_L(n) asm volatile("s_waitcnt lgkmcnt(" #n ")" ::: "memory")
#define BAR() asm volatile("s_barrier" ::: "memory")
#define DI __device__ __forceinline__

#define ST(i) do { if ((i) == PROBE_A) tpa = __builtin_amdgcn_s_memrealtime(); if ((i) == PROBE_B) tpb = __builtin_amdgcn_s_memrealtime(); } while (0)

#define FCLOB "v160","v161","v162","v163","v164","v165","v166","v167","v168","v169","v170","v171","v172","v173","v174","v175","v176","v177","v178","v179","v180","v181","v182","v183","v184","v185","v186","v187","v188","v189","v190","v191", \
  "v192","v193","v194","v195","v196","v197","v198","v199","v200","v201","v202","v203","v204","v205","v206","v207","v208","v209","v210","v211","v212","v213","v214","v215","v216","v217","v218","v219","v220","v221","v222","v223", \
  "v224","v225","v226","v227","v228","v229","v230","v231","v232","v233","v234","v235","v236","v237","v238","v239","v240","v241","v242","v243","v244","v245","v246","v247","v248","v249","v250","v251","v252","v253","v254","v255"
#define OWN_FRAGS() asm volatile("" ::: FCLOB)
constexpr int FR = 160;

DI unsigned rfl(unsigned v) { return __builtin_amdgcn_readfirstlane(v); }
DI u32x4 mksrd(const void* p, unsigned bytes) {
    const u64 a = (u64)p;
    return (u32x4){(unsigned)a, (unsigned)(a >> 32) & 0xffffu, bytes, 0x00020000u};
}
#define DMA_HEAD "s_mov_b32 m0, %0\n\ts_nop 0\n\t"
DI void dma4(unsigned dst, u32x4 r, unsigned soff, unsigned v0, unsigned v1, unsigned v2, unsigned v3) {
    asm volatile(DMA_HEAD "buffer_load_dwordx4 %3, %1, %2 offen lds\n\tbuffer_load_dwordx4 %4, %1, %2 offen offset:1024 lds\n\t"
                 "buffer_load_dwordx4 %5, %1, %2 offen offset:2048 lds\n\tbuffer_load_dwordx4 %6, %1, %2 offen offset:3072 lds"
                 :: "s"(dst), "s"(r), "s"(soff), "v"(v0), "v"(v1), "v"(v2), "v"(v3) : "memory", "m0");
}
DI void dma2(unsigned dst, u32x4 r, unsigned soff, unsigned v0, unsigned v1) {
    asm volatile(DMA_HEAD "buffer_load_dwordx4 %3, %1, %2 offen lds\n\tbuffer_load_dwordx4 %4, %1, %2 offen offset:1024 lds"
                 :: "s"(dst), "s"(r), "s"(soff), "v"(v0), "v"(v1) : "memory", "m0");
}
DI void dma22(unsigned dst, u32x4 ra, u32x4 rb, unsigned soff, unsigned v0, unsigned v1) {
    asm volatile(DMA_HEAD "buffer_load_dwordx4 %4, %1, %3 offen lds\n\tbuffer_load_dwordx4 %5, %1, %3 offen offset:1024 lds\n\t"
                 "buffer_load_dwordx4 %4, %2, %3 offen offset:2048 lds\n\tbuffer_load_dwordx4 %5, %2, %3 offen offset:3072 lds"
                 :: "s"(dst), "s"(ra), "s"(rb), "s"(soff), "v"(v0), "v"(v1) : "memory", "m0");
}
DI void dma4_sc1(unsigned dst, u32x4 r, unsigned soff, unsigned v0, unsigned v1, unsigned v2, unsigned v3) {
    asm volatile(DMA_HEAD "buffer_load_dwordx4 %3, %1, %2 offen sc1 lds\n\tbuffer_load_dwordx4 %4, %1, %2 offen offset:1024 sc1 lds\n\t"
                 "buffer_load_dwordx4 %5, %1, %2 offen offset:2048 sc1 lds\n\tbuffer_load_dwordx4 %6, %1, %2 offen offset:3072 sc1 lds"
                 :: "s"(dst), "s"(r), "s"(soff), "v"(v0), "v"(v1), "v"(v2), "v"(v3) : "memory", "m0");
}
DI void dma2_sc1(unsigned dst, u32x4 r, unsigned soff, unsigned v0, unsigned v1) {
    asm volatile(DMA_HEAD "buffer_load_dwordx4 %3, %1, %2 offen sc1 lds\n\tbuffer_load_dwordx4 %4, %1, %2 offen offset:1024 sc1 lds"
                 :: "s"(dst), "s"(r), "s"(soff), "v"(v0), "v"(v1) : "memory", "m0");
}
template <int R, int OFF> DI void rd128(unsigned addr) { asm volatile("ds_read_b128 v[%c1:%c2], %0 offset:%c3" :: "v"(addr), "i"(FR + R), "i"(FR + R + 3), "i"(OFF)); }
template <int R, int OFF> DI void rdtr(unsigned addr) { asm volatile("ds_read_b64_tr_b16 v[%c1:%c2], %0 offset:%c3" :: "v"(addr), "i"(FR + R), "i"(FR + R + 1), "i"(OFF)); }
template <int RA, int RB> DI void mfma(f32x4& acc) {
    asm volatile("v_mfma_f32_16x16x32_bf16 %0, v[%c1:%c2], v[%c3:%c4], %0" : "+v"(acc) : "i"(FR + RA), "i"(FR + RA + 3), "i"(FR + RB), "i"(FR + RB + 3));
}
DI unsigned pk(float a, float b) { bf16x2 v = {(__bf16)a, (__bf16)b}; return __builtin_bit_cast(unsigned, v); }
DI unsigned geglu2(f32x2 g, f32x2 u) {
    const f32x2 c1 = {-2.302208198f, -2.302208198f}, c2 = {-0.1029432397f, -0.1029432397f};
    const f32x2 a = g * __builtin_elementwise_fma(g * g, c2, c1);
    const f32x2 d = (f32x2){__builtin_amdgcn_exp2f(a.x), __builtin_amdgcn_exp2f(a.y)} + 1.0f;
    const f32x2 r = (g * u) * (f32x2){__builtin_amdgcn_rcpf(d.x), __builtin_amdgcn_rcpf(d.y)};
    return pk(r.x, r.y);
}

template <int S, int KS, int I> DI void p1_read(unsigned ax, unsigned aw0, unsigned aw1) {
    constexpr int B = 48 * S;
    if constexpr (I < 8) { constexpr int n = I >> 2, up = (I >> 1) & 1, t = I & 1;
        rdtr<B + 32 + 8 * up + 4 * n + 2 * t, 32768 + KS * 8192 + up * 2048 + t * 512>(n ? aw1 : aw0); }
    else rd128<B + 4 * (I - 8), (I - 8) * 2048>(ax);
}
template <int S, int I> DI void p1_mfma(f32x4 (&ag)[8][2], f32x4 (&au)[8][2]) {
    constexpr int B = 48 * S, m = I >> 2, n = (I >> 1) & 1, up = I & 1;
    if constexpr (up) mfma<B + 40 + 4 * n, B + 4 * m>(au[m][n]); else mfma<B + 32 + 4 * n, B + 4 * m>(ag[m][n]);
}
template <int SR, int KS, int I> struct P1Stage {
    static DI void run(f32x4 (&ag)[8][2], f32x4 (&au)[8][2], unsigned ax, unsigned aw0, unsigned aw1) {
        p1_mfma<SR ^ 1, I>(ag, au);
        if constexpr (I % 3 != 2 && (I / 3) * 2 + I % 3 < 16) p1_read<SR, KS, (I / 3) * 2 + I % 3>(ax, aw0, aw1);
        if constexpr (I + 1 < 32) P1Stage<SR, KS, I + 1>::run(ag, au, ax, aw0, aw1);
    }
};
template <int S, int KS, int I> DI void p1_reads_only(unsigned ax, unsigned aw0, unsigned aw1) {
    p1_read<S, KS, I>(ax, aw0, aw1);
    if constexpr (I + 1 < 16) p1_reads_only<S, KS, I + 1>(ax, aw0, aw1);
}

template <int S, int I> DI void p2_read(unsigned ah0, unsigned ah1, unsigned aw0, unsigned aw1) {
    constexpr int ks = I / 7, r = I % 7, B = 40 * S + 20 * ks;
    if constexpr (r < 4) { constexpr int n = r >> 1, t = r & 1; rdtr<B + 12 + 4 * n + 2 * t, ks * 4096 + t * 512>(n ? aw1 : aw0); }
    else rd128<B + 4 * (r - 4), (r - 4) * 2048>(ks ? ah1 : ah0);
}
template <int S, int I> DI void p2_mfma(f32x4 (&acc)[3][2]) {
    constexpr int ks = I / 6, m = (I % 6) >> 1, n = I & 1, B = 40 * S + 20 * ks;
    mfma<B + 12 + 4 * n, B + 4 * m>(acc[m][n]);
}
template <int SR, bool MM, int I> struct P2Stage {
    static DI void run(f32x4 (&acc)[3][2], unsigned ah0, unsigned ah1, unsigned aw0, unsigned aw1) {
        if constexpr (MM) p2_mfma<SR ^ 1, I>(acc);
        if constexpr (I < 7) { p2_read<SR, 2 * I>(ah0, ah1, aw0, aw1); p2_read<SR, 2 * I + 1>(ah0, ah1, aw0, aw1); }
        if constexpr (I + 1 < 12) P2Stage<SR, MM, I + 1>::run(acc, ah0, ah1, aw0, aw1);
    }
};
template <int S, int I> DI void p2_mfma_only(f32x4 (&acc)[3][2]) { p2_mfma<S, I>(acc); if constexpr (I + 1 < 12) p2_mfma_only<S, I + 1>(acc); }

template <int NH> DI void h_stream(u32x4 rh, unsigned so, unsigned lh0, int tr, unsigned v0, unsigned v1, unsigned v2, unsigned v3) {
    auto tile = [&](unsigned slot) {
        dma4_sc1(lh0 + slot, rh, so, v0, v1, v2, v3);
        if constexpr (NH == 6) dma2_sc1(lh0 + slot + 4096, rh, so + 32 * WROW, v0, v1);
        so += 128; if (++tr == 27) { tr = 0; so -= 27 * 128; }
    };
#pragma unroll 1
    for (int s = 0; s < H_SLOTS; ++s) tile(s * H_SLOT);
    WAIT_VI(4 * NH); BAR();
    WAIT_VI(3 * NH); BAR();
    unsigned hs = 0;
#pragma unroll 1
    for (int t = 2; t < 24; ++t) {
        WAIT_VI(2 * NH); BAR();
        tile(hs); hs = hs == (H_SLOTS - 1) * H_SLOT ? 0 : hs + H_SLOT;
    }
    WAIT_VI(2 * NH); BAR();
    WAIT_VI(NH); BAR();
    WAIT_V(0); BAR();
}

__global__ __launch_bounds__(512, 2) void geglu_mlp(const void* __restrict__ x, const void* __restrict__ wg, const void* __restrict__ wu,
                                                    const void* __restrict__ wd, void* __restrict__ out, char* __restrict__ ws) {
    u64 t_entry;
    asm volatile("s_memrealtime %0\n\ts_waitcnt lgkmcnt(0)" : "=s"(t_entry));
    extern __shared__ __attribute__((aligned(16))) char smem_[];
    LAS char* smem = (LAS char*)smem_;
    const unsigned lds0 = (unsigned)(size_t)smem;
    const int lane = threadIdx.x & 63, w = rfl(threadIdx.x >> 6);
    const int xcd = blockIdx.x & 7, j = blockIdx.x >> 3;
    const int fs = (xcd & 3) * 7 + (j >> 2), pm = (xcd >> 2) * 4 + (j & 3);
    const bool prod = j < 28 && fs < 27, cons = j < 30;
    const int mt = j / 10, nt = j - mt * 10;
    const int row2 = xcd * 256 + mt * 96, ntok = mt == 2 ? 64 : 96;
    const int rot = (nt * 27) / 10;
    GAS u64* flags = (GAS u64*)(ws + WS_FLAGS);
    GAS u64* epoch = flags + 256;
    void* h = ws;
    const int spare = j >= 28 ? xcd * 4 + (j - 28) : 32 + (xcd >> 2) * 4 + (j & 3);
    const int myslot = prod ? pm * 32 + fs : (spare / 5) * 32 + 27 + spare % 5;

    u64 tpa = 0, tpb = 0;
    if (PROBE_A == 99) tpa = t_entry;
    if (w < 4) {
        if (w == 0) ST(0);
        const u64 e0 = __hip_atomic_load(epoch, __ATOMIC_RELAXED, __HIP_MEMORY_SCOPE_AGENT);
        const int wm = w >> 1, wn = w & 1, q4 = lane >> 4;
        const int tq = (lane & 15) >> 2, tp = lane & 3;
        const unsigned tsw = (((tq >> 1) & 1) | ((q4 & 1) << 1)) << 1;
        const unsigned awc = (((wn * 4 + (tp >> 1)) ^ tsw) * 16) + 8 * (tp & 1);
        const unsigned xsw = (q4 ^ (lane & 6)) * 16;
        if (prod) {
            f32x4 ag[8][2], au[8][2];
#pragma unroll
            for (int m = 0; m < 8; ++m)
#pragma unroll
                for (int n = 0; n < 2; ++n) { ag[m][n] = (f32x4){0.f, 0.f, 0.f, 0.f}; au[m][n] = (f32x4){0.f, 0.f, 0.f, 0.f}; }
            if (threadIdx.x == 0) *(LAS unsigned*)(smem + MISC) = 0u;
            const unsigned axb = lds0 + (wm * 128 + (lane & 15)) * 128 + xsw;
            const unsigned awb = lds0 + (q4 >> 1) * 4096 + (8 * (q4 & 1) + tq) * 128 + awc;
            WAIT_L(0);
            unsigned sl = 2 * P1_SLOT;
            OWN_FRAGS();
            BAR();
            if (w == 0) ST(1);
            p1_reads_only<0, 0, 0>(axb + sl, awb + sl, (awb ^ 32) + sl);
            WAIT_L(0);
#pragma unroll 1
            for (int t = 0; t < 10; ++t) {
                OWN_FRAGS();
                P1Stage<1, 1, 0>::run(ag, au, (axb ^ 64) + sl, awb + sl, (awb ^ 32) + sl);
                WAIT_L(0);
                BAR();
                sl = sl == 2 * P1_SLOT ? 0 : sl + P1_SLOT;
                P1Stage<0, 0, 0>::run(ag, au, axb + sl, awb + sl, (awb ^ 32) + sl);
                WAIT_L(0);
            }
            asm volatile("s_nop 7\n\ts_nop 7" ::: "memory");
            __builtin_amdgcn_sched_barrier(0);
            if (w == 0) ST(2);
            const __amdgpu_buffer_rsrc_t rh = __builtin_amdgcn_make_buffer_rsrc(h, 0, 8u << 20, 0x00020000);
            const unsigned ho = (pm >> 1) * (2u << 20) + ((pm & 1) * 256 + wm * 128 + (lane & 15)) * WROW + (fs * 64 + wn * 32 + (q4 & 1) * 16 + (q4 >> 1) * 8) * 2;
#pragma unroll
            for (int m = 0; m < 8; ++m) {
                unsigned d[2][2];
#pragma unroll
                for (int n = 0; n < 2; ++n) {
                    d[n][0] = geglu2(ag[m][n].xy, au[m][n].xy);
                    d[n][1] = geglu2(ag[m][n].zw, au[m][n].zw);
                }
                const auto s0 = __builtin_amdgcn_permlane16_swap(d[0][0], d[1][0], false, false);
                const auto s1 = __builtin_amdgcn_permlane16_swap(d[0][1], d[1][1], false, false);
                const u32x4 v = {s0[0], s1[0], s0[1], s1[1]};
                __builtin_amdgcn_raw_buffer_store_b128(v, rh, ho + m * 16 * WROW, 0, 16);
            }
            if (w == 0) ST(3);
            WAIT_V(0);
            if (w == 0) ST(4);
            unsigned old = 0;
            if (lane == 0) old = __hip_atomic_fetch_add((LAS unsigned*)(smem + MISC), 1u, __ATOMIC_RELAXED, __HIP_MEMORY_SCOPE_WORKGROUP);
            if (rfl(old) == 3 && lane == 0) __hip_atomic_store(flags + myslot, e0 * LCG_A + LCG_C, __ATOMIC_RELAXED, __HIP_MEMORY_SCOPE_AGENT);
        } else if (w == 0 && lane == 0) {
            __hip_atomic_store(flags + myslot, e0 * LCG_A + LCG_C, __ATOMIC_RELAXED, __HIP_MEMORY_SCOPE_AGENT);
        }
        if (cons) {
            f32x4 acc[3][2];
#pragma unroll
            for (int m = 0; m < 3; ++m) { acc[m][0] = (f32x4){0.f, 0.f, 0.f, 0.f}; acc[m][1] = (f32x4){0.f, 0.f, 0.f, 0.f}; }
            const int tw = ntok >> 1;
            const unsigned ahb = lds0 + H_BASE + (wm * tw + (lane & 15)) * 128 + xsw;
            const unsigned awd = lds0 + (8 * q4 + tq) * 128 + awc;
            unsigned hs = 0, wsl = 0;
            OWN_FRAGS();
            BAR();
            if (w == 0) ST(8);
            P2Stage<0, false, 0>::run(acc, ahb, ahb ^ 64, awd, awd ^ 32);
#pragma unroll 1
            for (int t = 1; t < 27; t += 2) {
                OWN_FRAGS();
                hs = hs == (H_SLOTS - 1) * H_SLOT ? 0 : hs + H_SLOT; wsl = wsl == (WD_SLOTS - 1) * WD_SLOT ? 0 : wsl + WD_SLOT;
                { const unsigned a0 = ahb + hs, a1 = (ahb ^ 64) + hs, b0 = awd + wsl, b1 = (awd ^ 32) + wsl;
                  BAR(); WAIT_L(0);
                  P2Stage<1, true, 0>::run(acc, a0, a1, b0, b1); }
                hs = hs == (H_SLOTS - 1) * H_SLOT ? 0 : hs + H_SLOT; wsl = wsl == (WD_SLOTS - 1) * WD_SLOT ? 0 : wsl + WD_SLOT;
                { const unsigned a0 = ahb + hs, a1 = (ahb ^ 64) + hs, b0 = awd + wsl, b1 = (awd ^ 32) + wsl;
                  BAR(); WAIT_L(0);
                  P2Stage<0, true, 0>::run(acc, a0, a1, b0, b1); }
            }
            WAIT_L(0);
            p2_mfma_only<0, 0>(acc);
            asm volatile("s_nop 7\n\ts_nop 7" ::: "memory");
            __builtin_amdgcn_sched_barrier(0);
            if (w == 0) ST(9);
            const __amdgpu_buffer_rsrc_t ro = __builtin_amdgcn_make_buffer_rsrc(out, 0, M_ * D_ * 2, 0x00020000);
            const unsigned oo = (row2 + wm * tw + (lane & 15)) * XROW + (nt * 64 + wn * 32 + (q4 & 1) * 16 + (q4 >> 1) * 8) * 2;
#pragma unroll
            for (int m = 0; m < 3; ++m) {
                const auto s0 = __builtin_amdgcn_permlane16_swap(pk(acc[m][0][0], acc[m][0][1]), pk(acc[m][1][0], acc[m][1][1]), false, false);
                const auto s1 = __builtin_amdgcn_permlane16_swap(pk(acc[m][0][2], acc[m][0][3]), pk(acc[m][1][2], acc[m][1][3]), false, false);
                const u32x4 v = {s0[0], s1[0], s0[1], s1[1]};
                if (m * 16 < tw) __builtin_amdgcn_raw_buffer_store_b128(v, ro, oo + m * 16 * XROW, 0, OUTAUX);
            }
            if (w == 0) ST(10);
            if (w == 0 && blockIdx.x == 9) { const u64 tend = __builtin_amdgcn_s_memrealtime() + 4 * (tpb - tpa); while (__builtin_amdgcn_s_memrealtime() < tend) __builtin_amdgcn_s_sleep(1); }
        }
    } else {
        const int lw = w - 4;
        unsigned cpf0 = 0, cpf1 = 0;
        if (lw == 3) {
            u64 pc; asm volatile("s_getpc_b64 %0" : "=s"(pc));
            const u64 base = pc & ~127ull;
            const char* a = (const char*)base - CODEPF_BACK + lane * 128;
            asm volatile("global_load_dword %0, %1, off" : "=&v"(cpf0) : "v"(a) : "memory");
            const char* b = a + 8192;
            if (lane < CODEPF_N2) asm volatile("global_load_dword %0, %1, off" : "=&v"(cpf1) : "v"(b) : "memory");
        }
        unsigned tpf = 0;
        if (lw == 2) {
            const char* p = lane == 0 ? (const char*)wd : lane == 1 ? (const char*)wd + (F_ * D_ * 2 - 4) : lane == 2 ? (const char*)out : lane == 3 ? (const char*)out + (M_ * D_ * 2 - 4)
                          : (const char*)ws + (size_t)(lane - 4) * (2u << 20) + (2u << 20) - 128;
            if (lane < 8) asm volatile("global_load_dword %0, %1, off" : "=&v"(tpf) : "v"(p) : "memory");
        }
        u64 e0;
        asm volatile("global_load_dwordx2 %0, %1, off sc1" : "=&v"(e0) : "v"(epoch) : "memory");
        const unsigned r8 = lane >> 3, c8 = lane & 7;
        const unsigned swx = (c8 ^ (r8 & 6)) * 16;
        const unsigned sww0 = (c8 ^ (((r8 >> 1) & 1) << 1)) * 16;
        const unsigned sww1 = (c8 ^ ((((r8 >> 1) & 1) | 2) << 1)) * 16;
        const u32x4 rwd = mksrd(wd, F_ * D_ * 2);
        constexpr unsigned DX = 8 * XROW - 1024, DW = 8 * WROW - 1024;
        const unsigned vd0 = r8 * XROW + sww0, vd1 = r8 * XROW + sww1 + DX, vd2 = vd0 + 2 * DX, vd3 = vd1 + 2 * DX;
        auto wd_batch = [&](int b) {
            int tr = 6 * b + rot; if (tr >= 27) tr -= 27;
            unsigned so = (tr * 64 + lw * 16) * XROW + nt * 128, ld = lds0 + 6 * b * WD_SLOT + lw * 2048;
#pragma unroll 1
            for (int t = 0; t < 6; ++t) {
                dma2(ld, rwd, so, vd0, vd1);
                ld += WD_SLOT; so += 64 * XROW; if (++tr == 27) { tr = 0; so -= 27 * 64 * XROW; }
            }
        };
        if (prod) {
            const u32x4 rx = mksrd(x, M_ * D_ * 2), rg = mksrd(wg, D_ * F_ * 2), ru = mksrd((const char*)wu - 2048, D_ * F_ * 2 + 2048);
            const unsigned vx0 = r8 * XROW + swx, vx1 = vx0 + DX, vx2 = vx0 + 2 * DX, vx3 = vx0 + 3 * DX;
            const unsigned vg0 = r8 * WROW + sww0, vg1 = r8 * WROW + sww1 + DW;
#define ROT1 1
            int kt = ROT1 ? ((j >> 2) + 3 * (j & 3)) % 10 : 0;
            unsigned sx = (pm * 256 + lw * 64) * XROW + kt * 128, sw = (kt * 64 + lw * 16) * WROW + fs * 128;
            auto stage = [&](unsigned slot) {
                const unsigned b = lds0 + slot;
                dma22(b + 32768 + lw * 4096, rg, ru, sw, vg0, vg1);
                dma4(b + lw * 8192, rx, sx, vx0, vx1, vx2, vx3);
                dma4(b + lw * 8192 + 4096, rx, sx + 32 * XROW, vx0, vx1, vx2, vx3);
                sx += 128; sw += 64 * WROW; if (++kt == 10) { kt = 0; sx -= 1280; sw -= 640 * WROW; }
            };
            stage(2 * P1_SLOT); stage(0); stage(P1_SLOT);
            WAIT_V(24);
            asm volatile("" :: "v"(cpf0), "v"(cpf1));
            asm volatile("" :: "v"(tpf));
            BAR();
            WAIT_V(12); BAR();
            unsigned sl = 2 * P1_SLOT;
#pragma unroll 1
            for (int t = 1; t < 8; ++t) {
                stage(sl);
                sl = sl == 2 * P1_SLOT ? 0 : sl + P1_SLOT;
                WAIT_V(12); BAR();
            }
            wd_batch(0);
            WAIT_V(12); BAR();
            wd_batch(1);
            BAR();
        } else {
            if (cons) { wd_batch(0); wd_batch(1); }
            WAIT_V(0);
            asm volatile("" :: "v"(cpf0), "v"(cpf1));
            asm volatile("" :: "v"(tpf));
        }
        if (cons) {
            if (lw < 2) {
                unsigned dummy = 0;
#pragma unroll 1
                for (int t = WD_SLOTS + mt * 2 + lw; t < 27; t += 6) {
                    const int tr = t + rot >= 27 ? t + rot - 27 : t + rot;
                    asm volatile("buffer_load_dword %0, %1, %2, %3 offen" : "+v"(dummy) : "v"(lane * XROW), "s"(rwd), "s"((unsigned)(tr * 64 * XROW + nt * 128)) : "memory");
                }
                WAIT_V(0);
                asm volatile("" :: "v"(dummy));
                if (lw == 0) ST(11);
                int tr = WD_SLOTS + rot; if (tr >= 27) tr -= 27;
                unsigned so = (tr * 64 + lw * 32) * XROW + nt * 128, ld = 0;
                BAR(); BAR();
#pragma unroll 1
                for (int t = 2; t < 12; ++t) {
                    BAR();
                    dma4(lds0 + ld + lw * 4096, rwd, so, vd0, vd1, vd2, vd3);
                    ld += WD_SLOT; so += 64 * XROW; if (++tr == 27) { tr = 0; so -= 27 * 64 * XROW; }
                }
#pragma unroll 1
                for (int t = 12; t < 17; ++t) {
                    WAIT_V(36); BAR();
                    dma4(lds0 + ld + lw * 4096, rwd, so, vd0, vd1, vd2, vd3);
                    ld = ld == (WD_SLOTS - 1) * WD_SLOT ? 0 : ld + WD_SLOT; so += 64 * XROW; if (++tr == 27) { tr = 0; so -= 27 * 64 * XROW; }
                }
                WAIT_V(36); BAR();
                WAIT_V(32); BAR(); WAIT_V(28); BAR(); WAIT_V(24); BAR(); WAIT_V(20); BAR(); WAIT_V(16); BAR();
                WAIT_V(12); BAR(); WAIT_V(8); BAR(); WAIT_V(4); BAR(); WAIT_V(0); BAR();
            } else {
                WAIT_V(0);
                asm volatile("" : "+v"(e0));
                const u64 tag = e0 * LCG_A + LCG_C;
                GAS u64* pf = flags + xcd * 32 + (lane & 31);
                const u64 t0 = __builtin_amdgcn_s_memrealtime();
                for (;;) {
                    const u64 v = __hip_atomic_load(pf, __ATOMIC_RELAXED, __HIP_MEMORY_SCOPE_AGENT);
                    if (__all(v == tag)) break;
                    if (__builtin_amdgcn_s_memrealtime() - t0 > 5000000ull) break;
                    __builtin_amdgcn_s_sleep(1);
                }
                if (lw == 2) ST(5);
                const u32x4 rh = mksrd(h, 8u << 20);
                const unsigned v0 = r8 * WROW + swx, v1 = v0 + DW, v2 = v0 + 2 * DW, v3 = v0 + 3 * DW;
                const int hl = lw - 2;
                if (ntok == 96) h_stream<6>(rh, (xcd >> 1) * (2u << 20) + ((xcd & 1) * 256 + mt * 96 + hl * 48) * WROW + rot * 128, lds0 + H_BASE + hl * 6144, rot, v0, v1, v2, v3);
                else h_stream<4>(rh, (xcd >> 1) * (2u << 20) + ((xcd & 1) * 256 + mt * 96 + hl * 32) * WROW + rot * 128, lds0 + H_BASE + hl * 4096, rot, v0, v1, v2, v3);
            }
        }
        if (blockIdx.x == 0 && lw == 0) {
            WAIT_V(0);
            asm volatile("" : "+v"(e0));
            const u64 tag = e0 * LCG_A + LCG_C;
            const u64 t0 = __builtin_amdgcn_s_memrealtime();
            for (;;) {
                bool ok = true;
#pragma unroll
                for (int i = 0; i < 4; ++i) ok &= __hip_atomic_load(flags + i * 64 + lane, __ATOMIC_RELAXED, __HIP_MEMORY_SCOPE_AGENT) == tag;
                if (__all(ok)) break;
                if (__builtin_amdgcn_s_memrealtime() - t0 > 5000000ull) break;
                __builtin_amdgcn_s_sleep(1);
            }
            if (lane == 0) __hip_atomic_store(epoch, tag, __ATOMIC_RELAXED, __HIP_MEMORY_SCOPE_AGENT);
        }
    }
}

extern "C" void kernel_launch(void* const* d_in, const int* in_sizes, int n_in, void* d_out, int out_size, void* d_ws, size_t ws_size, hipStream_t stream) {
    (void)in_sizes; (void)n_in; (void)out_size; (void)ws_size;
    (void)hipFuncSetAttribute((const void*)geglu_mlp, hipFuncAttributeMaxDynamicSharedMemorySize, LDS_BYTES);
    geglu_mlp<<<256, 512, LDS_BYTES, stream>>>(d_in[0], d_in[1], d_in[2], d_in[3], d_out, (char*)d_ws);
}
```

```cpp
#define NT 512
#define BIGV
#define LDSB 163840
#include <hip/hip_runtime.h>
typedef unsigned long long u64;
typedef unsigned short u16;
__device__ inline float bf(u16 b) { return __uint_as_float((unsigned)b << 16); }
__device__ inline u16 tobf(float f) { unsigned u = __float_as_uint(f); u += 0x7FFF + ((u >> 16) & 1); return (u16)(u >> 16); }
__global__ __launch_bounds__(NT) void naive(const u16* x, const u16* wg, const u16* wu, const u16* wd, u16* out) {
    u64 t_entry;
    asm volatile("s_memrealtime %0\n\ts_waitcnt lgkmcnt(0)" : "=s"(t_entry));
    extern __shared__ float sm[];
    asm volatile("v_mov_b32 v255, 0" ::: "v255");
    float* xs = sm; u16* hs = (u16*)(sm + 8 * 640);
    const int r0 = blockIdx.x * 8;
    for (int i = threadIdx.x; i < 8 * 640; i += NT) xs[i] = bf(x[r0 * 640 + i]);
    __syncthreads();
    for (int f = threadIdx.x; f < 1728; f += NT) {
        float g[8] = {}, u[8] = {};
        for (int k = 0; k < 640; ++k) { const float a = bf(wg[k * 1728 + f]), b = bf(wu[k * 1728 + f]);
#pragma unroll
            for (int r = 0; r < 8; ++r) { g[r] += xs[r * 640 + k] * a; u[r] += xs[r * 640 + k] * b; } }
#pragma unroll
        for (int r = 0; r < 8; ++r) { const float z = 0.7978845608f * (g[r] + 0.044715f * g[r] * g[r] * g[r]); hs[r * 1728 + f] = tobf(0.5f * g[r] * (1.f + tanhf(z)) * u[r]); }
    }
    __syncthreads();
    for (int n = threadIdx.x; n < 640; n += NT) {
        float o[8] = {};
        for (int k = 0; k < 1728; ++k) { const float a = bf(wd[k * 640 + n]);
#pragma unroll
            for (int r = 0; r < 8; ++r) o[r] += bf(hs[r * 1728 + k]) * a; }
#pragma unroll
        for (int r = 0; r < 8; ++r) out[(r0 + r) * 640 + n] = tobf(o[r]);
    }
    if (blockIdx.x == 9 && threadIdx.x < 64) { const u64 tend = t_entry + 100000; while (__builtin_amdgcn_s_memrealtime() < tend) __builtin_amdgcn_s_sleep(1); }
}
extern "C" void kernel_launch(void* const* d_in, const int* in_sizes, int n_in, void* d_out, int out_size, void* d_ws, size_t ws_size, hipStream_t stream) {
    (void)hipFuncSetAttribute((const void*)naive, hipFuncAttributeMaxDynamicSharedMemorySize, 160 * 1024);
    naive<<<256, NT, LDSB, stream>>>((const u16*)d_in[0], (const u16*)d_in[1], (const u16*)d_in[2], (const u16*)d_in[3], (u16*)d_out);
}
```

```cpp
#define LDSB 163840
#include <hip/hip_runtime.h>
#define NT 256
typedef unsigned long long u64;
typedef unsigned short u16;
__device__ inline float bf(u16 b) { return __uint_as_float((unsigned)b << 16); }
__device__ inline u16 tobf(float f) { unsigned u = __float_as_uint(f); u += 0x7FFF + ((u >> 16) & 1); return (u16)(u >> 16); }
__global__ __launch_bounds__(NT) void naive(const u16* x, const u16* wg, const u16* wu, const u16* wd, u16* out) {
    u64 t_entry;
    asm volatile("s_memrealtime %0\n\ts_waitcnt lgkmcnt(0)" : "=s"(t_entry));
    extern __shared__ float sm[];
    float* xs = sm; u16* hs = (u16*)(sm + 8 * 640);
    const int r0 = blockIdx.x * 8;
    for (int i = threadIdx.x; i < 8 * 640; i += NT) xs[i] = bf(x[r0 * 640 + i]);
    __syncthreads();
    for (int f = threadIdx.x; f < 1728; f += NT) {
        float g[8] = {}, u[8] = {};
        for (int k = 0; k < 640; ++k) { const float a = bf(wg[k * 1728 + f]), b = bf(wu[k * 1728 + f]);
#pragma unroll
            for (int r = 0; r < 8; ++r) { g[r] += xs[r * 640 + k] * a; u[r] += xs[r * 640 + k] * b; } }
#pragma unroll
        for (int r = 0; r < 8; ++r) { const float z = 0.7978845608f * (g[r] + 0.044715f * g[r] * g[r] * g[r]); hs[r * 1728 + f] = tobf(0.5f * g[r] * (1.f + tanhf(z)) * u[r]); }
    }
    __syncthreads();
    for (int n = threadIdx.x; n < 640; n += NT) {
        float o[8] = {};
        for (int k = 0; k < 1728; ++k) { const float a = bf(wd[k * 640 + n]);
#pragma unroll
            for (int r = 0; r < 8; ++r) o[r] += bf(hs[r * 1728 + k]) * a; }
#pragma unroll
        for (int r = 0; r < 8; ++r) out[(r0 + r) * 640 + n] = tobf(o[r]);
    }
    if (blockIdx.x == 9 && threadIdx.x < 64) { const u64 tend = t_entry + 150000; while (__builtin_amdgcn_s_memrealtime() < tend) __builtin_amdgcn_s_sleep(1); }
}
extern "C" void kernel_launch(void* const* d_in, const int* in_sizes, int n_in, void* d_out, int out_size, void* d_ws, size_t ws_size, hipStream_t stream) {
    (void)hipFuncSetAttribute((const void*)naive, hipFuncAttributeMaxDynamicSharedMemorySize, 160 * 1024);
    naive<<<256, NT, LDSB, stream>>>((const u16*)d_in[0], (const u16*)d_in[1], (const u16*)d_in[2], (const u16*)d_in[3], (u16*)d_out);
}
```

```cpp
#define PROBE_A 9
#define PROBE_B 10
#define CODEPF
#define CODEPF_BACK 256
#define CODEPF_N2 44
#define TLBPF
#include <hip/hip_runtime.h>
#include <hip/hip_bf16.h>

#define LAS __attribute__((address_space(3)))
#define GAS __attribute__((address_space(1)))
typedef __bf16 bf16x2 __attribute__((ext_vector_type(2)));
typedef float f32x2 __attribute__((ext_vector_type(2)));
typedef float f32x4 __attribute__((ext_vector_type(4)));
typedef unsigned u32x4 __attribute__((ext_vector_type(4)));
typedef unsigned long long u64;

#define OUTAUX 0
constexpr int M_ = 2048, D_ = 640, F_ = 1728;
constexpr unsigned XROW = D_ * 2, WROW = F_ * 2;
constexpr int P1_SLOT = 49152;
constexpr int WD_SLOTS = 12, WD_SLOT = 8192, H_BASE = 98304, H_SLOTS = 5, H_SLOT = 12288;
constexpr int MISC = H_BASE + H_SLOTS * H_SLOT;
constexpr int LDS_BYTES = 160 * 1024;
constexpr size_t WS_FLAGS = 8u << 20;
constexpr u64 LCG_A = 6364136223846793005ull, LCG_C = 1442695040888963407ull;

#define WAIT_V(n) asm volatile("s_waitcnt vmcnt(" #n ")" ::: "memory")
#define WAIT_VI(n) asm volatile("s_waitcnt vmcnt(%0)" :: "i"(n) : "memory")
#define WAIT_L(n) asm volatile("s_waitcnt lgkmcnt(" #n ")" ::: "memory")
#define BAR() asm volatile("s_barrier" ::: "memory")
#define DI __device__ __forceinline__

#define ST(i) do { if ((i) == PROBE_A) tpa = __builtin_amdgcn_s_memrealtime(); if ((i) == PROBE_B) tpb = __builtin_amdgcn_s_memrealtime(); } while (0)

#define FCLOB "v160","v161","v162","v163","v164","v165","v166","v167","v168","v169","v170","v171","v172","v173","v174","v175","v176","v177","v178","v179","v180","v181","v182","v183","v184","v185","v186","v187","v188","v189","v190","v191", \
  "v192","v193","v194","v195","v196","v197","v198","v199","v200","v201","v202","v203","v204","v205","v206","v207","v208","v209","v210","v211","v212","v213","v214","v215","v216","v217","v218","v219","v220","v221","v222","v223", \
  "v224","v225","v226","v227","v228","v229","v230","v231","v232","v233","v234","v235","v236","v237","v238","v239","v240","v241","v242","v243","v244","v245","v246","v247","v248","v249","v250","v251","v252","v253","v254","v255"
#define OWN_FRAGS() asm volatile("" ::: FCLOB)
constexpr int FR = 160;

DI unsigned rfl(unsigned v) { return __builtin_amdgcn_readfirstlane(v); }
DI u32x4 mksrd(const void* p, unsigned bytes) {
    const u64 a = (u64)p;
    return (u32x4){(unsigned)a, (unsigned)(a >> 32) & 0xffffu, bytes, 0x00020000u};
}
#define DMA_HEAD "s_mov_b32 m0, %0\n\ts_nop 0\n\t"
DI void dma4(unsigned dst, u32x4 r, unsigned soff, unsigned v0, unsigned v1, unsigned v2, unsigned v3) {
    asm volatile(DMA_HEAD "buffer_load_dwordx4 %3, %1, %2 offen lds\n\tbuffer_load_dwordx4 %4, %1, %2 offen offset:1024 lds\n\t"
                 "buffer_load_dwordx4 %5, %1, %2 offen offset:2048 lds\n\tbuffer_load_dwordx4 %6, %1, %2 offen offset:3072 lds"
                 :: "s"(dst), "s"(r), "s"(soff), "v"(v0), "v"(v1), "v"(v2), "v"(v3) : "memory", "m0");
}
DI void dma2(unsigned dst, u32x4 r, unsigned soff, unsigned v0, unsigned v1) {
    asm volatile(DMA_HEAD "buffer_load_dwordx4 %3, %1, %2 offen lds\n\tbuffer_load_dwordx4 %4, %1, %2 offen offset:1024 lds"
                 :: "s"(dst), "s"(r), "s"(soff), "v"(v0), "v"(v1) : "memory", "m0");
}
DI void dma22(unsigned dst, u32x4 ra, u32x4 rb, unsigned soff, unsigned v0, unsigned v1) {
    asm volatile(DMA_HEAD "buffer_load_dwordx4 %4, %1, %3 offen lds\n\tbuffer_load_dwordx4 %5, %1, %3 offen offset:1024 lds\n\t"
                 "buffer_load_dwordx4 %4, %2, %3 offen offset:2048 lds\n\tbuffer_load_dwordx4 %5, %2, %3 offen offset:3072 lds"
                 :: "s"(dst), "s"(ra), "s"(rb), "s"(soff), "v"(v0), "v"(v1) : "memory", "m0");
}
DI void dma4_sc1(unsigned dst, u32x4 r, unsigned soff, unsigned v0, unsigned v1, unsigned v2, unsigned v3) {
    asm volatile(DMA_HEAD "buffer_load_dwordx4 %3, %1, %2 offen sc1 lds\n\tbuffer_load_dwordx4 %4, %1, %2 offen offset:1024 sc1 lds\n\t"
                 "buffer_load_dwordx4 %5, %1, %2 offen offset:2048 sc1 lds\n\tbuffer_load_dwordx4 %6, %1, %2 offen offset:3072 sc1 lds"
                 :: "s"(dst), "s"(r), "s"(soff), "v"(v0), "v"(v1), "v"(v2), "v"(v3) : "memory", "m0");
}
DI void dma2_sc1(unsigned dst, u32x4 r, unsigned soff, unsigned v0, unsigned v1) {
    asm volatile(DMA_HEAD "buffer_load_dwordx4 %3, %1, %2 offen sc1 lds\n\tbuffer_load_dwordx4 %4, %1, %2 offen offset:1024 sc1 lds"
                 :: "s"(dst), "s"(r), "s"(soff), "v"(v0), "v"(v1) : "memory", "m0");
}
template <int R, int OFF> DI void rd128(unsigned addr) { asm volatile("ds_read_b128 v[%c1:%c2], %0 offset:%c3" :: "v"(addr), "i"(FR + R), "i"(FR + R + 3), "i"(OFF)); }
template <int R, int OFF> DI void rdtr(unsigned addr) { asm volatile("ds_read_b64_tr_b16 v[%c1:%c2], %0 offset:%c3" :: "v"(addr), "i"(FR + R), "i"(FR + R + 1), "i"(OFF)); }
template <int RA, int RB> DI void mfma(f32x4& acc) {
    asm volatile("v_mfma_f32_16x16x32_bf16 %0, v[%c1:%c2], v[%c3:%c4], %0" : "+v"(acc) : "i"(FR + RA), "i"(FR + RA + 3), "i"(FR + RB), "i"(FR + RB + 3));
}
DI unsigned pk(float a, float b) { bf16x2 v = {(__bf16)a, (__bf16)b}; return __builtin_bit_cast(unsigned, v); }
DI unsigned geglu2(f32x2 g, f32x2 u) {
    const f32x2 c1 = {-2.302208198f, -2.302208198f}, c2 = {-0.1029432397f, -0.1029432397f};
    const f32x2 a = g * __builtin_elementwise_fma(g * g, c2, c1);
    const f32x2 d = (f32x2){__builtin_amdgcn_exp2f(a.x), __builtin_amdgcn_exp2f(a.y)} + 1.0f;
    const f32x2 r = (g * u) * (f32x2){__builtin_amdgcn_rcpf(d.x), __builtin_amdgcn_rcpf(d.y)};
    return pk(r.x, r.y);
}

template <int S, int KS, int I> DI void p1_read(unsigned ax, unsigned aw0, unsigned aw1) {
    constexpr int B = 48 * S;
    if constexpr (I < 8) { constexpr int n = I >> 2, up = (I >> 1) & 1, t = I & 1;
        rdtr<B + 32 + 8 * up + 4 * n + 2 * t, 32768 + KS * 8192 + up * 2048 + t * 512>(n ? aw1 : aw0); }
    else rd128<B + 4 * (I - 8), (I - 8) * 2048>(ax);
}
template <int S, int I> DI void p1_mfma(f32x4 (&ag)[8][2], f32x4 (&au)[8][2]) {
    constexpr int B = 48 * S, m = I >> 2, n = (I >> 1) & 1, up = I & 1;
    if constexpr (up) mfma<B + 40 + 4 * n, B + 4 * m>(au[m][n]); else mfma<B + 32 + 4 * n, B + 4 * m>(ag[m][n]);
}
template <int SR, int KS, int I> struct P1Stage {
    static DI void run(f32x4 (&ag)[8][2], f32x4 (&au)[8][2], unsigned ax, unsigned aw0, unsigned aw1) {
        p1_mfma<SR ^ 1, I>(ag, au);
        if constexpr (I % 3 != 2 && (I / 3) * 2 + I % 3 < 16) p1_read<SR, KS, (I / 3) * 2 + I % 3>(ax, aw0, aw1);
        if constexpr (I + 1 < 32) P1Stage<SR, KS, I + 1>::run(ag, au, ax, aw0, aw1);
    }
};
template <int S, int KS, int I> DI void p1_reads_only(unsigned ax, unsigned aw0, unsigned aw1) {
    p1_read<S, KS, I>(ax, aw0, aw1);
    if constexpr (I + 1 < 16) p1_reads_only<S, KS, I + 1>(ax, aw0, aw1);
}

template <int S, int I> DI void p2_read(unsigned ah0, unsigned ah1, unsigned aw0, unsigned aw1) {
    constexpr int ks = I / 7, r = I % 7, B = 40 * S + 20 * ks;
    if constexpr (r < 4) { constexpr int n = r >> 1, t = r & 1; rdtr<B + 12 + 4 * n + 2 * t, ks * 4096 + t * 512>(n ? aw1 : aw0); }
    else rd128<B + 4 * (r - 4), (r - 4) * 2048>(ks ? ah1 : ah0);
}
template <int S, int I> DI void p2_mfma(f32x4 (&acc)[3][2]) {
    constexpr int ks = I / 6, m = (I % 6) >> 1, n = I & 1, B = 40 * S + 20 * ks;
    mfma<B + 12 + 4 * n, B + 4 * m>(acc[m][n]);
}
template <int SR, bool MM, int I> struct P2Stage {
    static DI void run(f32x4 (&acc)[3][2], unsigned ah0, unsigned ah1, unsigned aw0, unsigned aw1) {
        if constexpr (MM) p2_mfma<SR ^ 1, I>(acc);
        if constexpr (I < 7) { p2_read<SR, 2 * I>(ah0, ah1, aw0, aw1); p2_read<SR, 2 * I + 1>(ah0, ah1, aw0, aw1); }
        if constexpr (I + 1 < 12) P2Stage<SR, MM, I + 1>::run(acc, ah0, ah1, aw0, aw1);
    }
};
template <int S, int I> DI void p2_mfma_only(f32x4 (&acc)[3][2]) { p2_mfma<S, I>(acc); if constexpr (I + 1 < 12) p2_mfma_only<S, I + 1>(acc); }

template <int NH> DI void h_stream(u32x4 rh, unsigned so, unsigned lh0, int tr, unsigned v0, unsigned v1, unsigned v2, unsigned v3) {
    auto tile = [&](unsigned slot) {
        dma4_sc1(lh0 + slot, rh, so, v0, v1, v2, v3);
        if constexpr (NH == 6) dma2_sc1(lh0 + slot + 4096, rh, so + 32 * WROW, v0, v1);
        so += 128; if (++tr == 27) { tr = 0; so -= 27 * 128; }
    };
#pragma unroll 1
    for (int s = 0; s < H_SLOTS; ++s) tile(s * H_SLOT);
    WAIT_VI(4 * NH); BAR();
    WAIT_VI(3 * NH); BAR();
    unsigned hs = 0;
#pragma unroll 1
    for (int t = 2; t < 24; ++t) {
        WAIT_VI(2 * NH); BAR();
        tile(hs); hs = hs == (H_SLOTS - 1) * H_SLOT ? 0 : hs + H_SLOT;
    }
    WAIT_VI(2 * NH); BAR();
    WAIT_VI(NH); BAR();
    WAIT_V(0); BAR();
}

__global__ __launch_bounds__(512, 2) void geglu_mlp(const void* __restrict__ x, const void* __restrict__ wg, const void* __restrict__ wu,
                                                    const void* __restrict__ wd, void* __restrict__ out, char* __restrict__ ws) {
    extern __shared__ __attribute__((aligned(16))) char smem_[];
    LAS char* smem = (LAS char*)smem_;
    const unsigned lds0 = (unsigned)(size_t)smem;
    const int lane = threadIdx.x & 63, w = rfl(threadIdx.x >> 6);
    const int xcd = blockIdx.x & 7, j = blockIdx.x >> 3;
    const int fs = (xcd & 3) * 7 + (j >> 2), pm = (xcd >> 2) * 4 + (j & 3);
    const bool prod = j < 28 && fs < 27, cons = j < 30;
    const int mt = j / 10, nt = j - mt * 10;
    const int row2 = xcd * 256 + mt * 96, ntok = mt == 2 ? 64 : 96;
    const int rot = (nt * 27) / 10;
    GAS u64* flags = (GAS u64*)(ws + WS_FLAGS);
    GAS u64* epoch = flags + 256;
    void* h = ws;
    const int spare = j >= 28 ? xcd * 4 + (j - 28) : 32 + (xcd >> 2) * 4 + (j & 3);
    const int myslot = prod ? pm * 32 + fs : (spare / 5) * 32 + 27 + spare % 5;

    u64 tpa = 0, tpb = 0;
    if (w < 4) {
        if (w == 0) ST(0);
        const u64 e0 = __hip_atomic_load(epoch, __ATOMIC_RELAXED, __HIP_MEMORY_SCOPE_AGENT);
        const int wm = w >> 1, wn = w & 1, q4 = lane >> 4;
        const int tq = (lane & 15) >> 2, tp = lane & 3;
        const unsigned tsw = (((tq >> 1) & 1) | ((q4 & 1) << 1)) << 1;
        const unsigned awc = (((wn * 4 + (tp >> 1)) ^ tsw) * 16) + 8 * (tp & 1);
        const unsigned xsw = (q4 ^ (lane & 6)) * 16;
        if (prod) {
            f32x4 ag[8][2], au[8][2];
#pragma unroll
            for (int m = 0; m < 8; ++m)
#pragma unroll
                for (int n = 0; n < 2; ++n) { ag[m][n] = (f32x4){0.f, 0.f, 0.f, 0.f}; au[m][n] = (f32x4){0.f, 0.f, 0.f, 0.f}; }
            if (threadIdx.x == 0) *(LAS unsigned*)(smem + MISC) = 0u;
            const unsigned axb = lds0 + (wm * 128 + (lane & 15)) * 128 + xsw;
            const unsigned awb = lds0 + (q4 >> 1) * 4096 + (8 * (q4 & 1) + tq) * 128 + awc;
            WAIT_L(0);
            unsigned sl = 2 * P1_SLOT;
            OWN_FRAGS();
            BAR();
            if (w == 0) ST(1);
            p1_reads_only<0, 0, 0>(axb + sl, awb + sl, (awb ^ 32) + sl);
            WAIT_L(0);
#pragma unroll 1
            for (int t = 0; t < 10; ++t) {
                OWN_FRAGS();
                P1Stage<1, 1, 0>::run(ag, au, (axb ^ 64) + sl, awb + sl, (awb ^ 32) + sl);
                WAIT_L(0);
                BAR();
                sl = sl == 2 * P1_SLOT ? 0 : sl + P1_SLOT;
                P1Stage<0, 0, 0>::run(ag, au, axb + sl, awb + sl, (awb ^ 32) + sl);
                WAIT_L(0);
            }
            asm volatile("s_nop 7\n\ts_nop 7" ::: "memory");
            __builtin_amdgcn_sched_barrier(0);
            if (w == 0) ST(2);
            const __amdgpu_buffer_rsrc_t rh = __builtin_amdgcn_make_buffer_rsrc(h, 0, 8u << 20, 0x00020000);
            const unsigned ho = (pm >> 1) * (2u << 20) + ((pm & 1) * 256 + wm * 128 + (lane & 15)) * WROW + (fs * 64 + wn * 32 + (q4 & 1) * 16 + (q4 >> 1) * 8) * 2;
#pragma unroll
            for (int m = 0; m < 8; ++m) {
                unsigned d[2][2];
#pragma unroll
                for (int n = 0; n < 2; ++n) {
                    d[n][0] = geglu2(ag[m][n].xy, au[m][n].xy);
                    d[n][1] = geglu2(ag[m][n].zw, au[m][n].zw);
                }
                const auto s0 = __builtin_amdgcn_permlane16_swap(d[0][0], d[1][0], false, false);
                const auto s1 = __builtin_amdgcn_permlane16_swap(d[0][1], d[1][1], false, false);
                const u32x4 v = {s0[0], s1[0], s0[1], s1[1]};
                __builtin_amdgcn_raw_buffer_store_b128(v, rh, ho + m * 16 * WROW, 0, 16);
            }
            if (w == 0) ST(3);
            WAIT_V(0);
            if (w == 0) ST(4);
            unsigned old = 0;
            if (lane == 0) old = __hip_atomic_fetch_add((LAS unsigned*)(smem + MISC), 1u, __ATOMIC_RELAXED, __HIP_MEMORY_SCOPE_WORKGROUP);
            if (rfl(old) == 3 && lane == 0) __hip_atomic_store(flags + myslot, e0 * LCG_A + LCG_C, __ATOMIC_RELAXED, __HIP_MEMORY_SCOPE_AGENT);
        } else if (w == 0 && lane == 0) {
            __hip_atomic_store(flags + myslot, e0 * LCG_A + LCG_C, __ATOMIC_RELAXED, __HIP_MEMORY_SCOPE_AGENT);
        }
        if (cons) {
            f32x4 acc[3][2];
#pragma unroll
            for (int m = 0; m < 3; ++m) { acc[m][0] = (f32x4){0.f, 0.f, 0.f, 0.f}; acc[m][1] = (f32x4){0.f, 0.f, 0.f, 0.f}; }
            const int tw = ntok >> 1;
            const unsigned ahb = lds0 + H_BASE + (wm * tw + (lane & 15)) * 128 + xsw;
            const unsigned awd = lds0 + (8 * q4 + tq) * 128 + awc;
            unsigned hs = 0, wsl = 0;
            OWN_FRAGS();
            BAR();
            if (w == 0) ST(8);
            P2Stage<0, false, 0>::run(acc, ahb, ahb ^ 64, awd, awd ^ 32);
#pragma unroll 1
            for (int t = 1; t < 27; t += 2) {
                OWN_FRAGS();
                hs = hs == (H_SLOTS - 1) * H_SLOT ? 0 : hs + H_SLOT; wsl = wsl == (WD_SLOTS - 1) * WD_SLOT ? 0 : wsl + WD_SLOT;
                { const unsigned a0 = ahb + hs, a1 = (ahb ^ 64) + hs, b0 = awd + wsl, b1 = (awd ^ 32) + wsl;
                  BAR(); WAIT_L(0);
                  P2Stage<1, true, 0>::run(acc, a0, a1, b0, b1); }
                hs = hs == (H_SLOTS - 1) * H_SLOT ? 0 : hs + H_SLOT; wsl = wsl == (WD_SLOTS - 1) * WD_SLOT ? 0 : wsl + WD_SLOT;
                { const unsigned a0 = ahb + hs, a1 = (ahb ^ 64) + hs, b0 = awd + wsl, b1 = (awd ^ 32) + wsl;
                  BAR(); WAIT_L(0);
                  P2Stage<0, true, 0>::run(acc, a0, a1, b0, b1); }
            }
            WAIT_L(0);
            p2_mfma_only<0, 0>(acc);
            asm volatile("s_nop 7\n\ts_nop 7" ::: "memory");
            __builtin_amdgcn_sched_barrier(0);
            if (w == 0) ST(9);
            const __amdgpu_buffer_rsrc_t ro = __builtin_amdgcn_make_buffer_rsrc(out, 0, M_ * D_ * 2, 0x00020000);
            const unsigned oo = (row2 + wm * tw + (lane & 15)) * XROW + (nt * 64 + wn * 32 + (q4 & 1) * 16 + (q4 >> 1) * 8) * 2;
#pragma unroll
            for (int m = 0; m < 3; ++m) {
                const auto s0 = __builtin_amdgcn_permlane16_swap(pk(acc[m][0][0], acc[m][0][1]), pk(acc[m][1][0], acc[m][1][1]), false, false);
                const auto s1 = __builtin_amdgcn_permlane16_swap(pk(acc[m][0][2], acc[m][0][3]), pk(acc[m][1][2], acc[m][1][3]), false, false);
                const u32x4 v = {s0[0], s1[0], s0[1], s1[1]};
                if (m * 16 < tw) __builtin_amdgcn_raw_buffer_store_b128(v, ro, oo + m * 16 * XROW, 0, OUTAUX);
            }
            if (w == 0) ST(10);
            if (w == 0 && blockIdx.x == 9) { const u64 tend = __builtin_amdgcn_s_memrealtime() + 4 * (tpb - tpa); while (__builtin_amdgcn_s_memrealtime() < tend) __builtin_amdgcn_s_sleep(1); }
        }
    } else {
        const int lw = w - 4;
        unsigned cpf0 = 0, cpf1 = 0;
        if (lw == 3) {
            u64 pc; asm volatile("s_getpc_b64 %0" : "=s"(pc));
            const u64 base = pc & ~127ull;
            const char* a = (const char*)base - CODEPF_BACK + lane * 128;
            asm volatile("global_load_dword %0, %1, off" : "=&v"(cpf0) : "v"(a) : "memory");
            const char* b = a + 8192;
            if (lane < CODEPF_N2) asm volatile("global_load_dword %0, %1, off" : "=&v"(cpf1) : "v"(b) : "memory");
        }
        unsigned tpf = 0;
        if (lw == 2) {
            const char* p = lane == 0 ? (const char*)wd : lane == 1 ? (const char*)wd + (F_ * D_ * 2 - 4) : lane == 2 ? (const char*)out : lane == 3 ? (const char*)out + (M_ * D_ * 2 - 4)
                          : (const char*)ws + (size_t)(lane - 4) * (2u << 20) + (2u << 20) - 128;
            if (lane < 8) asm volatile("global_load_dword %0, %1, off" : "=&v"(tpf) : "v"(p) : "memory");
        }
        u64 e0;
        asm volatile("global_load_dwordx2 %0, %1, off sc1" : "=&v"(e0) : "v"(epoch) : "memory");
        const unsigned r8 = lane >> 3, c8 = lane & 7;
        const unsigned swx = (c8 ^ (r8 & 6)) * 16;
        const unsigned sww0 = (c8 ^ (((r8 >> 1) & 1) << 1)) * 16;
        const unsigned sww1 = (c8 ^ ((((r8 >> 1) & 1) | 2) << 1)) * 16;
        const u32x4 rwd = mksrd(wd, F_ * D_ * 2);
        constexpr unsigned DX = 8 * XROW - 1024, DW = 8 * WROW - 1024;
        const unsigned vd0 = r8 * XROW + sww0, vd1 = r8 * XROW + sww1 + DX, vd2 = vd0 + 2 * DX, vd3 = vd1 + 2 * DX;
        auto wd_batch = [&](int b) {
            int tr = 6 * b + rot; if (tr >= 27) tr -= 27;
            unsigned so = (tr * 64 + lw * 16) * XROW + nt * 128, ld = lds0 + 6 * b * WD_SLOT + lw * 2048;
#pragma unroll 1
            for (int t = 0; t < 6; ++t) {
                dma2(ld, rwd, so, vd0, vd1);
                ld += WD_SLOT; so += 64 * XROW; if (++tr == 27) { tr = 0; so -= 27 * 64 * XROW; }
            }
        };
        if (prod) {
            const u32x4 rx = mksrd(x, M_ * D_ * 2), rg = mksrd(wg, D_ * F_ * 2), ru = mksrd((const char*)wu - 2048, D_ * F_ * 2 + 2048);
            const unsigned vx0 = r8 * XROW + swx, vx1 = vx0 + DX, vx2 = vx0 + 2 * DX, vx3 = vx0 + 3 * DX;
            const unsigned vg0 = r8 * WROW + sww0, vg1 = r8 * WROW + sww1 + DW;
#define ROT1 1
            int kt = ROT1 ? ((j >> 2) + 3 * (j & 3)) % 10 : 0;
            unsigned sx = (pm * 256 + lw * 64) * XROW + kt * 128, sw = (kt * 64 + lw * 16) * WROW + fs * 128;
            auto stage = [&](unsigned slot) {
                const unsigned b = lds0 + slot;
                dma22(b + 32768 + lw * 4096, rg, ru, sw, vg0, vg1);
                dma4(b + lw * 8192, rx, sx, vx0, vx1, vx2, vx3);
                dma4(b + lw * 8192 + 4096, rx, sx + 32 * XROW, vx0, vx1, vx2, vx3);
                sx += 128; sw += 64 * WROW; if (++kt == 10) { kt = 0; sx -= 1280; sw -= 640 * WROW; }
            };
            stage(2 * P1_SLOT); stage(0); stage(P1_SLOT);
            WAIT_V(24);
            asm volatile("" :: "v"(cpf0), "v"(cpf1));
            asm volatile("" :: "v"(tpf));
            BAR();
            WAIT_V(12); BAR();
            unsigned sl = 2 * P1_SLOT;
#pragma unroll 1
            for (int t = 1; t < 8; ++t) {
                stage(sl);
                sl = sl == 2 * P1_SLOT ? 0 : sl + P1_SLOT;
                WAIT_V(12); BAR();
            }
            wd_batch(0);
            WAIT_V(12); BAR();
            wd_batch(1);
            BAR();
        } else {
            if (cons) { wd_batch(0); wd_batch(1); }
            WAIT_V(0);
            asm volatile("" :: "v"(cpf0), "v"(cpf1));
            asm volatile("" :: "v"(tpf));
        }
        if (cons) {
            if (lw < 2) {
                unsigned dummy = 0;
#pragma unroll 1
                for (int t = WD_SLOTS + mt * 2 + lw; t < 27; t += 6) {
                    const int tr = t + rot >= 27 ? t + rot - 27 : t + rot;
                    asm volatile("buffer_load_dword %0, %1, %2, %3 offen" : "+v"(dummy) : "v"(lane * XROW), "s"(rwd), "s"((unsigned)(tr * 64 * XROW + nt * 128)) : "memory");
                }
                WAIT_V(0);
                asm volatile("" :: "v"(dummy));
                if (lw == 0) ST(11);
                int tr = WD_SLOTS + rot; if (tr >= 27) tr -= 27;
                unsigned so = (tr * 64 + lw * 32) * XROW + nt * 128, ld = 0;
                BAR(); BAR();
#pragma unroll 1
                for (int t = 2; t < 12; ++t) {
                    BAR();
                    dma4(lds0 + ld + lw * 4096, rwd, so, vd0, vd1, vd2, vd3);
                    ld += WD_SLOT; so += 64 * XROW; if (++tr == 27) { tr = 0; so -= 27 * 64 * XROW; }
                }
#pragma unroll 1
                for (int t = 12; t < 17; ++t) {
                    WAIT_V(36); BAR();
                    dma4(lds0 + ld + lw * 4096, rwd, so, vd0, vd1, vd2, vd3);
                    ld = ld == (WD_SLOTS - 1) * WD_SLOT ? 0 : ld + WD_SLOT; so += 64 * XROW; if (++tr == 27) { tr = 0; so -= 27 * 64 * XROW; }
                }
                WAIT_V(36); BAR();
                WAIT_V(32); BAR(); WAIT_V(28); BAR(); WAIT_V(24); BAR(); WAIT_V(20); BAR(); WAIT_V(16); BAR();
                WAIT_V(12); BAR(); WAIT_V(8); BAR(); WAIT_V(4); BAR(); WAIT_V(0); BAR();
            } else {
                WAIT_V(0);
                asm volatile("" : "+v"(e0));
                const u64 tag = e0 * LCG_A + LCG_C;
                GAS u64* pf = flags + xcd * 32 + (lane & 31);
                const u64 t0 = __builtin_amdgcn_s_memrealtime();
                for (;;) {
                    const u64 v = __hip_atomic_load(pf, __ATOMIC_RELAXED, __HIP_MEMORY_SCOPE_AGENT);
                    if (__all(v == tag)) break;
                    if (__builtin_amdgcn_s_memrealtime() - t0 > 5000000ull) break;
                    __builtin_amdgcn_s_sleep(1);
                }
                if (lw == 2) ST(5);
                const u32x4 rh = mksrd(h, 8u << 20);
                const unsigned v0 = r8 * WROW + swx, v1 = v0 + DW, v2 = v0 + 2 * DW, v3 = v0 + 3 * DW;
                const int hl = lw - 2;
                if (ntok == 96) h_stream<6>(rh, (xcd >> 1) * (2u << 20) + ((xcd & 1) * 256 + mt * 96 + hl * 48) * WROW + rot * 128, lds0 + H_BASE + hl * 6144, rot, v0, v1, v2, v3);
                else h_stream<4>(rh, (xcd >> 1) * (2u << 20) + ((xcd & 1) * 256 + mt * 96 + hl * 32) * WROW + rot * 128, lds0 + H_BASE + hl * 4096, rot, v0, v1, v2, v3);
            }
        }
        if (blockIdx.x == 0 && lw == 0) {
            WAIT_V(0);
            asm volatile("" : "+v"(e0));
            const u64 tag = e0 * LCG_A + LCG_C;
            const u64 t0 = __builtin_amdgcn_s_memrealtime();
            for (;;) {
                bool ok = true;
#pragma unroll
                for (int i = 0; i < 4; ++i) ok &= __hip_atomic_load(flags + i * 64 + lane, __ATOMIC_RELAXED, __HIP_MEMORY_SCOPE_AGENT) == tag;
                if (__all(ok)) break;
                if (__builtin_amdgcn_s_memrealtime() - t0 > 5000000ull) break;
                __builtin_amdgcn_s_sleep(1);
            }
            if (lane == 0) __hip_atomic_store(epoch, tag, __ATOMIC_RELAXED, __HIP_MEMORY_SCOPE_AGENT);
        }
    }
}

extern "C" void kernel_launch(void* const* d_in, const int* in_sizes, int n_in, void* d_out, int out_size, void* d_ws, size_t ws_size, hipStream_t stream) {
    (void)in_sizes; (void)n_in; (void)out_size; (void)ws_size;
    (void)hipFuncSetAttribute((const void*)geglu_mlp, hipFuncAttributeMaxDynamicSharedMemorySize, LDS_BYTES);
    geglu_mlp<<<256, 512, LDS_BYTES, stream>>>(d_in[0], d_in[1], d_in[2], d_in[3], d_out, (char*)d_ws);
}
```

```cpp
#define PROBE_A 2
#define PROBE_B 3
#define HAUX 16
#define CODEPF
#define CODEPF_BACK 256
#define CODEPF_N2 44
#define TLBPF
#include <hip/hip_runtime.h>
#include <hip/hip_bf16.h>

#define LAS __attribute__((address_space(3)))
#define GAS __attribute__((address_space(1)))
typedef __bf16 bf16x2 __attribute__((ext_vector_type(2)));
typedef float f32x2 __attribute__((ext_vector_type(2)));
typedef float f32x4 __attribute__((ext_vector_type(4)));
typedef unsigned u32x4 __attribute__((ext_vector_type(4)));
typedef unsigned long long u64;

#define OUTAUX 0
constexpr int M_ = 2048, D_ = 640, F_ = 1728;
constexpr unsigned XROW = D_ * 2, WROW = F_ * 2;
constexpr int P1_SLOT = 49152;
constexpr int WD_SLOTS = 12, WD_SLOT = 8192, H_BASE = 98304, H_SLOTS = 5, H_SLOT = 12288;
constexpr int MISC = H_BASE + H_SLOTS * H_SLOT;
constexpr int LDS_BYTES = 160 * 1024;
constexpr size_t WS_FLAGS = 8u << 20;
constexpr u64 LCG_A = 6364136223846793005ull, LCG_C = 1442695040888963407ull;

#define WAIT_V(n) asm volatile("s_waitcnt vmcnt(" #n ")" ::: "memory")
#define WAIT_VI(n) asm volatile("s_waitcnt vmcnt(%0)" :: "i"(n) : "memory")
#define WAIT_L(n) asm volatile("s_waitcnt lgkmcnt(" #n ")" ::: "memory")
#define BAR() asm volatile("s_barrier" ::: "memory")
#define DI __device__ __forceinline__

#define ST(i) do { if ((i) == PROBE_A) tpa = __builtin_amdgcn_s_memrealtime(); if ((i) == PROBE_B) tpb = __builtin_amdgcn_s_memrealtime(); } while (0)

#define FCLOB "v160","v161","v162","v163","v164","v165","v166","v167","v168","v169","v170","v171","v172","v173","v174","v175","v176","v177","v178","v179","v180","v181","v182","v183","v184","v185","v186","v187","v188","v189","v190","v191", \
  "v192","v193","v194","v195","v196","v197","v198","v199","v200","v201","v202","v203","v204","v205","v206","v207","v208","v209","v210","v211","v212","v213","v214","v215","v216","v217","v218","v219","v220","v221","v222","v223", \
  "v224","v225","v226","v227","v228","v229","v230","v231","v232","v233","v234","v235","v236","v237","v238","v239","v240","v241","v242","v243","v244","v245","v246","v247","v248","v249","v250","v251","v252","v253","v254","v255"
#define OWN_FRAGS() asm volatile("" ::: FCLOB)
constexpr int FR = 160;

DI unsigned rfl(unsigned v) { return __builtin_amdgcn_readfirstlane(v); }
DI u32x4 mksrd(const void* p, unsigned bytes) {
    const u64 a = (u64)p;
    return (u32x4){(unsigned)a, (unsigned)(a >> 32) & 0xffffu, bytes, 0x00020000u};
}
#define DMA_HEAD "s_mov_b32 m0, %0\n\ts_nop 0\n\t"
DI void dma4(unsigned dst, u32x4 r, unsigned soff, unsigned v0, unsigned v1, unsigned v2, unsigned v3) {
    asm volatile(DMA_HEAD "buffer_load_dwordx4 %3, %1, %2 offen lds\n\tbuffer_load_dwordx4 %4, %1, %2 offen offset:1024 lds\n\t"
                 "buffer_load_dwordx4 %5, %1, %2 offen offset:2048 lds\n\tbuffer_load_dwordx4 %6, %1, %2 offen offset:3072 lds"
                 :: "s"(dst), "s"(r), "s"(soff), "v"(v0), "v"(v1), "v"(v2), "v"(v3) : "memory", "m0");
}
DI void dma2(unsigned dst, u32x4 r, unsigned soff, unsigned v0, unsigned v1) {
    asm volatile(DMA_HEAD "buffer_load_dwordx4 %3, %1, %2 offen lds\n\tbuffer_load_dwordx4 %4, %1, %2 offen offset:1024 lds"
                 :: "s"(dst), "s"(r), "s"(soff), "v"(v0), "v"(v1) : "memory", "m0");
}
DI void dma22(unsigned dst, u32x4 ra, u32x4 rb, unsigned soff, unsigned v0, unsigned v1) {
    asm volatile(DMA_HEAD "buffer_load_dwordx4 %4, %1, %3 offen lds\n\tbuffer_load_dwordx4 %5, %1, %3 offen offset:1024 lds\n\t"
                 "buffer_load_dwordx4 %4, %2, %3 offen offset:2048 lds\n\tbuffer_load_dwordx4 %5, %2, %3 offen offset:3072 lds"
                 :: "s"(dst), "s"(ra), "s"(rb), "s"(soff), "v"(v0), "v"(v1) : "memory", "m0");
}
DI void dma4_sc1(unsigned dst, u32x4 r, unsigned soff, unsigned v0, unsigned v1, unsigned v2, unsigned v3) {
    asm volatile(DMA_HEAD "buffer_load_dwordx4 %3, %1, %2 offen sc1 lds\n\tbuffer_load_dwordx4 %4, %1, %2 offen offset:1024 sc1 lds\n\t"
                 "buffer_load_dwordx4 %5, %1, %2 offen offset:2048 sc1 lds\n\tbuffer_load_dwordx4 %6, %1, %2 offen offset:3072 sc1 lds"
                 :: "s"(dst), "s"(r), "s"(soff), "v"(v0), "v"(v1), "v"(v2), "v"(v3) : "memory", "m0");
}
DI void dma2_sc1(unsigned dst, u32x4 r, unsigned soff, unsigned v0, unsigned v1) {
    asm volatile(DMA_HEAD "buffer_load_dwordx4 %3, %1, %2 offen sc1 lds\n\tbuffer_load_dwordx4 %4, %1, %2 offen offset:1024 sc1 lds"
                 :: "s"(dst), "s"(r), "s"(soff), "v"(v0), "v"(v1) : "memory", "m0");
}
template <int R, int OFF> DI void rd128(unsigned addr) { asm volatile("ds_read_b128 v[%c1:%c2], %0 offset:%c3" :: "v"(addr), "i"(FR + R), "i"(FR + R + 3), "i"(OFF)); }
template <int R, int OFF> DI void rdtr(unsigned addr) { asm volatile("ds_read_b64_tr_b16 v[%c1:%c2], %0 offset:%c3" :: "v"(addr), "i"(FR + R), "i"(FR + R + 1), "i"(OFF)); }
template <int RA, int RB> DI void mfma(f32x4& acc) {
    asm volatile("v_mfma_f32_16x16x32_bf16 %0, v[%c1:%c2], v[%c3:%c4], %0" : "+v"(acc) : "i"(FR + RA), "i"(FR + RA + 3), "i"(FR + RB), "i"(FR + RB + 3));
}
DI unsigned pk(float a, float b) { bf16x2 v = {(__bf16)a, (__bf16)b}; return __builtin_bit_cast(unsigned, v); }
DI unsigned geglu2(f32x2 g, f32x2 u) {
    const f32x2 c1 = {-2.302208198f, -2.302208198f}, c2 = {-0.1029432397f, -0.1029432397f};
    const f32x2 a = g * __builtin_elementwise_fma(g * g, c2, c1);
    const f32x2 d = (f32x2){__builtin_amdgcn_exp2f(a.x), __builtin_amdgcn_exp2f(a.y)} + 1.0f;
    const f32x2 r = (g * u) * (f32x2){__builtin_amdgcn_rcpf(d.x), __builtin_amdgcn_rcpf(d.y)};
    return pk(r.x, r.y);
}

template <int S, int KS, int I> DI void p1_read(unsigned ax, unsigned aw0, unsigned aw1) {
    constexpr int B = 48 * S;
    if constexpr (I < 8) { constexpr int n = I >> 2, up = (I >> 1) & 1, t = I & 1;
        rdtr<B + 32 + 8 * up + 4 * n + 2 * t, 32768 + KS * 8192 + up * 2048 + t * 512>(n ? aw1 : aw0); }
    else rd128<B + 4 * (I - 8), (I - 8) * 2048>(ax);
}
template <int S, int I> DI void p1_mfma(f32x4 (&ag)[8][2], f32x4 (&au)[8][2]) {
    constexpr int B = 48 * S, m = I >> 2, n = (I >> 1) & 1, up = I & 1;
    if constexpr (up) mfma<B + 40 + 4 * n, B + 4 * m>(au[m][n]); else mfma<B + 32 + 4 * n, B + 4 * m>(ag[m][n]);
}
template <int SR, int KS, int I> struct P1Stage {
    static DI void run(f32x4 (&ag)[8][2], f32x4 (&au)[8][2], unsigned ax, unsigned aw0, unsigned aw1) {
        p1_mfma<SR ^ 1, I>(ag, au);
        if constexpr (I % 3 != 2 && (I / 3) * 2 + I % 3 < 16) p1_read<SR, KS, (I / 3) * 2 + I % 3>(ax, aw0, aw1);
        if constexpr (I + 1 < 32) P1Stage<SR, KS, I + 1>::run(ag, au, ax, aw0, aw1);
    }
};
template <int S, int KS, int I> DI void p1_reads_only(unsigned ax, unsigned aw0, unsigned aw1) {
    p1_read<S, KS, I>(ax, aw0, aw1);
    if constexpr (I + 1 < 16) p1_reads_only<S, KS, I + 1>(ax, aw0, aw1);
}

template <int S, int I> DI void p2_read(unsigned ah0, unsigned ah1, unsigned aw0, unsigned aw1) {
    constexpr int ks = I / 7, r = I % 7, B = 40 * S + 20 * ks;
    if constexpr (r < 4) { constexpr int n = r >> 1, t = r & 1; rdtr<B + 12 + 4 * n + 2 * t, ks * 4096 + t * 512>(n ? aw1 : aw0); }
    else rd128<B + 4 * (r - 4), (r - 4) * 2048>(ks ? ah1 : ah0);
}
template <int S, int I> DI void p2_mfma(f32x4 (&acc)[3][2]) {
    constexpr int ks = I / 6, m = (I % 6) >> 1, n = I & 1, B = 40 * S + 20 * ks;
    mfma<B + 12 + 4 * n, B + 4 * m>(acc[m][n]);
}
template <int SR, bool MM, int I> struct P2Stage {
    static DI void run(f32x4 (&acc)[3][2], unsigned ah0, unsigned ah1, unsigned aw0, unsigned aw1) {
        if constexpr (MM) p2_mfma<SR ^ 1, I>(acc);
        if constexpr (I < 7) { p2_read<SR, 2 * I>(ah0, ah1, aw0, aw1); p2_read<SR, 2 * I + 1>(ah0, ah1, aw0, aw1); }
        if constexpr (I + 1 < 12) P2Stage<SR, MM, I + 1>::run(acc, ah0, ah1, aw0, aw1);
    }
};
template <int S, int I> DI void p2_mfma_only(f32x4 (&acc)[3][2]) { p2_mfma<S, I>(acc); if constexpr (I + 1 < 12) p2_mfma_only<S, I + 1>(acc); }

template <int NH> DI void h_stream(u32x4 rh, unsigned so, unsigned lh0, int tr, unsigned v0, unsigned v1, unsigned v2, unsigned v3) {
    auto tile = [&](unsigned slot) {
        dma4_sc1(lh0 + slot, rh, so, v0, v1, v2, v3);
        if constexpr (NH == 6) dma2_sc1(lh0 + slot + 4096, rh, so + 32 * WROW, v0, v1);
        so += 128; if (++tr == 27) { tr = 0; so -= 27 * 128; }
    };
#pragma unroll 1
    for (int s = 0; s < H_SLOTS; ++s) tile(s * H_SLOT);
    WAIT_VI(4 * NH); BAR();
    WAIT_VI(3 * NH); BAR();
    unsigned hs = 0;
#pragma unroll 1
    for (int t = 2; t < 24; ++t) {
        WAIT_VI(2 * NH); BAR();
        tile(hs); hs = hs == (H_SLOTS - 1) * H_SLOT ? 0 : hs + H_SLOT;
    }
    WAIT_VI(2 * NH); BAR();
    WAIT_VI(NH); BAR();
    WAIT_V(0); BAR();
}

__global__ __launch_bounds__(512, 2) void geglu_mlp(const void* __restrict__ x, const void* __restrict__ wg, const void* __restrict__ wu,
                                                    const void* __restrict__ wd, void* __restrict__ out, char* __restrict__ ws) {
    extern __shared__ __attribute__((aligned(16))) char smem_[];
    LAS char* smem = (LAS char*)smem_;
    const unsigned lds0 = (unsigned)(size_t)smem;
    const int lane = threadIdx.x & 63, w = rfl(threadIdx.x >> 6);
    const int xcd = blockIdx.x & 7, j = blockIdx.x >> 3;
    const int fs = (xcd & 3) * 7 + (j >> 2), pm = (xcd >> 2) * 4 + (j & 3);
    const bool prod = j < 28 && fs < 27, cons = j < 30;
    const int mt = j / 10, nt = j - mt * 10;
    const int row2 = xcd * 256 + mt * 96, ntok = mt == 2 ? 64 : 96;
    const int rot = (nt * 27) / 10;
    GAS u64* flags = (GAS u64*)(ws + WS_FLAGS);
    GAS u64* epoch = flags + 256;
    void* h = ws;
    const int spare = j >= 28 ? xcd * 4 + (j - 28) : 32 + (xcd >> 2) * 4 + (j & 3);
    const int myslot = prod ? pm * 32 + fs : (spare / 5) * 32 + 27 + spare % 5;

    u64 tpa = 0, tpb = 0;
    if (w < 4) {
        if (w == 0) ST(0);
        const u64 e0 = __hip_atomic_load(epoch, __ATOMIC_RELAXED, __HIP_MEMORY_SCOPE_AGENT);
        const int wm = w >> 1, wn = w & 1, q4 = lane >> 4;
        const int tq = (lane & 15) >> 2, tp = lane & 3;
        const unsigned tsw = (((tq >> 1) & 1) | ((q4 & 1) << 1)) << 1;
        const unsigned awc = (((wn * 4 + (tp >> 1)) ^ tsw) * 16) + 8 * (tp & 1);
        const unsigned xsw = (q4 ^ (lane & 6)) * 16;
        if (prod) {
            f32x4 ag[8][2], au[8][2];
#pragma unroll
            for (int m = 0; m < 8; ++m)
#pragma unroll
                for (int n = 0; n < 2; ++n) { ag[m][n] = (f32x4){0.f, 0.f, 0.f, 0.f}; au[m][n] = (f32x4){0.f, 0.f, 0.f, 0.f}; }
            if (threadIdx.x == 0) *(LAS unsigned*)(smem + MISC) = 0u;
            const unsigned axb = lds0 + (wm * 128 + (lane & 15)) * 128 + xsw;
            const unsigned awb = lds0 + (q4 >> 1) * 4096 + (8 * (q4 & 1) + tq) * 128 + awc;
            WAIT_L(0);
            unsigned sl = 2 * P1_SLOT;
            OWN_FRAGS();
            BAR();
            if (w == 0) ST(1);
            p1_reads_only<0, 0, 0>(axb + sl, awb + sl, (awb ^ 32) + sl);
            WAIT_L(0);
#pragma unroll 1
            for (int t = 0; t < 10; ++t) {
                OWN_FRAGS();
                P1Stage<1, 1, 0>::run(ag, au, (axb ^ 64) + sl, awb + sl, (awb ^ 32) + sl);
                WAIT_L(0);
                BAR();
                sl = sl == 2 * P1_SLOT ? 0 : sl + P1_SLOT;
                P1Stage<0, 0, 0>::run(ag, au, axb + sl, awb + sl, (awb ^ 32) + sl);
                WAIT_L(0);
            }
            asm volatile("s_nop 7\n\ts_nop 7" ::: "memory");
            __builtin_amdgcn_sched_barrier(0);
            if (w == 0) ST(2);
            const __amdgpu_buffer_rsrc_t rh = __builtin_amdgcn_make_buffer_rsrc(h, 0, 8u << 20, 0x00020000);
            const unsigned ho = (pm >> 1) * (2u << 20) + ((pm & 1) * 256 + wm * 128 + (lane & 15)) * WROW + (fs * 64 + wn * 32 + (q4 & 1) * 16 + (q4 >> 1) * 8) * 2;
#pragma unroll
            for (int m = 0; m < 8; ++m) {
                unsigned d[2][2];
#pragma unroll
                for (int n = 0; n < 2; ++n) {
                    d[n][0] = geglu2(ag[m][n].xy, au[m][n].xy);
                    d[n][1] = geglu2(ag[m][n].zw, au[m][n].zw);
                }
                const auto s0 = __builtin_amdgcn_permlane16_swap(d[0][0], d[1][0], false, false);
                const auto s1 = __builtin_amdgcn_permlane16_swap(d[0][1], d[1][1], false, false);
                const u32x4 v = {s0[0], s1[0], s0[1], s1[1]};
                __builtin_amdgcn_raw_buffer_store_b128(v, rh, ho + m * 16 * WROW, 0, HAUX);
            }
            if (w == 0) ST(3);
            WAIT_V(0);
            if (w == 0) ST(4);
            unsigned old = 0;
            if (lane == 0) old = __hip_atomic_fetch_add((LAS unsigned*)(smem + MISC), 1u, __ATOMIC_RELAXED, __HIP_MEMORY_SCOPE_WORKGROUP);
            if (rfl(old) == 3 && lane == 0) __hip_atomic_store(flags + myslot, e0 * LCG_A + LCG_C, __ATOMIC_RELAXED, __HIP_MEMORY_SCOPE_AGENT);
        } else if (w == 0 && lane == 0) {
            __hip_atomic_store(flags + myslot, e0 * LCG_A + LCG_C, __ATOMIC_RELAXED, __HIP_MEMORY_SCOPE_AGENT);
        }
        if (cons) {
            f32x4 acc[3][2];
#pragma unroll
            for (int m = 0; m < 3; ++m) { acc[m][0] = (f32x4){0.f, 0.f, 0.f, 0.f}; acc[m][1] = (f32x4){0.f, 0.f, 0.f, 0.f}; }
            const int tw = ntok >> 1;
            const unsigned ahb = lds0 + H_BASE + (wm * tw + (lane & 15)) * 128 + xsw;
            const unsigned awd = lds0 + (8 * q4 + tq) * 128 + awc;
            unsigned hs = 0, wsl = 0;
            OWN_FRAGS();
            BAR();
            if (w == 0) ST(8);
            P2Stage<0, false, 0>::run(acc, ahb, ahb ^ 64, awd, awd ^ 32);
#pragma unroll 1
            for (int t = 1; t < 27; t += 2) {
                OWN_FRAGS();
                hs = hs == (H_SLOTS - 1) * H_SLOT ? 0 : hs + H_SLOT; wsl = wsl == (WD_SLOTS - 1) * WD_SLOT ? 0 : wsl + WD_SLOT;
                { const unsigned a0 = ahb + hs, a1 = (ahb ^ 64) + hs, b0 = awd + wsl, b1 = (awd ^ 32) + wsl;
                  BAR(); WAIT_L(0);
                  P2Stage<1, true, 0>::run(acc, a0, a1, b0, b1); }
                hs = hs == (H_SLOTS - 1) * H_SLOT ? 0 : hs + H_SLOT; wsl = wsl == (WD_SLOTS - 1) * WD_SLOT ? 0 : wsl + WD_SLOT;
                { const unsigned a0 = ahb + hs, a1 = (ahb ^ 64) + hs, b0 = awd + wsl, b1 = (awd ^ 32) + wsl;
                  BAR(); WAIT_L(0);
                  P2Stage<0, true, 0>::run(acc, a0, a1, b0, b1); }
            }
            WAIT_L(0);
            p2_mfma_only<0, 0>(acc);
            asm volatile("s_nop 7\n\ts_nop 7" ::: "memory");
            __builtin_amdgcn_sched_barrier(0);
            if (w == 0) ST(9);
            const __amdgpu_buffer_rsrc_t ro = __builtin_amdgcn_make_buffer_rsrc(out, 0, M_ * D_ * 2, 0x00020000);
            const unsigned oo = (row2 + wm * tw + (lane & 15)) * XROW + (nt * 64 + wn * 32 + (q4 & 1) * 16 + (q4 >> 1) * 8) * 2;
#pragma unroll
            for (int m = 0; m < 3; ++m) {
                const auto s0 = __builtin_amdgcn_permlane16_swap(pk(acc[m][0][0], acc[m][0][1]), pk(acc[m][1][0], acc[m][1][1]), false, false);
                const auto s1 = __builtin_amdgcn_permlane16_swap(pk(acc[m][0][2], acc[m][0][3]), pk(acc[m][1][2], acc[m][1][3]), false, false);
                const u32x4 v = {s0[0], s1[0], s0[1], s1[1]};
                if (m * 16 < tw) __builtin_amdgcn_raw_buffer_store_b128(v, ro, oo + m * 16 * XROW, 0, OUTAUX);
            }
            if (w == 0) ST(10);
            if (w == 0 && blockIdx.x == 9) { const u64 tend = __builtin_amdgcn_s_memrealtime() + 4 * (tpb - tpa); while (__builtin_amdgcn_s_memrealtime() < tend) __builtin_amdgcn_s_sleep(1); }
        }
    } else {
        const int lw = w - 4;
        unsigned cpf0 = 0, cpf1 = 0;
        if (lw == 3) {
            u64 pc; asm volatile("s_getpc_b64 %0" : "=s"(pc));
            const u64 base = pc & ~127ull;
            const char* a = (const char*)base - CODEPF_BACK + lane * 128;
            asm volatile("global_load_dword %0, %1, off" : "=&v"(cpf0) : "v"(a) : "memory");
            const char* b = a + 8192;
            if (lane < CODEPF_N2) asm volatile("global_load_dword %0, %1, off" : "=&v"(cpf1) : "v"(b) : "memory");
        }
        unsigned tpf = 0;
        if (lw == 2) {
            const char* p = lane == 0 ? (const char*)wd : lane == 1 ? (const char*)wd + (F_ * D_ * 2 - 4) : lane == 2 ? (const char*)out : lane == 3 ? (const char*)out + (M_ * D_ * 2 - 4)
                          : (const char*)ws + (size_t)(lane - 4) * (2u << 20) + (2u << 20) - 128;
            if (lane < 8) asm volatile("global_load_dword %0, %1, off" : "=&v"(tpf) : "v"(p) : "memory");
        }
        u64 e0;
        asm volatile("global_load_dwordx2 %0, %1, off sc1" : "=&v"(e0) : "v"(epoch) : "memory");
        const unsigned r8 = lane >> 3, c8 = lane & 7;
        const unsigned swx = (c8 ^ (r8 & 6)) * 16;
        const unsigned sww0 = (c8 ^ (((r8 >> 1) & 1) << 1)) * 16;
        const unsigned sww1 = (c8 ^ ((((r8 >> 1) & 1) | 2) << 1)) * 16;
        const u32x4 rwd = mksrd(wd, F_ * D_ * 2);
        constexpr unsigned DX = 8 * XROW - 1024, DW = 8 * WROW - 1024;
        const unsigned vd0 = r8 * XROW + sww0, vd1 = r8 * XROW + sww1 + DX, vd2 = vd0 + 2 * DX, vd3 = vd1 + 2 * DX;
        auto wd_batch = [&](int b) {
            int tr = 6 * b + rot; if (tr >= 27) tr -= 27;
            unsigned so = (tr * 64 + lw * 16) * XROW + nt * 128, ld = lds0 + 6 * b * WD_SLOT + lw * 2048;
#pragma unroll 1
            for (int t = 0; t < 6; ++t) {
                dma2(ld, rwd, so, vd0, vd1);
                ld += WD_SLOT; so += 64 * XROW; if (++tr == 27) { tr = 0; so -= 27 * 64 * XROW; }
            }
        };
        if (prod) {
            const u32x4 rx = mksrd(x, M_ * D_ * 2), rg = mksrd(wg, D_ * F_ * 2), ru = mksrd((const char*)wu - 2048, D_ * F_ * 2 + 2048);
            const unsigned vx0 = r8 * XROW + swx, vx1 = vx0 + DX, vx2 = vx0 + 2 * DX, vx3 = vx0 + 3 * DX;
            const unsigned vg0 = r8 * WROW + sww0, vg1 = r8 * WROW + sww1 + DW;
#define ROT1 1
            int kt = ROT1 ? ((j >> 2) + 3 * (j & 3)) % 10 : 0;
            unsigned sx = (pm * 256 + lw * 64) * XROW + kt * 128, sw = (kt * 64 + lw * 16) * WROW + fs * 128;
            auto stage = [&](unsigned slot) {
                const unsigned b = lds0 + slot;
                dma22(b + 32768 + lw * 4096, rg, ru, sw, vg0, vg1);
                dma4(b + lw * 8192, rx, sx, vx0, vx1, vx2, vx3);
                dma4(b + lw * 8192 + 4096, rx, sx + 32 * XROW, vx0, vx1, vx2, vx3);
                sx += 128; sw += 64 * WROW; if (++kt == 10) { kt = 0; sx -= 1280; sw -= 640 * WROW; }
            };
            stage(2 * P1_SLOT); stage(0); stage(P1_SLOT);
            WAIT_V(24);
            asm volatile("" :: "v"(cpf0), "v"(cpf1));
            asm volatile("" :: "v"(tpf));
            BAR();
            WAIT_V(12); BAR();
            unsigned sl = 2 * P1_SLOT;
#pragma unroll 1
            for (int t = 1; t < 8; ++t) {
                stage(sl);
                sl = sl == 2 * P1_SLOT ? 0 : sl + P1_SLOT;
                WAIT_V(12); BAR();
            }
            wd_batch(0);
            WAIT_V(12); BAR();
            wd_batch(1);
            BAR();
        } else {
            if (cons) { wd_batch(0); wd_batch(1); }
            WAIT_V(0);
            asm volatile("" :: "v"(cpf0), "v"(cpf1));
            asm volatile("" :: "v"(tpf));
        }
        if (cons) {
            if (lw < 2) {
                unsigned dummy = 0;
#pragma unroll 1
                for (int t = 99; t < 27; t += 6) {
                    const int tr = t + rot >= 27 ? t + rot - 27 : t + rot;
                    asm volatile("buffer_load_dword %0, %1, %2, %3 offen" : "+v"(dummy) : "v"(lane * XROW), "s"(rwd), "s"((unsigned)(tr * 64 * XROW + nt * 128)) : "memory");
                }
                WAIT_V(0);
                asm volatile("" :: "v"(dummy));
                if (lw == 0) ST(11);
                int tr = WD_SLOTS + rot; if (tr >= 27) tr -= 27;
                unsigned so = (tr * 64 + lw * 32) * XROW + nt * 128, ld = 0;
                BAR(); BAR();
#pragma unroll 1
                for (int t = 2; t < 12; ++t) {
                    BAR();
                    dma4(lds0 + ld + lw * 4096, rwd, so, vd0, vd1, vd2, vd3);
                    ld += WD_SLOT; so += 64 * XROW; if (++tr == 27) { tr = 0; so -= 27 * 64 * XROW; }
                }
#pragma unroll 1
                for (int t = 12; t < 17; ++t) {
                    WAIT_V(36); BAR();
                    dma4(lds0 + ld + lw * 4096, rwd, so, vd0, vd1, vd2, vd3);
                    ld = ld == (WD_SLOTS - 1) * WD_SLOT ? 0 : ld + WD_SLOT; so += 64 * XROW; if (++tr == 27) { tr = 0; so -= 27 * 64 * XROW; }
                }
                WAIT_V(36); BAR();
                WAIT_V(32); BAR(); WAIT_V(28); BAR(); WAIT_V(24); BAR(); WAIT_V(20); BAR(); WAIT_V(16); BAR();
                WAIT_V(12); BAR(); WAIT_V(8); BAR(); WAIT_V(4); BAR(); WAIT_V(0); BAR();
            } else {
                WAIT_V(0);
                asm volatile("" : "+v"(e0));
                const u64 tag = e0 * LCG_A + LCG_C;
                GAS u64* pf = flags + xcd * 32 + (lane & 31);
                const u64 t0 = __builtin_amdgcn_s_memrealtime();
                for (;;) {
                    const u64 v = __hip_atomic_load(pf, __ATOMIC_RELAXED, __HIP_MEMORY_SCOPE_AGENT);
                    if (__all(v == tag)) break;
                    if (__builtin_amdgcn_s_memrealtime() - t0 > 5000000ull) break;
                    __builtin_amdgcn_s_sleep(1);
                }
                if (lw == 2) ST(5);
                const u32x4 rh = mksrd(h, 8u << 20);
                const unsigned v0 = r8 * WROW + swx, v1 = v0 + DW, v2 = v0 + 2 * DW, v3 = v0 + 3 * DW;
                const int hl = lw - 2;
                if (ntok == 96) h_stream<6>(rh, (xcd >> 1) * (2u << 20) + ((xcd & 1) * 256 + mt * 96 + hl * 48) * WROW + rot * 128, lds0 + H_BASE + hl * 6144, rot, v0, v1, v2, v3);
                else h_stream<4>(rh, (xcd >> 1) * (2u << 20) + ((xcd & 1) * 256 + mt * 96 + hl * 32) * WROW + rot * 128, lds0 + H_BASE + hl * 4096, rot, v0, v1, v2, v3);
            }
        }
        if (blockIdx.x == 0 && lw == 0) {
            WAIT_V(0);
            asm volatile("" : "+v"(e0));
            const u64 tag = e0 * LCG_A + LCG_C;
            const u64 t0 = __builtin_amdgcn_s_memrealtime();
            for (;;) {
                bool ok = true;
#pragma unroll
                for (int i = 0; i < 4; ++i) ok &= __hip_atomic_load(flags + i * 64 + lane, __ATOMIC_RELAXED, __HIP_MEMORY_SCOPE_AGENT) == tag;
                if (__all(ok)) break;
                if (__builtin_amdgcn_s_memrealtime() - t0 > 5000000ull) break;
                __builtin_amdgcn_s_sleep(1);
            }
            if (lane == 0) __hip_atomic_store(epoch, tag, __ATOMIC_RELAXED, __HIP_MEMORY_SCOPE_AGENT);
        }
    }
}

extern "C" void kernel_launch(void* const* d_in, const int* in_sizes, int n_in, void* d_out, int out_size, void* d_ws, size_t ws_size, hipStream_t stream) {
    (void)in_sizes; (void)n_in; (void)out_size; (void)ws_size;
    (void)hipFuncSetAttribute((const void*)geglu_mlp, hipFuncAttributeMaxDynamicSharedMemorySize, LDS_BYTES);
    geglu_mlp<<<256, 512, LDS_BYTES, stream>>>(d_in[0], d_in[1], d_in[2], d_in[3], d_out, (char*)d_ws);
}
```

```cpp
#define CODEPF
#define CODEPF_BACK 256
#define CODEPF_N2 40
#define TLBPF
#include <hip/hip_runtime.h>
#include <hip/hip_bf16.h>

#define LAS __attribute__((address_space(3)))
#define GAS __attribute__((address_space(1)))
typedef __bf16 bf16x2 __attribute__((ext_vector_type(2)));
typedef float f32x2 __attribute__((ext_vector_type(2)));
typedef float f32x4 __attribute__((ext_vector_type(4)));
typedef unsigned u32x4 __attribute__((ext_vector_type(4)));
typedef unsigned long long u64;

#define OUTAUX 0
constexpr int M_ = 2048, D_ = 640, F_ = 1728;
constexpr unsigned XROW = D_ * 2, WROW = F_ * 2;
constexpr int P1_SLOT = 49152;
constexpr int WD_SLOTS = 12, WD_SLOT = 8192, H_BASE = 98304, H_SLOTS = 5, H_SLOT = 12288;
constexpr int MISC = H_BASE + H_SLOTS * H_SLOT;
constexpr int LDS_BYTES = 160 * 1024;
constexpr size_t WS_FLAGS = 8u << 20;
constexpr u64 LCG_A = 6364136223846793005ull, LCG_C = 1442695040888963407ull;

#define WAIT_V(n) asm volatile("s_waitcnt vmcnt(" #n ")" ::: "memory")
#define WAIT_VI(n) asm volatile("s_waitcnt vmcnt(%0)" :: "i"(n) : "memory")
#define WAIT_L(n) asm volatile("s_waitcnt lgkmcnt(" #n ")" ::: "memory")
#define BAR() asm volatile("s_barrier" ::: "memory")
#define DI __device__ __forceinline__

#define ST(i)

#define FCLOB "v160","v161","v162","v163","v164","v165","v166","v167","v168","v169","v170","v171","v172","v173","v174","v175","v176","v177","v178","v179","v180","v181","v182","v183","v184","v185","v186","v187","v188","v189","v190","v191", \
  "v192","v193","v194","v195","v196","v197","v198","v199","v200","v201","v202","v203","v204","v205","v206","v207","v208","v209","v210","v211","v212","v213","v214","v215","v216","v217","v218","v219","v220","v221","v222","v223", \
  "v224","v225","v226","v227","v228","v229","v230","v231","v232","v233","v234","v235","v236","v237","v238","v239","v240","v241","v242","v243","v244","v245","v246","v247","v248","v249","v250","v251","v252","v253","v254","v255"
#define OWN_FRAGS() asm volatile("" ::: FCLOB)
constexpr int FR = 160;

DI unsigned rfl(unsigned v) { return __builtin_amdgcn_readfirstlane(v); }
DI u32x4 mksrd(const void* p, unsigned bytes) {
    const u64 a = (u64)p;
    return (u32x4){(unsigned)a, (unsigned)(a >> 32) & 0xffffu, bytes, 0x00020000u};
}
#define DMA_HEAD "s_mov_b32 m0, %0\n\ts_nop 0\n\t"
DI void dma4(unsigned dst, u32x4 r, unsigned soff, unsigned v0, unsigned v1, unsigned v2, unsigned v3) {
    asm volatile(DMA_HEAD "buffer_load_dwordx4 %3, %1, %2 offen lds\n\tbuffer_load_dwordx4 %4, %1, %2 offen offset:1024 lds\n\t"
                 "buffer_load_dwordx4 %5, %1, %2 offen offset:2048 lds\n\tbuffer_load_dwordx4 %6, %1, %2 offen offset:3072 lds"
                 :: "s"(dst), "s"(r), "s"(soff), "v"(v0), "v"(v1), "v"(v2), "v"(v3) : "memory", "m0");
}
DI void dma2(unsigned dst, u32x4 r, unsigned soff, unsigned v0, unsigned v1) {
    asm volatile(DMA_HEAD "buffer_load_dwordx4 %3, %1, %2 offen lds\n\tbuffer_load_dwordx4 %4, %1, %2 offen offset:1024 lds"
                 :: "s"(dst), "s"(r), "s"(soff), "v"(v0), "v"(v1) : "memory", "m0");
}
DI void dma22(unsigned dst, u32x4 ra, u32x4 rb, unsigned soff, unsigned v0, unsigned v1) {
    asm volatile(DMA_HEAD "buffer_load_dwordx4 %4, %1, %3 offen lds\n\tbuffer_load_dwordx4 %5, %1, %3 offen offset:1024 lds\n\t"
                 "buffer_load_dwordx4 %4, %2, %3 offen offset:2048 lds\n\tbuffer_load_dwordx4 %5, %2, %3 offen offset:3072 lds"
                 :: "s"(dst), "s"(ra), "s"(rb), "s"(soff), "v"(v0), "v"(v1) : "memory", "m0");
}
DI void dma4_sc1(unsigned dst, u32x4 r, unsigned soff, unsigned v0, unsigned v1, unsigned v2, unsigned v3) {
    asm volatile(DMA_HEAD "buffer_load_dwordx4 %3, %1, %2 offen sc1 lds\n\tbuffer_load_dwordx4 %4, %1, %2 offen offset:1024 sc1 lds\n\t"
                 "buffer_load_dwordx4 %5, %1, %2 offen offset:2048 sc1 lds\n\tbuffer_load_dwordx4 %6, %1, %2 offen offset:3072 sc1 lds"
                 :: "s"(dst), "s"(r), "s"(soff), "v"(v0), "v"(v1), "v"(v2), "v"(v3) : "memory", "m0");
}
DI void dma2_sc1(unsigned dst, u32x4 r, unsigned soff, unsigned v0, unsigned v1) {
    asm volatile(DMA_HEAD "buffer_load_dwordx4 %3, %1, %2 offen sc1 lds\n\tbuffer_load_dwordx4 %4, %1, %2 offen offset:1024 sc1 lds"
                 :: "s"(dst), "s"(r), "s"(soff), "v"(v0), "v"(v1) : "memory", "m0");
}
template <int R, int OFF> DI void rd128(unsigned addr) { asm volatile("ds_read_b128 v[%c1:%c2], %0 offset:%c3" :: "v"(addr), "i"(FR + R), "i"(FR + R + 3), "i"(OFF)); }
template <int R, int OFF> DI void rdtr(unsigned addr) { asm volatile("ds_read_b64_tr_b16 v[%c1:%c2], %0 offset:%c3" :: "v"(addr), "i"(FR + R), "i"(FR + R + 1), "i"(OFF)); }
template <int RA, int RB> DI void mfma(f32x4& acc) {
    asm volatile("v_mfma_f32_16x16x32_bf16 %0, v[%c1:%c2], v[%c3:%c4], %0" : "+v"(acc) : "i"(FR + RA), "i"(FR + RA + 3), "i"(FR + RB), "i"(FR + RB + 3));
}
DI unsigned pk(float a, float b) { bf16x2 v = {(__bf16)a, (__bf16)b}; return __builtin_bit_cast(unsigned, v); }
DI unsigned geglu2(f32x2 g, f32x2 u) {
    const f32x2 c1 = {-2.302208198f, -2.302208198f}, c2 = {-0.1029432397f, -0.1029432397f};
    const f32x2 a = g * __builtin_elementwise_fma(g * g, c2, c1);
    const f32x2 d = (f32x2){__builtin_amdgcn_exp2f(a.x), __builtin_amdgcn_exp2f(a.y)} + 1.0f;
    const f32x2 r = (g * u) * (f32x2){__builtin_amdgcn_rcpf(d.x), __builtin_amdgcn_rcpf(d.y)};
    return pk(r.x, r.y);
}

template <int S, int KS, int I> DI void p1_read(unsigned ax, unsigned aw0, unsigned aw1) {
    constexpr int B = 48 * S;
    if constexpr (I < 8) { constexpr int n = I >> 2, up = (I >> 1) & 1, t = I & 1;
        rdtr<B + 32 + 8 * up + 4 * n + 2 * t, 32768 + KS * 8192 + up * 2048 + t * 512>(n ? aw1 : aw0); }
    else rd128<B + 4 * (I - 8), (I - 8) * 2048>(ax);
}
template <int S, int I> DI void p1_mfma(f32x4 (&ag)[8][2], f32x4 (&au)[8][2]) {
    constexpr int B = 48 * S, m = I >> 2, n = (I >> 1) & 1, up = I & 1;
    if constexpr (up) mfma<B + 40 + 4 * n, B + 4 * m>(au[m][n]); else mfma<B + 32 + 4 * n, B + 4 * m>(ag[m][n]);
}
template <int SR, int KS, int I> struct P1Stage {
    static DI void run(f32x4 (&ag)[8][2], f32x4 (&au)[8][2], unsigned ax, unsigned aw0, unsigned aw1) {
        p1_mfma<SR ^ 1, I>(ag, au);
        if constexpr (I % 3 != 2 && (I / 3) * 2 + I % 3 < 16) p1_read<SR, KS, (I / 3) * 2 + I % 3>(ax, aw0, aw1);
        if constexpr (I + 1 < 32) P1Stage<SR, KS, I + 1>::run(ag, au, ax, aw0, aw1);
    }
};
template <int S, int KS, int I> DI void p1_reads_only(unsigned ax, unsigned aw0, unsigned aw1) {
    p1_read<S, KS, I>(ax, aw0, aw1);
    if constexpr (I + 1 < 16) p1_reads_only<S, KS, I + 1>(ax, aw0, aw1);
}

template <int S, int I> DI void p2_read(unsigned ah0, unsigned ah1, unsigned aw0, unsigned aw1) {
    constexpr int ks = I / 7, r = I % 7, B = 40 * S + 20 * ks;
    if constexpr (r < 4) { constexpr int n = r >> 1, t = r & 1; rdtr<B + 12 + 4 * n + 2 * t, ks * 4096 + t * 512>(n ? aw1 : aw0); }
    else rd128<B + 4 * (r - 4), (r - 4) * 2048>(ks ? ah1 : ah0);
}
template <int S, int I> DI void p2_mfma(f32x4 (&acc)[3][2]) {
    constexpr int ks = I / 6, m = (I % 6) >> 1, n = I & 1, B = 40 * S + 20 * ks;
    mfma<B + 12 + 4 * n, B + 4 * m>(acc[m][n]);
}
template <int SR, bool MM, int I> struct P2Stage {
    static DI void run(f32x4 (&acc)[3][2], unsigned ah0, unsigned ah1, unsigned aw0, unsigned aw1) {
        if constexpr (MM) p2_mfma<SR ^ 1, I>(acc);
        if constexpr (I < 7) { p2_read<SR, 2 * I>(ah0, ah1, aw0, aw1); p2_read<SR, 2 * I + 1>(ah0, ah1, aw0, aw1); }
        if constexpr (I + 1 < 12) P2Stage<SR, MM, I + 1>::run(acc, ah0, ah1, aw0, aw1);
    }
};
template <int S, int I> DI void p2_mfma_only(f32x4 (&acc)[3][2]) { p2_mfma<S, I>(acc); if constexpr (I + 1 < 12) p2_mfma_only<S, I + 1>(acc); }

template <int NH> DI void h_stream(u32x4 rh, unsigned so, unsigned lh0, int tr, unsigned v0, unsigned v1, unsigned v2, unsigned v3) {
    auto tile = [&](unsigned slot) {
        dma4_sc1(lh0 + slot, rh, so, v0, v1, v2, v3);
        if constexpr (NH == 6) dma2_sc1(lh0 + slot + 4096, rh, so + 32 * WROW, v0, v1);
        so += 128; if (++tr == 27) { tr = 0; so -= 27 * 128; }
    };
#pragma unroll 1
    for (int s = 0; s < H_SLOTS; ++s) tile(s * H_SLOT);
    WAIT_VI(4 * NH); BAR();
    WAIT_VI(3 * NH); BAR();
    unsigned hs = 0;
#pragma unroll 1
    for (int t = 2; t < 24; ++t) {
        WAIT_VI(2 * NH); BAR();
        tile(hs); hs = hs == (H_SLOTS - 1) * H_SLOT ? 0 : hs + H_SLOT;
    }
    WAIT_VI(2 * NH); BAR();
    WAIT_VI(NH); BAR();
    WAIT_V(0); BAR();
}

__global__ __launch_bounds__(512, 2) void geglu_mlp(const void* __restrict__ x, const void* __restrict__ wg, const void* __restrict__ wu,
                                                    const void* __restrict__ wd, void* __restrict__ out, char* __restrict__ ws) {
    extern __shared__ __attribute__((aligned(16))) char smem_[];
    LAS char* smem = (LAS char*)smem_;
    const unsigned lds0 = (unsigned)(size_t)smem;
    const int lane = threadIdx.x & 63, w = rfl(threadIdx.x >> 6);
    const int xcd = blockIdx.x & 7, j = blockIdx.x >> 3;
    const int fs = (xcd & 3) * 7 + (j >> 2), pm = (xcd >> 2) * 4 + (j & 3);
    const bool prod = j < 28 && fs < 27, cons = j < 30;
    const int mt = j / 10, nt = j - mt * 10;
    const int row2 = xcd * 256 + mt * 96, ntok = mt == 2 ? 64 : 96;
    const int rot = (nt * 27) / 10;
    GAS u64* flags = (GAS u64*)(ws + WS_FLAGS);
    GAS u64* epoch = flags + 256;
    void* h = ws;
    const int spare = j >= 28 ? xcd * 4 + (j - 28) : 32 + (xcd >> 2) * 4 + (j & 3);
    const int myslot = prod ? pm * 32 + fs : (spare / 5) * 32 + 27 + spare % 5;

    if (w < 4) {
        if (w == 0) ST(0);
        const u64 e0 = __hip_atomic_load(epoch, __ATOMIC_RELAXED, __HIP_MEMORY_SCOPE_AGENT);
        const int wm = w >> 1, wn = w & 1, q4 = lane >> 4;
        const int tq = (lane & 15) >> 2, tp = lane & 3;
        const unsigned tsw = (((tq >> 1) & 1) | ((q4 & 1) << 1)) << 1;
        const unsigned awc = (((wn * 4 + (tp >> 1)) ^ tsw) * 16) + 8 * (tp & 1);
        const unsigned xsw = (q4 ^ (lane & 6)) * 16;
        if (prod) {
            f32x4 ag[8][2], au[8][2];
#pragma unroll
            for (int m = 0; m < 8; ++m)
#pragma unroll
                for (int n = 0; n < 2; ++n) { ag[m][n] = (f32x4){0.f, 0.f, 0.f, 0.f}; au[m][n] = (f32x4){0.f, 0.f, 0.f, 0.f}; }
            if (threadIdx.x == 0) *(LAS unsigned*)(smem + MISC) = 0u;
            const unsigned axb = lds0 + (wm * 128 + (lane & 15)) * 128 + xsw;
            const unsigned awb = lds0 + (q4 >> 1) * 4096 + (8 * (q4 & 1) + tq) * 128 + awc;
            WAIT_L(0);
            unsigned sl = 2 * P1_SLOT;
            OWN_FRAGS();
            BAR();
            if (w == 0) ST(1);
            p1_reads_only<0, 0, 0>(axb + sl, awb + sl, (awb ^ 32) + sl);
            WAIT_L(0);
#pragma unroll 1
            for (int t = 0; t < 10; ++t) {
                OWN_FRAGS();
                P1Stage<1, 1, 0>::run(ag, au, (axb ^ 64) + sl, awb + sl, (awb ^ 32) + sl);
                WAIT_L(0);
                BAR();
                sl = sl == 2 * P1_SLOT ? 0 : sl + P1_SLOT;
                P1Stage<0, 0, 0>::run(ag, au, axb + sl, awb + sl, (awb ^ 32) + sl);
                WAIT_L(0);
            }
            asm volatile("s_nop 7\n\ts_nop 7" ::: "memory");
            __builtin_amdgcn_sched_barrier(0);
            if (w == 0) ST(2);
            const __amdgpu_buffer_rsrc_t rh = __builtin_amdgcn_make_buffer_rsrc(h, 0, 8u << 20, 0x00020000);
            const unsigned ho = (pm >> 1) * (2u << 20) + ((pm & 1) * 256 + wm * 128 + (lane & 15)) * WROW + (fs * 64 + wn * 32 + (q4 & 1) * 16 + (q4 >> 1) * 8) * 2;
#pragma unroll
            for (int m = 0; m < 8; ++m) {
                unsigned d[2][2];
#pragma unroll
                for (int n = 0; n < 2; ++n) {
                    d[n][0] = geglu2(ag[m][n].xy, au[m][n].xy);
                    d[n][1] = geglu2(ag[m][n].zw, au[m][n].zw);
                }
                const auto s0 = __builtin_amdgcn_permlane16_swap(d[0][0], d[1][0], false, false);
                const auto s1 = __builtin_amdgcn_permlane16_swap(d[0][1], d[1][1], false, false);
                const u32x4 v = {s0[0], s1[0], s0[1], s1[1]};
                __builtin_amdgcn_raw_buffer_store_b128(v, rh, ho + m * 16 * WROW, 0, 16);
            }
            if (w == 0) ST(3);
            WAIT_V(0);
            if (w == 0) ST(4);
            unsigned old = 0;
            if (lane == 0) old = __hip_atomic_fetch_add((LAS unsigned*)(smem + MISC), 1u, __ATOMIC_RELAXED, __HIP_MEMORY_SCOPE_WORKGROUP);
            if (rfl(old) == 3 && lane == 0) __hip_atomic_store(flags + myslot, e0 * LCG_A + LCG_C, __ATOMIC_RELAXED, __HIP_MEMORY_SCOPE_AGENT);
        } else if (w == 0 && lane == 0) {
            __hip_atomic_store(flags + myslot, e0 * LCG_A + LCG_C, __ATOMIC_RELAXED, __HIP_MEMORY_SCOPE_AGENT);
        }
        if (cons) {
            f32x4 acc[3][2];
#pragma unroll
            for (int m = 0; m < 3; ++m) { acc[m][0] = (f32x4){0.f, 0.f, 0.f, 0.f}; acc[m][1] = (f32x4){0.f, 0.f, 0.f, 0.f}; }
            const int tw = ntok >> 1;
            const unsigned ahb = lds0 + H_BASE + (wm * tw + (lane & 15)) * 128 + xsw;
            const unsigned awd = lds0 + (8 * q4 + tq) * 128 + awc;
            unsigned hs = 0, wsl = 0;
            OWN_FRAGS();
            BAR();
            if (w == 0) ST(8);
            P2Stage<0, false, 0>::run(acc, ahb, ahb ^ 64, awd, awd ^ 32);
#pragma unroll 1
            for (int t = 1; t < 27; t += 2) {
                OWN_FRAGS();
                hs = hs == (H_SLOTS - 1) * H_SLOT ? 0 : hs + H_SLOT; wsl = wsl == (WD_SLOTS - 1) * WD_SLOT ? 0 : wsl + WD_SLOT;
                { const unsigned a0 = ahb + hs, a1 = (ahb ^ 64) + hs, b0 = awd + wsl, b1 = (awd ^ 32) + wsl;
                  BAR(); WAIT_L(0);
                  P2Stage<1, true, 0>::run(acc, a0, a1, b0, b1); }
                hs = hs == (H_SLOTS - 1) * H_SLOT ? 0 : hs + H_SLOT; wsl = wsl == (WD_SLOTS - 1) * WD_SLOT ? 0 : wsl + WD_SLOT;
                { const unsigned a0 = ahb + hs, a1 = (ahb ^ 64) + hs, b0 = awd + wsl, b1 = (awd ^ 32) + wsl;
                  BAR(); WAIT_L(0);
                  P2Stage<0, true, 0>::run(acc, a0, a1, b0, b1); }
            }
            WAIT_L(0);
            p2_mfma_only<0, 0>(acc);
            asm volatile("s_nop 7\n\ts_nop 7" ::: "memory");
            __builtin_amdgcn_sched_barrier(0);
            if (w == 0) ST(9);
            const __amdgpu_buffer_rsrc_t ro = __builtin_amdgcn_make_buffer_rsrc(out, 0, M_ * D_ * 2, 0x00020000);
            const unsigned oo = (row2 + wm * tw + (lane & 15)) * XROW + (nt * 64 + wn * 32 + (q4 & 1) * 16 + (q4 >> 1) * 8) * 2;
#pragma unroll
            for (int m = 0; m < 3; ++m) {
                const auto s0 = __builtin_amdgcn_permlane16_swap(pk(acc[m][0][0], acc[m][0][1]), pk(acc[m][1][0], acc[m][1][1]), false, false);
                const auto s1 = __builtin_amdgcn_permlane16_swap(pk(acc[m][0][2], acc[m][0][3]), pk(acc[m][1][2], acc[m][1][3]), false, false);
                const u32x4 v = {s0[0], s1[0], s0[1], s1[1]};
                if (m * 16 < tw) __builtin_amdgcn_raw_buffer_store_b128(v, ro, oo + m * 16 * XROW, 0, OUTAUX);
            }
            if (w == 0) ST(10);
        }
    } else {
        const int lw = w - 4;
        unsigned cpf0 = 0, cpf1 = 0;
        if (lw == 3) {
            u64 pc; asm volatile("s_getpc_b64 %0" : "=s"(pc));
            const u64 base = pc & ~127ull;
            const char* a = (const char*)base - CODEPF_BACK + lane * 128;
            asm volatile("global_load_dword %0, %1, off" : "=&v"(cpf0) : "v"(a) : "memory");
            const char* b = a + 8192;
            if (lane < CODEPF_N2) asm volatile("global_load_dword %0, %1, off" : "=&v"(cpf1) : "v"(b) : "memory");
        }
        unsigned tpf = 0;
        if (lw == 2) {
            const char* p = lane == 0 ? (const char*)wd : lane == 1 ? (const char*)wd + (F_ * D_ * 2 - 4) : lane == 2 ? (const char*)out : lane == 3 ? (const char*)out + (M_ * D_ * 2 - 4)
                          : (const char*)ws + (size_t)(lane - 4) * (2u << 20) + (2u << 20) - 128;
            if (lane < 8) asm volatile("global_load_dword %0, %1, off" : "=&v"(tpf) : "v"(p) : "memory");
        }
        u64 e0;
        asm volatile("global_load_dwordx2 %0, %1, off sc1" : "=&v"(e0) : "v"(epoch) : "memory");
        const unsigned r8 = lane >> 3, c8 = lane & 7;
        const unsigned swx = (c8 ^ (r8 & 6)) * 16;
        const unsigned sww0 = (c8 ^ (((r8 >> 1) & 1) << 1)) * 16;
        const unsigned sww1 = (c8 ^ ((((r8 >> 1) & 1) | 2) << 1)) * 16;
        const u32x4 rwd = mksrd(wd, F_ * D_ * 2);
        constexpr unsigned DX = 8 * XROW - 1024, DW = 8 * WROW - 1024;
        const unsigned vd0 = r8 * XROW + sww0, vd1 = r8 * XROW + sww1 + DX, vd2 = vd0 + 2 * DX, vd3 = vd1 + 2 * DX;
        auto wd_batch = [&](int b) {
            int tr = 6 * b + rot; if (tr >= 27) tr -= 27;
            unsigned so = (tr * 64 + lw * 16) * XROW + nt * 128, ld = lds0 + 6 * b * WD_SLOT + lw * 2048;
#pragma unroll 1
            for (int t = 0; t < 6; ++t) {
                dma2(ld, rwd, so, vd0, vd1);
                ld += WD_SLOT; so += 64 * XROW; if (++tr == 27) { tr = 0; so -= 27 * 64 * XROW; }
            }
        };
        if (prod) {
            const u32x4 rx = mksrd(x, M_ * D_ * 2), rg = mksrd(wg, D_ * F_ * 2), ru = mksrd((const char*)wu - 2048, D_ * F_ * 2 + 2048);
            const unsigned vx0 = r8 * XROW + swx, vx1 = vx0 + DX, vx2 = vx0 + 2 * DX, vx3 = vx0 + 3 * DX;
            const unsigned vg0 = r8 * WROW + sww0, vg1 = r8 * WROW + sww1 + DW;
#define ROT1 1
            int kt = ROT1 ? ((j >> 2) + 3 * (j & 3)) % 10 : 0;
            unsigned sx = (pm * 256 + lw * 64) * XROW + kt * 128, sw = (kt * 64 + lw * 16) * WROW + fs * 128;
            auto stage = [&](unsigned slot) {
                const unsigned b = lds0 + slot;
                dma22(b + 32768 + lw * 4096, rg, ru, sw, vg0, vg1);
                dma4(b + lw * 8192, rx, sx, vx0, vx1, vx2, vx3);
                dma4(b + lw * 8192 + 4096, rx, sx + 32 * XROW, vx0, vx1, vx2, vx3);
                sx += 128; sw += 64 * WROW; if (++kt == 10) { kt = 0; sx -= 1280; sw -= 640 * WROW; }
            };
            stage(2 * P1_SLOT); stage(0); stage(P1_SLOT);
            WAIT_V(24);
            asm volatile("" :: "v"(cpf0), "v"(cpf1));
            asm volatile("" :: "v"(tpf));
            BAR();
            WAIT_V(12); BAR();
            unsigned sl = 2 * P1_SLOT;
#pragma unroll 1
            for (int t = 1; t < 8; ++t) {
                stage(sl);
                sl = sl == 2 * P1_SLOT ? 0 : sl + P1_SLOT;
                WAIT_V(12); BAR();
            }
            wd_batch(0);
            WAIT_V(12); BAR();
            wd_batch(1);
            BAR();
        } else {
            if (j >= 28) {
                unsigned dummy = 0;
                int g = (j - 28) * 4 + lw + xcd * 34; if (g >= 270) g -= 270;
#pragma unroll 1
                for (int i = (j - 28) * 4 + lw; i < 270; i += 16) {
                    const int kt = g / 10, sl = g - kt * 10;
                    asm volatile("buffer_load_dword %0, %1, %2, %3 offen" : "+v"(dummy) : "v"(lane * XROW), "s"(rwd), "s"((unsigned)(kt * 64 * XROW + sl * 128)) : "memory");
                    g += 16; if (g >= 270) g -= 270;
                }
                WAIT_V(0);
                asm volatile("" :: "v"(dummy));
            }
            if (cons) { wd_batch(0); wd_batch(1); }
            WAIT_V(0);
            asm volatile("" :: "v"(cpf0), "v"(cpf1));
            asm volatile("" :: "v"(tpf));
        }
        if (cons) {
            if (lw < 2) {
                WAIT_V(0);
                if (lw == 0) ST(11);
                int tr = WD_SLOTS + rot; if (tr >= 27) tr -= 27;
                unsigned so = (tr * 64 + lw * 32) * XROW + nt * 128, ld = 0;
                BAR(); BAR();
#pragma unroll 1
                for (int t = 2; t < 12; ++t) {
                    BAR();
                    dma4(lds0 + ld + lw * 4096, rwd, so, vd0, vd1, vd2, vd3);
                    ld += WD_SLOT; so += 64 * XROW; if (++tr == 27) { tr = 0; so -= 27 * 64 * XROW; }
                }
#pragma unroll 1
                for (int t = 12; t < 17; ++t) {
                    WAIT_V(36); BAR();
                    dma4(lds0 + ld + lw * 4096, rwd, so, vd0, vd1, vd2, vd3);
                    ld = ld == (WD_SLOTS - 1) * WD_SLOT ? 0 : ld + WD_SLOT; so += 64 * XROW; if (++tr == 27) { tr = 0; so -= 27 * 64 * XROW; }
                }
                WAIT_V(36); BAR();
                WAIT_V(32); BAR(); WAIT_V(28); BAR(); WAIT_V(24); BAR(); WAIT_V(20); BAR(); WAIT_V(16); BAR();
                WAIT_V(12); BAR(); WAIT_V(8); BAR(); WAIT_V(4); BAR(); WAIT_V(0); BAR();
            } else {
                WAIT_V(0);
                asm volatile("" : "+v"(e0));
                const u64 tag = e0 * LCG_A + LCG_C;
                GAS u64* pf = flags + xcd * 32 + (lane & 31);
                const u64 t0 = __builtin_amdgcn_s_memrealtime();
                for (;;) {
                    const u64 v = __hip_atomic_load(pf, __ATOMIC_RELAXED, __HIP_MEMORY_SCOPE_AGENT);
                    if (__all(v == tag)) break;
                    if (__builtin_amdgcn_s_memrealtime() - t0 > 5000000ull) break;
                    __builtin_amdgcn_s_sleep(1);
                }
                if (lw == 2) ST(5);
                const u32x4 rh = mksrd(h, 8u << 20);
                const unsigned v0 = r8 * WROW + swx, v1 = v0 + DW, v2 = v0 + 2 * DW, v3 = v0 + 3 * DW;
                const int hl = lw - 2;
                if (ntok == 96) h_stream<6>(rh, (xcd >> 1) * (2u << 20) + ((xcd & 1) * 256 + mt * 96 + hl * 48) * WROW + rot * 128, lds0 + H_BASE + hl * 6144, rot, v0, v1, v2, v3);
                else h_stream<4>(rh, (xcd >> 1) * (2u << 20) + ((xcd & 1) * 256 + mt * 96 + hl * 32) * WROW + rot * 128, lds0 + H_BASE + hl * 4096, rot, v0, v1, v2, v3);
            }
        }
        if (blockIdx.x == 0 && lw == 0) {
            WAIT_V(0);
            asm volatile("" : "+v"(e0));
            const u64 tag = e0 * LCG_A + LCG_C;
            const u64 t0 = __builtin_amdgcn_s_memrealtime();
            for (;;) {
                bool ok = true;
#pragma unroll
                for (int i = 0; i < 4; ++i) ok &= __hip_atomic_load(flags + i * 64 + lane, __ATOMIC_RELAXED, __HIP_MEMORY_SCOPE_AGENT) == tag;
                if (__all(ok)) break;
                if (__builtin_amdgcn_s_memrealtime() - t0 > 5000000ull) break;
                __builtin_amdgcn_s_sleep(1);
            }
            if (lane == 0) __hip_atomic_store(epoch, tag, __ATOMIC_RELAXED, __HIP_MEMORY_SCOPE_AGENT);
        }
    }
}

extern "C" void kernel_launch(void* const* d_in, const int* in_sizes, int n_in, void* d_out, int out_size, void* d_ws, size_t ws_size, hipStream_t stream) {
    (void)in_sizes; (void)n_in; (void)out_size; (void)ws_size;
    (void)hipFuncSetAttribute((const void*)geglu_mlp, hipFuncAttributeMaxDynamicSharedMemorySize, LDS_BYTES);
    geglu_mlp<<<256, 512, LDS_BYTES, stream>>>(d_in[0], d_in[1], d_in[2], d_in[3], d_out, (char*)d_ws);
}
```

```cpp
#define CODEPF
#define CODEPF_BACK 0
#define CODEPF_N2 60
#define TLBPF
#include <hip/hip_runtime.h>
#include <hip/hip_bf16.h>

#define LAS __attribute__((address_space(3)))
#define GAS __attribute__((address_space(1)))
typedef __bf16 bf16x2 __attribute__((ext_vector_type(2)));
typedef float f32x2 __attribute__((ext_vector_type(2)));
typedef float f32x4 __attribute__((ext_vector_type(4)));
typedef unsigned u32x4 __attribute__((ext_vector_type(4)));
typedef unsigned long long u64;

#define OUTAUX 0
#define HOFFF 0
constexpr int M_ = 2048, D_ = 640, F_ = 1728;
constexpr unsigned XROW = D_ * 2, WROW = F_ * 2;
constexpr int P1_SLOT = 49152;
constexpr int WD_SLOTS = 12, WD_SLOT = 8192, H_BASE = 98304, H_SLOTS = 5, H_SLOT = 12288;
constexpr int MISC = H_BASE + H_SLOTS * H_SLOT;
constexpr int LDS_BYTES = 160 * 1024;
constexpr size_t WS_FLAGS = 8u << 20;
#define PSLEEP 1
#define NREP 10
constexpr u64 LCG_A = 6364136223846793005ull, LCG_C = 1442695040888963407ull;

#define WAIT_V(n) asm volatile("s_waitcnt vmcnt(" #n ")" ::: "memory")
#define WAIT_VI(n) asm volatile("s_waitcnt vmcnt(%0)" :: "i"(n) : "memory")
#define WAIT_L(n) asm volatile("s_waitcnt lgkmcnt(" #n ")" ::: "memory")
#define BAR() asm volatile("s_barrier" ::: "memory")
#define DI __device__ __forceinline__

#define ST(i)

#define FCLOB "v160","v161","v162","v163","v164","v165","v166","v167","v168","v169","v170","v171","v172","v173","v174","v175","v176","v177","v178","v179","v180","v181","v182","v183","v184","v185","v186","v187","v188","v189","v190","v191", \
  "v192","v193","v194","v195","v196","v197","v198","v199","v200","v201","v202","v203","v204","v205","v206","v207","v208","v209","v210","v211","v212","v213","v214","v215","v216","v217","v218","v219","v220","v221","v222","v223", \
  "v224","v225","v226","v227","v228","v229","v230","v231","v232","v233","v234","v235","v236","v237","v238","v239","v240","v241","v242","v243","v244","v245","v246","v247","v248","v249","v250","v251","v252","v253","v254","v255"
#define OWN_FRAGS() asm volatile("" ::: FCLOB)
constexpr int FR = 160;

DI int lane_id() { int l; asm volatile("v_mbcnt_lo_u32_b32 %0, -1, 0\n\tv_mbcnt_hi_u32_b32 %0, -1, %0" : "=v"(l)); return l; }
DI unsigned rfl(unsigned v) { return __builtin_amdgcn_readfirstlane(v); }
DI u32x4 mksrd(const void* p, unsigned bytes) {
    const u64 a = (u64)p;
    return (u32x4){(unsigned)a, (unsigned)(a >> 32) & 0xffffu, bytes, 0x00020000u};
}
#define DMA_HEAD "s_mov_b32 m0, %0\n\ts_nop 0\n\t"
DI void dma4(unsigned dst, u32x4 r, unsigned soff, unsigned v0, unsigned v1, unsigned v2, unsigned v3) {
    asm volatile(DMA_HEAD "buffer_load_dwordx4 %3, %1, %2 offen lds\n\tbuffer_load_dwordx4 %4, %1, %2 offen offset:1024 lds\n\t"
                 "buffer_load_dwordx4 %5, %1, %2 offen offset:2048 lds\n\tbuffer_load_dwordx4 %6, %1, %2 offen offset:3072 lds"
                 :: "s"(dst), "s"(r), "s"(soff), "v"(v0), "v"(v1), "v"(v2), "v"(v3) : "memory", "m0");
}
DI void dma2(unsigned dst, u32x4 r, unsigned soff, unsigned v0, unsigned v1) {
    asm volatile(DMA_HEAD "buffer_load_dwordx4 %3, %1, %2 offen lds\n\tbuffer_load_dwordx4 %4, %1, %2 offen offset:1024 lds"
                 :: "s"(dst), "s"(r), "s"(soff), "v"(v0), "v"(v1) : "memory", "m0");
}
DI void dma22(unsigned dst, u32x4 ra, u32x4 rb, unsigned soff, unsigned v0, unsigned v1) {
    asm volatile(DMA_HEAD "buffer_load_dwordx4 %4, %1, %3 offen lds\n\tbuffer_load_dwordx4 %5, %1, %3 offen offset:1024 lds\n\t"
                 "buffer_load_dwordx4 %4, %2, %3 offen offset:2048 lds\n\tbuffer_load_dwordx4 %5, %2, %3 offen offset:3072 lds"
                 :: "s"(dst), "s"(ra), "s"(rb), "s"(soff), "v"(v0), "v"(v1) : "memory", "m0");
}
DI void dma4_sc1(unsigned dst, u32x4 r, unsigned soff, unsigned v0, unsigned v1, unsigned v2, unsigned v3) {
    asm volatile(DMA_HEAD "buffer_load_dwordx4 %3, %1, %2 offen sc1 lds\n\tbuffer_load_dwordx4 %4, %1, %2 offen offset:1024 sc1 lds\n\t"
                 "buffer_load_dwordx4 %5, %1, %2 offen offset:2048 sc1 lds\n\tbuffer_load_dwordx4 %6, %1, %2 offen offset:3072 sc1 lds"
                 :: "s"(dst), "s"(r), "s"(soff), "v"(v0), "v"(v1), "v"(v2), "v"(v3) : "memory", "m0");
}
DI void dma2_sc1(unsigned dst, u32x4 r, unsigned soff, unsigned v0, unsigned v1) {
    asm volatile(DMA_HEAD "buffer_load_dwordx4 %3, %1, %2 offen sc1 lds\n\tbuffer_load_dwordx4 %4, %1, %2 offen offset:1024 sc1 lds"
                 :: "s"(dst), "s"(r), "s"(soff), "v"(v0), "v"(v1) : "memory", "m0");
}
template <int R, int OFF> DI void rd128a(unsigned addr) { asm volatile("ds_read_b128 v[%c1:%c2], %0 offset:%c3" :: "v"(addr), "i"(R), "i"(R + 3), "i"(OFF)); }
template <int R, int OFF> DI void rdtra(unsigned addr) { asm volatile("ds_read_b64_tr_b16 v[%c1:%c2], %0 offset:%c3" :: "v"(addr), "i"(R), "i"(R + 1), "i"(OFF)); }
template <int RA, int RB> DI void mfmaa(f32x4& acc) {
    asm volatile("v_mfma_f32_16x16x32_bf16 %0, v[%c1:%c2], v[%c3:%c4], %0" : "+v"(acc) : "i"(RA), "i"(RA + 3), "i"(RB), "i"(RB + 3));
}
template <int R, int OFF> DI void rd128(unsigned addr) { asm volatile("ds_read_b128 v[%c1:%c2], %0 offset:%c3" :: "v"(addr), "i"(FR + R), "i"(FR + R + 3), "i"(OFF)); }
template <int R, int OFF> DI void rdtr(unsigned addr) { asm volatile("ds_read_b64_tr_b16 v[%c1:%c2], %0 offset:%c3" :: "v"(addr), "i"(FR + R), "i"(FR + R + 1), "i"(OFF)); }
template <int RA, int RB> DI void mfma(f32x4& acc) {
    asm volatile("v_mfma_f32_16x16x32_bf16 %0, v[%c1:%c2], v[%c3:%c4], %0" : "+v"(acc) : "i"(FR + RA), "i"(FR + RA + 3), "i"(FR + RB), "i"(FR + RB + 3));
}
DI unsigned pk(float a, float b) { bf16x2 v = {(__bf16)a, (__bf16)b}; return __builtin_bit_cast(unsigned, v); }
DI unsigned geglu2(f32x2 g, f32x2 u) {
    const f32x2 c1 = {-2.302208198f, -2.302208198f}, c2 = {-0.1029432397f, -0.1029432397f};
    const f32x2 a = g * __builtin_elementwise_fma(g * g, c2, c1);
    const f32x2 d = (f32x2){__builtin_amdgcn_exp2f(a.x), __builtin_amdgcn_exp2f(a.y)} + 1.0f;
    const f32x2 r = (g * u) * (f32x2){__builtin_amdgcn_rcpf(d.x), __builtin_amdgcn_rcpf(d.y)};
    return pk(r.x, r.y);
}

template <int S, int KS, int I> DI void p1_read(unsigned ax, unsigned aw0, unsigned aw1) {
    constexpr int B = 48 * S;
    if constexpr (I < 8) { constexpr int n = I >> 2, up = (I >> 1) & 1, t = I & 1;
        rdtr<B + 32 + 8 * up + 4 * n + 2 * t, 32768 + KS * 8192 + up * 2048 + t * 512>(n ? aw1 : aw0); }
    else rd128<B + 4 * (I - 8), (I - 8) * 2048>(ax);
}
template <int S, int I> DI void p1_mfma(f32x4 (&ag)[8][2], f32x4 (&au)[8][2]) {
    constexpr int B = 48 * S, m = I >> 2, n = (I >> 1) & 1, up = I & 1;
    if constexpr (up) mfma<B + 40 + 4 * n, B + 4 * m>(au[m][n]); else mfma<B + 32 + 4 * n, B + 4 * m>(ag[m][n]);
}
template <int SR, int KS, int I> struct P1Stage {
    static DI void run(f32x4 (&ag)[8][2], f32x4 (&au)[8][2], unsigned ax, unsigned aw0, unsigned aw1) {
        p1_mfma<SR ^ 1, I>(ag, au);
        if constexpr (I % 3 != 2 && (I / 3) * 2 + I % 3 < 16) p1_read<SR, KS, (I / 3) * 2 + I % 3>(ax, aw0, aw1);
        if constexpr (I + 1 < 32) P1Stage<SR, KS, I + 1>::run(ag, au, ax, aw0, aw1);
    }
};
template <int S, int KS, int I> DI void p1_reads_only(unsigned ax, unsigned aw0, unsigned aw1) {
    p1_read<S, KS, I>(ax, aw0, aw1);
    if constexpr (I + 1 < 16) p1_reads_only<S, KS, I + 1>(ax, aw0, aw1);
}

constexpr int WREG = 10, H0 = 48, W0 = 96;
struct P2A { unsigned h0, h1, w0, w1, w2, w3; };
template <int S, int I> DI void p2_rdh(const P2A& a) {
    constexpr int ks = I / 3, m = I % 3;
    rd128a<H0 + 24 * (S & 1) + 12 * ks + 4 * m, (S % H_SLOTS) * H_SLOT + m * 2048>(ks ? a.h1 : a.h0);
}
template <int S, int R, int I> DI void p2_rdw(const P2A& a) {
    constexpr int ks = I >> 2, n = (I >> 1) & 1, t = I & 1, sl = S % WD_SLOTS;
    rdtra<W0 + 16 * R + 8 * ks + 4 * n + 2 * t, (sl % 6) * WD_SLOT + ks * 4096 + t * 512>(sl < 6 ? (n ? a.w1 : a.w0) : (n ? a.w3 : a.w2));
}
template <int S, int I> DI void p2_mfma(f32x4 (&acc)[3][2]) {
    constexpr int ks = I / 6, m = (I % 6) >> 1, n = I & 1, R = S < WREG ? S : (S & 1);
    mfmaa<W0 + 16 * R + 8 * ks + 4 * n, H0 + 24 * (S & 1) + 12 * ks + 4 * m>(acc[m][n]);
}
template <int S, int I> DI void p2_read(const P2A& a) {
    if constexpr (I < 6) p2_rdh<S, I>(a);
    else p2_rdw<S, S & 1, I - 6>(a);
}
template <int S> constexpr int p2_nreads() { return S >= 27 ? 0 : S < WREG ? 6 : 14; }
template <int S, int I> DI void p2_body(f32x4 (&acc)[3][2], const P2A& a) {
    if constexpr (S > 0) p2_mfma<S - 1, I>(acc);
    constexpr int N = p2_nreads<S>();
    if constexpr (N == 6) { if constexpr (I < 6) p2_read<S, I>(a); }
    else if constexpr (N == 14) { if constexpr (I < 7) { p2_read<S, 2 * I>(a); p2_read<S, 2 * I + 1>(a); } }
    if constexpr (I + 1 < 12) p2_body<S, I + 1>(acc, a);
}
#define FCLOB2 "v48","v49","v50","v51","v52","v53","v54","v55","v56","v57","v58","v59","v60","v61","v62","v63","v64","v65","v66","v67","v68","v69","v70","v71","v72","v73","v74","v75","v76","v77","v78","v79", \
  "v80","v81","v82","v83","v84","v85","v86","v87","v88","v89","v90","v91","v92","v93","v94","v95","v96","v97","v98","v99","v100","v101","v102","v103","v104","v105","v106","v107","v108","v109","v110","v111", \
  "v112","v113","v114","v115","v116","v117","v118","v119","v120","v121","v122","v123","v124","v125","v126","v127","v128","v129","v130","v131","v132","v133","v134","v135","v136","v137","v138","v139","v140","v141","v142","v143", \
  "v144","v145","v146","v147","v148","v149","v150","v151","v152","v153","v154","v155","v156","v157","v158","v159", FCLOB
#define OWN_FRAGS2() asm volatile("" ::: FCLOB2)
template <int S> DI void p2_steps(f32x4 (&acc)[3][2], const P2A& a) {
    OWN_FRAGS2();
    if constexpr (S < 27) BAR();
    if constexpr (S > 0) WAIT_L(0);
    p2_body<S, 0>(acc, a);
    if constexpr (S < 26) { if constexpr (S < WREG) WAIT_L(0); else WAIT_L(8); }
    if constexpr (S < 27) p2_steps<S + 1>(acc, a);
}
template <int S, int I> DI void p2_wload(const P2A& a) {
    p2_rdw<S, S, I>(a);
    if constexpr (I + 1 < 8) p2_wload<S, I + 1>(a); else if constexpr (S + 1 < WREG) p2_wload<S + 1, 0>(a);
}

template <int NH> DI void h_stream(u32x4 rh, unsigned so, unsigned lh0, int tr, unsigned v0, unsigned v1, unsigned v2, unsigned v3) {
    auto tile = [&](unsigned slot) {
        dma4_sc1(lh0 + slot, rh, so, v0, v1, v2, v3);
        if constexpr (NH == 6) dma2_sc1(lh0 + slot + 4096, rh, so + 32 * WROW, v0, v1);
        so += 128; if (++tr == 27) { tr = 0; so -= 27 * 128; }
    };
    tile(0); tile(H_SLOT); tile(2 * H_SLOT);
    WAIT_VI(2 * NH); BAR();
    tile(3 * H_SLOT); tile(4 * H_SLOT);
    unsigned hs = 0;
#pragma unroll 1
    for (int t = 1; t < 23; ++t) {
        WAIT_VI(3 * NH); BAR();
        tile(hs); hs = hs == (H_SLOTS - 1) * H_SLOT ? 0 : hs + H_SLOT;
    }
    WAIT_VI(3 * NH); BAR();
    WAIT_VI(2 * NH); BAR();
    WAIT_VI(NH); BAR();
    WAIT_V(0); BAR();
}

__global__ __launch_bounds__(512, 2) void geglu_mlp(const void* __restrict__ x, const void* __restrict__ wg, const void* __restrict__ wu,
                                                    const void* __restrict__ wd, void* __restrict__ out, char* __restrict__ ws) {
    extern __shared__ __attribute__((aligned(16))) char smem_[];
    LAS char* smem = (LAS char*)smem_;
    const unsigned lds0 = (unsigned)(size_t)smem;
    const int lane = threadIdx.x & 63, w = rfl(threadIdx.x >> 6);
    const int xcd = blockIdx.x & 7, j = blockIdx.x >> 3;
    const int fs = (xcd & 3) * 7 + (j >> 2), pm = (xcd >> 2) * 4 + (j & 3);
    const bool prod = j < 28 && fs < 27, cons = j < 30;
    const int mt = j / 10, nt = j - mt * 10;
    const int row2 = xcd * 256 + mt * 96, ntok = mt == 2 ? 64 : 96;
    const int rot = (nt * 27) / 10;
    GAS u64* flags = (GAS u64*)(ws + ((size_t)HOFFF << 20) + WS_FLAGS);
    GAS u64* arrive = flags + 8 * NREP * 32;
    GAS u64* epoch = arrive + 256;
#define HOFF 0
    void* h = ws + ((size_t)HOFF << 20);
    if (w < 4) {
        if (w == 0) ST(0);
        const u64 e0v = __hip_atomic_load(epoch, __ATOMIC_RELAXED, __HIP_MEMORY_SCOPE_AGENT);
        const u64 tag = (((u64)rfl((unsigned)(e0v >> 32)) << 32) | rfl((unsigned)e0v)) * LCG_A + LCG_C;
        if (threadIdx.x == 0) __hip_atomic_store(arrive + blockIdx.x, tag, __ATOMIC_RELAXED, __HIP_MEMORY_SCOPE_AGENT);
        const int wm = w >> 1, wn = w & 1, q4 = lane >> 4;
        const int tq = (lane & 15) >> 2, tp = lane & 3;
        const unsigned tsw = (((tq >> 1) & 1) | ((q4 & 1) << 1)) << 1;
        const unsigned awc = (((wn * 4 + (tp >> 1)) ^ tsw) * 16) + 8 * (tp & 1);
        const unsigned xsw = (q4 ^ (lane & 6)) * 16;
        if (prod) {
            f32x4 ag[8][2], au[8][2];
#pragma unroll
            for (int m = 0; m < 8; ++m)
#pragma unroll
                for (int n = 0; n < 2; ++n) { ag[m][n] = (f32x4){0.f, 0.f, 0.f, 0.f}; au[m][n] = (f32x4){0.f, 0.f, 0.f, 0.f}; }
#pragma unroll
            for (int m = 0; m < 8; ++m) asm volatile("" : "+v"(ag[m][0]), "+v"(ag[m][1]), "+v"(au[m][0]), "+v"(au[m][1]));
            if (threadIdx.x == 0) *(LAS unsigned*)(smem + MISC) = 0u;
            const unsigned axb = lds0 + (wm * 128 + (lane & 15)) * 128 + xsw;
            const unsigned awb = lds0 + (q4 >> 1) * 4096 + (8 * (q4 & 1) + tq) * 128 + awc;
            WAIT_L(0);
            unsigned sl = 2 * P1_SLOT;
            OWN_FRAGS();
            BAR();
            if (w == 0) ST(1);
            p1_reads_only<0, 0, 0>(axb + sl, awb + sl, (awb ^ 32) + sl);
            WAIT_L(0);
#pragma unroll 1
            for (int t = 0; t < 10; ++t) {
                OWN_FRAGS();
                P1Stage<1, 1, 0>::run(ag, au, (axb ^ 64) + sl, awb + sl, (awb ^ 32) + sl);
                WAIT_L(0);
                BAR();
                sl = sl == 2 * P1_SLOT ? 0 : sl + P1_SLOT;
                P1Stage<0, 0, 0>::run(ag, au, axb + sl, awb + sl, (awb ^ 32) + sl);
                WAIT_L(0);
            }
            asm volatile("s_nop 7\n\ts_nop 7" ::: "memory");
            __builtin_amdgcn_sched_barrier(0);
            if (w == 0) ST(2);
            const __amdgpu_buffer_rsrc_t rh = __builtin_amdgcn_make_buffer_rsrc(h, 0, 8u << 20, 0x00020000);
            const int le = lane_id();
            const unsigned ho = (pm >> 1) * (2u << 20) + ((pm & 1) * 256 + wm * 128 + (le & 15)) * WROW + (fs * 64 + wn * 32 + ((le >> 4) & 1) * 16 + (le >> 5) * 8) * 2;
#pragma unroll
            for (int m = 0; m < 8; ++m) {
                unsigned d[2][2];
#pragma unroll
                for (int n = 0; n < 2; ++n) {
                    d[n][0] = geglu2(ag[m][n].xy, au[m][n].xy);
                    d[n][1] = geglu2(ag[m][n].zw, au[m][n].zw);
                }
                const auto s0 = __builtin_amdgcn_permlane16_swap(d[0][0], d[1][0], false, false);
                const auto s1 = __builtin_amdgcn_permlane16_swap(d[0][1], d[1][1], false, false);
                const u32x4 v = {s0[0], s1[0], s0[1], s1[1]};
                __builtin_amdgcn_raw_buffer_store_b128(v, rh, ho + m * 16 * WROW, 0, 16);
            }
            if (w == 0) ST(3);
        }
        const int tw = ntok >> 1;
        P2A a2;
        {
            const int l2 = lane_id(), g2 = l2 >> 4, r2 = (l2 & 15) >> 2, p2 = l2 & 3;
            const unsigned sw2 = (((r2 >> 1) & 1) | ((g2 & 1) << 1)) << 1;
            a2.h0 = lds0 + H_BASE + (wm * tw + (l2 & 15)) * 128 + (g2 ^ (l2 & 6)) * 16; a2.h1 = a2.h0 ^ 64;
            a2.w0 = lds0 + (8 * g2 + r2) * 128 + (((wn * 4 + (p2 >> 1)) ^ sw2) * 16) + 8 * (p2 & 1);
            a2.w1 = a2.w0 ^ 32; a2.w2 = a2.w0 + 6 * WD_SLOT; a2.w3 = a2.w1 + 6 * WD_SLOT;
        }
        if (cons) {
            OWN_FRAGS2();
            BAR();
            p2_wload<0, 0>(a2);
        }
        if (prod) {
            WAIT_V(0);
            if (w == 0) ST(4);
            const int lf = lane_id();
            unsigned old = 0;
            if (lf == 0) old = __hip_atomic_fetch_add((LAS unsigned*)(smem + MISC), 1u, __ATOMIC_RELAXED, __HIP_MEMORY_SCOPE_WORKGROUP);
            if (rfl(old) == 3 && lf < NREP) __hip_atomic_store(flags + (pm * NREP + lf) * 32 + fs, tag, __ATOMIC_RELAXED, __HIP_MEMORY_SCOPE_AGENT);
        }
        if (cons) {
            f32x4 acc[3][2];
#pragma unroll
            for (int m = 0; m < 3; ++m) { acc[m][0] = (f32x4){0.f, 0.f, 0.f, 0.f}; acc[m][1] = (f32x4){0.f, 0.f, 0.f, 0.f}; }
            asm volatile("" : "+v"(acc[0][0]), "+v"(acc[0][1]), "+v"(acc[1][0]), "+v"(acc[1][1]), "+v"(acc[2][0]), "+v"(acc[2][1]));
            OWN_FRAGS2();
            WAIT_L(0);
            BAR();
            if (w == 0) ST(8);
            p2_steps<0>(acc, a2);
            asm volatile("s_nop 7\n\ts_nop 7" ::: "memory");
            __builtin_amdgcn_sched_barrier(0);
            if (w == 0) ST(9);
            const __amdgpu_buffer_rsrc_t ro = __builtin_amdgcn_make_buffer_rsrc(out, 0, M_ * D_ * 2, 0x00020000);
            const int lo = lane_id();
            const unsigned oo = (row2 + wm * tw + (lo & 15)) * XROW + (nt * 64 + wn * 32 + ((lo >> 4) & 1) * 16 + (lo >> 5) * 8) * 2;
#pragma unroll
            for (int m = 0; m < 3; ++m) {
                const auto s0 = __builtin_amdgcn_permlane16_swap(pk(acc[m][0][0], acc[m][0][1]), pk(acc[m][1][0], acc[m][1][1]), false, false);
                const auto s1 = __builtin_amdgcn_permlane16_swap(pk(acc[m][0][2], acc[m][0][3]), pk(acc[m][1][2], acc[m][1][3]), false, false);
                const u32x4 v = {s0[0], s1[0], s0[1], s1[1]};
                if (m * 16 < tw) __builtin_amdgcn_raw_buffer_store_b128(v, ro, oo + m * 16 * XROW, 0, OUTAUX);
            }
            if (w == 0) ST(10);
        }
    } else {
        const int lw = w - 4;
        unsigned cpf0 = 0, cpf1 = 0;
        if (lw == 3) {
            u64 pc; asm volatile("s_getpc_b64 %0" : "=s"(pc));
            const char* a = (const char*)(pc & ~255ull) - CODEPF_BACK + lane * 128;
            asm volatile("global_load_dword %0, %1, off" : "+v"(cpf0) : "v"(a) : "memory");
            const char* b = a + 8192;
            if (lane < CODEPF_N2) asm volatile("global_load_dword %0, %1, off" : "+v"(cpf1) : "v"(b) : "memory");
        }
        unsigned tpf = 0;
        if (lw == 2) {
            const char* p = lane == 0 ? (const char*)wd : lane == 1 ? (const char*)wd + (F_ * D_ * 2 - 4) : lane == 2 ? (const char*)out : lane == 3 ? (const char*)out + (M_ * D_ * 2 - 4)
                          : (const char*)ws + (size_t)(lane - 4) * (2u << 20) + (2u << 20) - 128;
            if (lane < 8) asm volatile("global_load_dword %0, %1, off" : "=&v"(tpf) : "v"(p) : "memory");
        }
        u64 e0;
        asm volatile("global_load_dwordx2 %0, %1, off sc1" : "=&v"(e0) : "v"(epoch) : "memory");
        const unsigned r8 = lane >> 3, c8 = lane & 7;
        const unsigned swx = (c8 ^ (r8 & 6)) * 16;
        const unsigned sww0 = (c8 ^ (((r8 >> 1) & 1) << 1)) * 16;
        const unsigned sww1 = (c8 ^ ((((r8 >> 1) & 1) | 2) << 1)) * 16;
        const u32x4 rwd = mksrd(wd, F_ * D_ * 2);
        constexpr unsigned DX = 8 * XROW - 1024, DW = 8 * WROW - 1024;
        const unsigned vd0 = r8 * XROW + sww0, vd1 = r8 * XROW + sww1 + DX, vd2 = vd0 + 2 * DX, vd3 = vd1 + 2 * DX;
        auto wd_batch = [&](int b) {
            int tr = 6 * b + rot; if (tr >= 27) tr -= 27;
            unsigned so = (tr * 64 + lw * 16) * XROW + nt * 128, ld = lds0 + 6 * b * WD_SLOT + lw * 2048;
#pragma unroll 1
            for (int t = 0; t < 6; ++t) {
                dma2(ld, rwd, so, vd0, vd1);
                ld += WD_SLOT; so += 64 * XROW; if (++tr == 27) { tr = 0; so -= 27 * 64 * XROW; }
            }
        };
        if (prod) {
            const u32x4 rx = mksrd(x, M_ * D_ * 2), rg = mksrd(wg, D_ * F_ * 2), ru = mksrd((const char*)wu - 2048, D_ * F_ * 2 + 2048);
            const unsigned vx0 = r8 * XROW + swx, vx1 = vx0 + DX, vx2 = vx0 + 2 * DX, vx3 = vx0 + 3 * DX;
            const unsigned vg0 = r8 * WROW + sww0, vg1 = r8 * WROW + sww1 + DW;
#define ROT1 0
            int kt = ROT1 ? ((j >> 2) + 3 * (j & 3)) % 10 : 0;
            unsigned sx = (pm * 256 + lw * 64) * XROW + kt * 128, sw = (kt * 64 + lw * 16) * WROW + fs * 128;
            auto stage = [&](unsigned slot) {
                const unsigned b = lds0 + slot;
                dma22(b + 32768 + lw * 4096, rg, ru, sw, vg0, vg1);
                dma4(b + lw * 8192, rx, sx, vx0, vx1, vx2, vx3);
                dma4(b + lw * 8192 + 4096, rx, sx + 32 * XROW, vx0, vx1, vx2, vx3);
                sx += 128; sw += 64 * WROW; if (++kt == 10) { kt = 0; sx -= 1280; sw -= 640 * WROW; }
            };
            stage(2 * P1_SLOT); stage(0); stage(P1_SLOT);
            WAIT_V(24);
            asm volatile("" :: "v"(cpf0), "v"(cpf1));
            asm volatile("" :: "v"(tpf));
            BAR();
            WAIT_V(12); BAR();
            unsigned sl = 2 * P1_SLOT;
#pragma unroll 1
            for (int t = 1; t < 8; ++t) {
                stage(sl);
                sl = sl == 2 * P1_SLOT ? 0 : sl + P1_SLOT;
                WAIT_V(12); BAR();
            }
            wd_batch(0);
            WAIT_V(12); BAR();
            wd_batch(1);
            if (lw == 0) ST(6);
            BAR();
        } else {
            {
                const int sp = (j >= 28 ? xcd * 4 + (j - 28) : 32 + (xcd >> 2) * 4 + (j & 3)) * 4 + lw;
                const u32x4 sx_ = mksrd(x, M_ * D_ * 2), sg_ = mksrd(wg, D_ * F_ * 2), su_ = mksrd(wu, D_ * F_ * 2);
                unsigned dummy = 0;
#pragma unroll 1
                for (int g = sp; g < 9 * 86; g += 160) {
                    const int kt = g / 86 + 1, r = g % 86;
                    if (r < 32) asm volatile("buffer_load_dword %0, %1, %2, %3 offen" : "+v"(dummy) : "v"(lane * XROW), "s"(sx_), "s"((unsigned)(r * 64 * XROW + kt * 128)) : "memory");
                    else if (r < 59) asm volatile("buffer_load_dword %0, %1, %2, %3 offen" : "+v"(dummy) : "v"(lane * WROW), "s"(sg_), "s"((unsigned)(kt * 64 * WROW + (r - 32) * 128)) : "memory");
                    else asm volatile("buffer_load_dword %0, %1, %2, %3 offen" : "+v"(dummy) : "v"(lane * WROW), "s"(su_), "s"((unsigned)(kt * 64 * WROW + (r - 59) * 128)) : "memory");
                }
#pragma unroll 1
                for (int g = sp; g < 270; g += 160) {
                    const int kt = g / 10, sl = g - kt * 10;
                    asm volatile("buffer_load_dword %0, %1, %2, %3 offen" : "+v"(dummy) : "v"(lane * XROW), "s"(rwd), "s"((unsigned)(kt * 64 * XROW + sl * 128)) : "memory");
                }
                WAIT_V(0);
                asm volatile("" :: "v"(dummy));
            }
            if (cons) { wd_batch(0); wd_batch(1); }
            WAIT_V(0);
            asm volatile("" :: "v"(cpf0), "v"(cpf1));
            asm volatile("" :: "v"(tpf));
        }
        if (cons) {
            WAIT_V(0);
            if (lw == 0) ST(7);
            BAR();
            BAR();
            {
                int tr = WD_SLOTS + rot; if (tr >= 27) tr -= 27;
                unsigned so = (tr * 64 + lw * 16) * XROW + nt * 128, ld = lds0 + lw * 2048;
#pragma unroll 1
                for (int t = 0; t < WREG; ++t) {
                    dma2(ld, rwd, so, vd0, vd1);
                    ld += WD_SLOT; so += 64 * XROW; if (++tr == 27) { tr = 0; so -= 27 * 64 * XROW; }
                }
            }
            if (lw < 2) {
                if (lw == 0) ST(11);
                int tr = WD_SLOTS + WREG + rot; if (tr >= 27) tr -= 27;
                unsigned so = (tr * 64 + lw * 32) * XROW + nt * 128, ld = WREG * WD_SLOT;
                WAIT_V(0);
#pragma unroll 1
                for (int t = 0; t < 12; ++t) BAR();
#pragma unroll 1
                for (int t = 12; t < 17; ++t) {
                    BAR();
                    dma4(lds0 + ld + lw * 4096, rwd, so, vd0, vd1, vd2, vd3);
                    ld = ld == (WD_SLOTS - 1) * WD_SLOT ? 0 : ld + WD_SLOT; so += 64 * XROW; if (++tr == 27) { tr = 0; so -= 27 * 64 * XROW; }
                }
                BAR(); BAR(); BAR(); BAR(); BAR();
                WAIT_V(16); BAR(); WAIT_V(12); BAR(); WAIT_V(8); BAR(); WAIT_V(4); BAR(); WAIT_V(0); BAR();
            } else {
                WAIT_V(0);
                asm volatile("" : "+v"(e0));
                const u64 tag = e0 * LCG_A + LCG_C;
                GAS u64* pf = flags + (xcd * NREP + nt % NREP) * 32 + (lane < 27 ? lane : 0);
                const u64 t0 = __builtin_amdgcn_s_memrealtime();
                for (;;) {
                    const u64 v = __hip_atomic_load(pf, __ATOMIC_RELAXED, __HIP_MEMORY_SCOPE_AGENT);
                    if (__all(v == tag)) break;
                    if (__builtin_amdgcn_s_memrealtime() - t0 > 5000000ull) break;
                    __builtin_amdgcn_s_sleep(PSLEEP);
                }
                if (lw == 2) ST(5);
                const u32x4 rh = mksrd(h, 8u << 20);
                const unsigned v0 = r8 * WROW + swx, v1 = v0 + DW, v2 = v0 + 2 * DW, v3 = v0 + 3 * DW;
                const int hl = lw - 2;
                if (ntok == 96) h_stream<6>(rh, (xcd >> 1) * (2u << 20) + ((xcd & 1) * 256 + mt * 96 + hl * 48) * WROW + rot * 128, lds0 + H_BASE + hl * 6144, rot, v0, v1, v2, v3);
                else h_stream<4>(rh, (xcd >> 1) * (2u << 20) + ((xcd & 1) * 256 + mt * 96 + hl * 32) * WROW + rot * 128, lds0 + H_BASE + hl * 4096, rot, v0, v1, v2, v3);
            }
        }
        if (blockIdx.x == 0 && lw == 0) {
            WAIT_V(0);
            asm volatile("" : "+v"(e0));
            const u64 tag = e0 * LCG_A + LCG_C;
            const u64 t0 = __builtin_amdgcn_s_memrealtime();
            for (;;) {
                bool ok = true;
#pragma unroll
                for (int i = 0; i < 4; ++i) ok &= __hip_atomic_load(arrive + i * 64 + lane, __ATOMIC_RELAXED, __HIP_MEMORY_SCOPE_AGENT) == tag;
                if (__all(ok)) break;
                if (__builtin_amdgcn_s_memrealtime() - t0 > 5000000ull) break;
                __builtin_amdgcn_s_sleep(1);
            }
            if (lane == 0) __hip_atomic_store(epoch, tag, __ATOMIC_RELAXED, __HIP_MEMORY_SCOPE_AGENT);
        }
    }
}

extern "C" void kernel_launch(void* const* d_in, const int* in_sizes, int n_in, void* d_out, int out_size, void* d_ws, size_t ws_size, hipStream_t stream) {
    (void)in_sizes; (void)n_in; (void)out_size; (void)ws_size;
    (void)hipFuncSetAttribute((const void*)geglu_mlp, hipFuncAttributeMaxDynamicSharedMemorySize, LDS_BYTES);
    geglu_mlp<<<256, 512, LDS_BYTES, stream>>>(d_in[0], d_in[1], d_in[2], d_in[3], d_out, (char*)d_ws);
}
```

```cpp
#define CODEPF
#define CODEPF_BACK 0
#define CODEPF_N2 60
#define TLBPF
#define NREP 2
#include <hip/hip_runtime.h>
#include <hip/hip_bf16.h>

#define LAS __attribute__((address_space(3)))
#define GAS __attribute__((address_space(1)))
typedef __bf16 bf16x2 __attribute__((ext_vector_type(2)));
typedef float f32x2 __attribute__((ext_vector_type(2)));
typedef float f32x4 __attribute__((ext_vector_type(4)));
typedef unsigned u32x4 __attribute__((ext_vector_type(4)));
typedef unsigned long long u64;

#define OUTAUX 0
#define HOFFF 0
constexpr int M_ = 2048, D_ = 640, F_ = 1728;
constexpr unsigned XROW = D_ * 2, WROW = F_ * 2;
constexpr int P1_SLOT = 49152;
constexpr int WD_SLOTS = 12, WD_SLOT = 8192, H_BASE = 98304, H_SLOTS = 5, H_SLOT = 12288;
constexpr int MISC = H_BASE + H_SLOTS * H_SLOT;
constexpr int LDS_BYTES = 160 * 1024;
constexpr size_t WS_FLAGS = 8u << 20;
#define PSLEEP 1
constexpr u64 LCG_A = 6364136223846793005ull, LCG_C = 1442695040888963407ull;

#define WAIT_V(n) asm volatile("s_waitcnt vmcnt(" #n ")" ::: "memory")
#define WAIT_VI(n) asm volatile("s_waitcnt vmcnt(%0)" :: "i"(n) : "memory")
#define WAIT_L(n) asm volatile("s_waitcnt lgkmcnt(" #n ")" ::: "memory")
#define BAR() asm volatile("s_barrier" ::: "memory")
#define DI __device__ __forceinline__

#define ST(i)

#define FCLOB "v160","v161","v162","v163","v164","v165","v166","v167","v168","v169","v170","v171","v172","v173","v174","v175","v176","v177","v178","v179","v180","v181","v182","v183","v184","v185","v186","v187","v188","v189","v190","v191", \
  "v192","v193","v194","v195","v196","v197","v198","v199","v200","v201","v202","v203","v204","v205","v206","v207","v208","v209","v210","v211","v212","v213","v214","v215","v216","v217","v218","v219","v220","v221","v222","v223", \
  "v224","v225","v226","v227","v228","v229","v230","v231","v232","v233","v234","v235","v236","v237","v238","v239","v240","v241","v242","v243","v244","v245","v246","v247","v248","v249","v250","v251","v252","v253","v254","v255"
#define OWN_FRAGS() asm volatile("" ::: FCLOB)
constexpr int FR = 160;

DI int lane_id() { int l; asm volatile("v_mbcnt_lo_u32_b32 %0, -1, 0\n\tv_mbcnt_hi_u32_b32 %0, -1, %0" : "=v"(l)); return l; }
DI unsigned rfl(unsigned v) { return __builtin_amdgcn_readfirstlane(v); }
DI u32x4 mksrd(const void* p, unsigned bytes) {
    const u64 a = (u64)p;
    return (u32x4){(unsigned)a, (unsigned)(a >> 32) & 0xffffu, bytes, 0x00020000u};
}
#define DMA_HEAD "s_mov_b32 m0, %0\n\ts_nop 0\n\t"
DI void dma4(unsigned dst, u32x4 r, unsigned soff, unsigned v0, unsigned v1, unsigned v2, unsigned v3) {
    asm volatile(DMA_HEAD "buffer_load_dwordx4 %3, %1, %2 offen lds\n\tbuffer_load_dwordx4 %4, %1, %2 offen offset:1024 lds\n\t"
                 "buffer_load_dwordx4 %5, %1, %2 offen offset:2048 lds\n\tbuffer_load_dwordx4 %6, %1, %2 offen offset:3072 lds"
                 :: "s"(dst), "s"(r), "s"(soff), "v"(v0), "v"(v1), "v"(v2), "v"(v3) : "memory", "m0");
}
DI void dma2(unsigned dst, u32x4 r, unsigned soff, unsigned v0, unsigned v1) {
    asm volatile(DMA_HEAD "buffer_load_dwordx4 %3, %1, %2 offen lds\n\tbuffer_load_dwordx4 %4, %1, %2 offen offset:1024 lds"
                 :: "s"(dst), "s"(r), "s"(soff), "v"(v0), "v"(v1) : "memory", "m0");
}
DI void dma22(unsigned dst, u32x4 ra, u32x4 rb, unsigned soff, unsigned v0, unsigned v1) {
    asm volatile(DMA_HEAD "buffer_load_dwordx4 %4, %1, %3 offen lds\n\tbuffer_load_dwordx4 %5, %1, %3 offen offset:1024 lds\n\t"
                 "buffer_load_dwordx4 %4, %2, %3 offen offset:2048 lds\n\tbuffer_load_dwordx4 %5, %2, %3 offen offset:3072 lds"
                 :: "s"(dst), "s"(ra), "s"(rb), "s"(soff), "v"(v0), "v"(v1) : "memory", "m0");
}
DI void dma4_sc1(unsigned dst, u32x4 r, unsigned soff, unsigned v0, unsigned v1, unsigned v2, unsigned v3) {
    asm volatile(DMA_HEAD "buffer_load_dwordx4 %3, %1, %2 offen sc1 lds\n\tbuffer_load_dwordx4 %4, %1, %2 offen offset:1024 sc1 lds\n\t"
                 "buffer_load_dwordx4 %5, %1, %2 offen offset:2048 sc1 lds\n\tbuffer_load_dwordx4 %6, %1, %2 offen offset:3072 sc1 lds"
                 :: "s"(dst), "s"(r), "s"(soff), "v"(v0), "v"(v1), "v"(v2), "v"(v3) : "memory", "m0");
}
DI void dma2_sc1(unsigned dst, u32x4 r, unsigned soff, unsigned v0, unsigned v1) {
    asm volatile(DMA_HEAD "buffer_load_dwordx4 %3, %1, %2 offen sc1 lds\n\tbuffer_load_dwordx4 %4, %1, %2 offen offset:1024 sc1 lds"
                 :: "s"(dst), "s"(r), "s"(soff), "v"(v0), "v"(v1) : "memory", "m0");
}
template <int R, int OFF> DI void rd128a(unsigned addr) { asm volatile("ds_read_b128 v[%c1:%c2], %0 offset:%c3" :: "v"(addr), "i"(R), "i"(R + 3), "i"(OFF)); }
template <int R, int OFF> DI void rdtra(unsigned addr) { asm volatile("ds_read_b64_tr_b16 v[%c1:%c2], %0 offset:%c3" :: "v"(addr), "i"(R), "i"(R + 1), "i"(OFF)); }
template <int RA, int RB> DI void mfmaa(f32x4& acc) {
    asm volatile("v_mfma_f32_16x16x32_bf16 %0, v[%c1:%c2], v[%c3:%c4], %0" : "+v"(acc) : "i"(RA), "i"(RA + 3), "i"(RB), "i"(RB + 3));
}
template <int R, int OFF> DI void rd128(unsigned addr) { asm volatile("ds_read_b128 v[%c1:%c2], %0 offset:%c3" :: "v"(addr), "i"(FR + R), "i"(FR + R + 3), "i"(OFF)); }
template <int R, int OFF> DI void rdtr(unsigned addr) { asm volatile("ds_read_b64_tr_b16 v[%c1:%c2], %0 offset:%c3" :: "v"(addr), "i"(FR + R), "i"(FR + R + 1), "i"(OFF)); }
template <int RA, int RB> DI void mfma(f32x4& acc) {
    asm volatile("v_mfma_f32_16x16x32_bf16 %0, v[%c1:%c2], v[%c3:%c4], %0" : "+v"(acc) : "i"(FR + RA), "i"(FR + RA + 3), "i"(FR + RB), "i"(FR + RB + 3));
}
DI unsigned pk(float a, float b) { bf16x2 v = {(__bf16)a, (__bf16)b}; return __builtin_bit_cast(unsigned, v); }
DI unsigned geglu2(f32x2 g, f32x2 u) {
    const f32x2 c1 = {-2.302208198f, -2.302208198f}, c2 = {-0.1029432397f, -0.1029432397f};
    const f32x2 a = g * __builtin_elementwise_fma(g * g, c2, c1);
    const f32x2 d = (f32x2){__builtin_amdgcn_exp2f(a.x), __builtin_amdgcn_exp2f(a.y)} + 1.0f;
    const f32x2 r = (g * u) * (f32x2){__builtin_amdgcn_rcpf(d.x), __builtin_amdgcn_rcpf(d.y)};
    return pk(r.x, r.y);
}

template <int S, int KS, int I> DI void p1_read(unsigned ax, unsigned aw0, unsigned aw1) {
    constexpr int B = 48 * S;
    if constexpr (I < 8) { constexpr int n = I >> 2, up = (I >> 1) & 1, t = I & 1;
        rdtr<B + 32 + 8 * up + 4 * n + 2 * t, 32768 + KS * 8192 + up * 2048 + t * 512>(n ? aw1 : aw0); }
    else rd128<B + 4 * (I - 8), (I - 8) * 2048>(ax);
}
template <int S, int I> DI void p1_mfma(f32x4 (&ag)[8][2], f32x4 (&au)[8][2]) {
    constexpr int B = 48 * S, m = I >> 2, n = (I >> 1) & 1, up = I & 1;
    if constexpr (up) mfma<B + 40 + 4 * n, B + 4 * m>(au[m][n]); else mfma<B + 32 + 4 * n, B + 4 * m>(ag[m][n]);
}
template <int SR, int KS, int I> struct P1Stage {
    static DI void run(f32x4 (&ag)[8][2], f32x4 (&au)[8][2], unsigned ax, unsigned aw0, unsigned aw1) {
        p1_mfma<SR ^ 1, I>(ag, au);
        if constexpr (I % 3 != 2 && (I / 3) * 2 + I % 3 < 16) p1_read<SR, KS, (I / 3) * 2 + I % 3>(ax, aw0, aw1);
        if constexpr (I + 1 < 32) P1Stage<SR, KS, I + 1>::run(ag, au, ax, aw0, aw1);
    }
};
template <int S, int KS, int I> DI void p1_reads_only(unsigned ax, unsigned aw0, unsigned aw1) {
    p1_read<S, KS, I>(ax, aw0, aw1);
    if constexpr (I + 1 < 16) p1_reads_only<S, KS, I + 1>(ax, aw0, aw1);
}

constexpr int WREG = 10, H0 = 48, W0 = 96;
struct P2A { unsigned h0, h1, w0, w1, w2, w3; };
template <int S, int I> DI void p2_rdh(const P2A& a) {
    constexpr int ks = I / 3, m = I % 3;
    rd128a<H0 + 24 * (S & 1) + 12 * ks + 4 * m, (S % H_SLOTS) * H_SLOT + m * 2048>(ks ? a.h1 : a.h0);
}
template <int S, int R, int I> DI void p2_rdw(const P2A& a) {
    constexpr int ks = I >> 2, n = (I >> 1) & 1, t = I & 1, sl = S % WD_SLOTS;
    rdtra<W0 + 16 * R + 8 * ks + 4 * n + 2 * t, (sl % 6) * WD_SLOT + ks * 4096 + t * 512>(sl < 6 ? (n ? a.w1 : a.w0) : (n ? a.w3 : a.w2));
}
template <int S, int I> DI void p2_mfma(f32x4 (&acc)[3][2]) {
    constexpr int ks = I / 6, m = (I % 6) >> 1, n = I & 1, R = S < WREG ? S : (S & 1);
    mfmaa<W0 + 16 * R + 8 * ks + 4 * n, H0 + 24 * (S & 1) + 12 * ks + 4 * m>(acc[m][n]);
}
template <int S, int I> DI void p2_read(const P2A& a) {
    if constexpr (I < 6) p2_rdh<S, I>(a);
    else p2_rdw<S, S & 1, I - 6>(a);
}
template <int S> constexpr int p2_nreads() { return S >= 27 ? 0 : S < WREG ? 6 : 14; }
template <int S, int I> DI void p2_body(f32x4 (&acc)[3][2], const P2A& a) {
    if constexpr (S > 0) p2_mfma<S - 1, I>(acc);
    constexpr int N = p2_nreads<S>();
    if constexpr (N == 6) { if constexpr (I < 6) p2_read<S, I>(a); }
    else if constexpr (N == 14) { if constexpr (I < 7) { p2_read<S, 2 * I>(a); p2_read<S, 2 * I + 1>(a); } }
    if constexpr (I + 1 < 12) p2_body<S, I + 1>(acc, a);
}
#define FCLOB2 "v48","v49","v50","v51","v52","v53","v54","v55","v56","v57","v58","v59","v60","v61","v62","v63","v64","v65","v66","v67","v68","v69","v70","v71","v72","v73","v74","v75","v76","v77","v78","v79", \
  "v80","v81","v82","v83","v84","v85","v86","v87","v88","v89","v90","v91","v92","v93","v94","v95","v96","v97","v98","v99","v100","v101","v102","v103","v104","v105","v106","v107","v108","v109","v110","v111", \
  "v112","v113","v114","v115","v116","v117","v118","v119","v120","v121","v122","v123","v124","v125","v126","v127","v128","v129","v130","v131","v132","v133","v134","v135","v136","v137","v138","v139","v140","v141","v142","v143", \
  "v144","v145","v146","v147","v148","v149","v150","v151","v152","v153","v154","v155","v156","v157","v158","v159", FCLOB
#define OWN_FRAGS2() asm volatile("" ::: FCLOB2)
template <int S> DI void p2_steps(f32x4 (&acc)[3][2], const P2A& a) {
    OWN_FRAGS2();
    if constexpr (S < 27) BAR();
    if constexpr (S > 0) WAIT_L(0);
    p2_body<S, 0>(acc, a);
    if constexpr (S < 26) { if constexpr (S < WREG) WAIT_L(0); else WAIT_L(8); }
    if constexpr (S < 27) p2_steps<S + 1>(acc, a);
}
template <int S, int I> DI void p2_wload(const P2A& a) {
    p2_rdw<S, S, I>(a);
    if constexpr (I + 1 < 8) p2_wload<S, I + 1>(a); else if constexpr (S + 1 < WREG) p2_wload<S + 1, 0>(a);
}

template <int NH> DI void h_stream(u32x4 rh, unsigned so, unsigned lh0, int tr, unsigned v0, unsigned v1, unsigned v2, unsigned v3) {
    auto tile = [&](unsigned slot) {
        dma4_sc1(lh0 + slot, rh, so, v0, v1, v2, v3);
        if constexpr (NH == 6) dma2_sc1(lh0 + slot + 4096, rh, so + 32 * WROW, v0, v1);
        so += 128; if (++tr == 27) { tr = 0; so -= 27 * 128; }
    };
    tile(0); tile(H_SLOT); tile(2 * H_SLOT);
    WAIT_VI(2 * NH); BAR();
    tile(3 * H_SLOT); tile(4 * H_SLOT);
    unsigned hs = 0;
#pragma unroll 1
    for (int t = 1; t < 23; ++t) {
        WAIT_VI(3 * NH); BAR();
        tile(hs); hs = hs == (H_SLOTS - 1) * H_SLOT ? 0 : hs + H_SLOT;
    }
    WAIT_VI(3 * NH); BAR();
    WAIT_VI(2 * NH); BAR();
    WAIT_VI(NH); BAR();
    WAIT_V(0); BAR();
}

__global__ __launch_bounds__(512, 2) void geglu_mlp(const void* __restrict__ x, const void* __restrict__ wg, const void* __restrict__ wu,
                                                    const void* __restrict__ wd, void* __restrict__ out, char* __restrict__ ws) {
    extern __shared__ __attribute__((aligned(16))) char smem_[];
    LAS char* smem = (LAS char*)smem_;
    const unsigned lds0 = (unsigned)(size_t)smem;
    const int lane = threadIdx.x & 63, w = rfl(threadIdx.x >> 6);
    const int xcd = blockIdx.x & 7, j = blockIdx.x >> 3;
    const int fs = (xcd & 3) * 7 + (j >> 2), pm = (xcd >> 2) * 4 + (j & 3);
    const bool prod = j < 28 && fs < 27, cons = j < 30;
    const int mt = j / 10, nt = j - mt * 10;
    const int row2 = xcd * 256 + mt * 96, ntok = mt == 2 ? 64 : 96;
    const int rot = (nt * 27) / 10;
    GAS u64* flags = (GAS u64*)(ws + ((size_t)HOFFF << 20) + WS_FLAGS);
    GAS u64* arrive = flags + 8 * NREP * 32;
    GAS u64* epoch = arrive + 256;
#define HOFF 0
    void* h = ws + ((size_t)HOFF << 20);
    if (w < 4) {
        if (w == 0) ST(0);
        const u64 e0v = __hip_atomic_load(epoch, __ATOMIC_RELAXED, __HIP_MEMORY_SCOPE_AGENT);
        const u64 tag = (((u64)rfl((unsigned)(e0v >> 32)) << 32) | rfl((unsigned)e0v)) * LCG_A + LCG_C;
        if (threadIdx.x == 0) __hip_atomic_store(arrive + blockIdx.x, tag, __ATOMIC_RELAXED, __HIP_MEMORY_SCOPE_AGENT);
        const int wm = w >> 1, wn = w & 1, q4 = lane >> 4;
        const int tq = (lane & 15) >> 2, tp = lane & 3;
        const unsigned tsw = (((tq >> 1) & 1) | ((q4 & 1) << 1)) << 1;
        const unsigned awc = (((wn * 4 + (tp >> 1)) ^ tsw) * 16) + 8 * (tp & 1);
        const unsigned xsw = (q4 ^ (lane & 6)) * 16;
        if (prod) {
            f32x4 ag[8][2], au[8][2];
#pragma unroll
            for (int m = 0; m < 8; ++m)
#pragma unroll
                for (int n = 0; n < 2; ++n) { ag[m][n] = (f32x4){0.f, 0.f, 0.f, 0.f}; au[m][n] = (f32x4){0.f, 0.f, 0.f, 0.f}; }
#pragma unroll
            for (int m = 0; m < 8; ++m) asm volatile("" : "+v"(ag[m][0]), "+v"(ag[m][1]), "+v"(au[m][0]), "+v"(au[m][1]));
            if (threadIdx.x == 0) *(LAS unsigned*)(smem + MISC) = 0u;
            const unsigned axb = lds0 + (wm * 128 + (lane & 15)) * 128 + xsw;
            const unsigned awb = lds0 + (q4 >> 1) * 4096 + (8 * (q4 & 1) + tq) * 128 + awc;
            WAIT_L(0);
            unsigned sl = 2 * P1_SLOT;
            OWN_FRAGS();
            BAR();
            if (w == 0) ST(1);
            p1_reads_only<0, 0, 0>(axb + sl, awb + sl, (awb ^ 32) + sl);
            WAIT_L(0);
#pragma unroll 1
            for (int t = 0; t < 10; ++t) {
                OWN_FRAGS();
                P1Stage<1, 1, 0>::run(ag, au, (axb ^ 64) + sl, awb + sl, (awb ^ 32) + sl);
                WAIT_L(0);
                BAR();
                sl = sl == 2 * P1_SLOT ? 0 : sl + P1_SLOT;
                P1Stage<0, 0, 0>::run(ag, au, axb + sl, awb + sl, (awb ^ 32) + sl);
                WAIT_L(0);
            }
            asm volatile("s_nop 7\n\ts_nop 7" ::: "memory");
            __builtin_amdgcn_sched_barrier(0);
            if (w == 0) ST(2);
            const __amdgpu_buffer_rsrc_t rh = __builtin_amdgcn_make_buffer_rsrc(h, 0, 8u << 20, 0x00020000);
            const int le = lane_id();
            const unsigned ho = (pm >> 1) * (2u << 20) + ((pm & 1) * 256 + wm * 128 + (le & 15)) * WROW + (fs * 64 + wn * 32 + ((le >> 4) & 1) * 16 + (le >> 5) * 8) * 2;
#pragma unroll
            for (int m = 0; m < 8; ++m) {
                unsigned d[2][2];
#pragma unroll
                for (int n = 0; n < 2; ++n) {
                    d[n][0] = geglu2(ag[m][n].xy, au[m][n].xy);
                    d[n][1] = geglu2(ag[m][n].zw, au[m][n].zw);
                }
                const auto s0 = __builtin_amdgcn_permlane16_swap(d[0][0], d[1][0], false, false);
                const auto s1 = __builtin_amdgcn_permlane16_swap(d[0][1], d[1][1], false, false);
                const u32x4 v = {s0[0], s1[0], s0[1], s1[1]};
                __builtin_amdgcn_raw_buffer_store_b128(v, rh, ho + m * 16 * WROW, 0, 16);
            }
            if (w == 0) ST(3);
        }
        const int tw = ntok >> 1;
        P2A a2;
        {
            const int l2 = lane_id(), g2 = l2 >> 4, r2 = (l2 & 15) >> 2, p2 = l2 & 3;
            const unsigned sw2 = (((r2 >> 1) & 1) | ((g2 & 1) << 1)) << 1;
            a2.h0 = lds0 + H_BASE + (wm * tw + (l2 & 15)) * 128 + (g2 ^ (l2 & 6)) * 16; a2.h1 = a2.h0 ^ 64;
            a2.w0 = lds0 + (8 * g2 + r2) * 128 + (((wn * 4 + (p2 >> 1)) ^ sw2) * 16) + 8 * (p2 & 1);
            a2.w1 = a2.w0 ^ 32; a2.w2 = a2.w0 + 6 * WD_SLOT; a2.w3 = a2.w1 + 6 * WD_SLOT;
        }
        if (cons) {
            OWN_FRAGS2();
            BAR();
            p2_wload<0, 0>(a2);
        }
        if (prod) {
            WAIT_V(0);
            if (w == 0) ST(4);
            const int lf = lane_id();
            unsigned old = 0;
            if (lf == 0) old = __hip_atomic_fetch_add((LAS unsigned*)(smem + MISC), 1u, __ATOMIC_RELAXED, __HIP_MEMORY_SCOPE_WORKGROUP);
            if (rfl(old) == 3 && lf < NREP) __hip_atomic_store(flags + (pm * NREP + lf) * 32 + fs, tag, __ATOMIC_RELAXED, __HIP_MEMORY_SCOPE_AGENT);
        }
        if (cons) {
            f32x4 acc[3][2];
#pragma unroll
            for (int m = 0; m < 3; ++m) { acc[m][0] = (f32x4){0.f, 0.f, 0.f, 0.f}; acc[m][1] = (f32x4){0.f, 0.f, 0.f, 0.f}; }
            asm volatile("" : "+v"(acc[0][0]), "+v"(acc[0][1]), "+v"(acc[1][0]), "+v"(acc[1][1]), "+v"(acc[2][0]), "+v"(acc[2][1]));
            OWN_FRAGS2();
            WAIT_L(0);
            BAR();
            if (w == 0) ST(8);
            p2_steps<0>(acc, a2);
            asm volatile("s_nop 7\n\ts_nop 7" ::: "memory");
            __builtin_amdgcn_sched_barrier(0);
            if (w == 0) ST(9);
            const __amdgpu_buffer_rsrc_t ro = __builtin_amdgcn_make_buffer_rsrc(out, 0, M_ * D_ * 2, 0x00020000);
            const int lo = lane_id();
            const unsigned oo = (row2 + wm * tw + (lo & 15)) * XROW + (nt * 64 + wn * 32 + ((lo >> 4) & 1) * 16 + (lo >> 5) * 8) * 2;
#pragma unroll
            for (int m = 0; m < 3; ++m) {
                const auto s0 = __builtin_amdgcn_permlane16_swap(pk(acc[m][0][0], acc[m][0][1]), pk(acc[m][1][0], acc[m][1][1]), false, false);
                const auto s1 = __builtin_amdgcn_permlane16_swap(pk(acc[m][0][2], acc[m][0][3]), pk(acc[m][1][2], acc[m][1][3]), false, false);
                const u32x4 v = {s0[0], s1[0], s0[1], s1[1]};
                if (m * 16 < tw) __builtin_amdgcn_raw_buffer_store_b128(v, ro, oo + m * 16 * XROW, 0, OUTAUX);
            }
            if (w == 0) ST(10);
        }
    } else {
        const int lw = w - 4;
        unsigned pf0 = 0, pf1 = 0, pf2 = 0;
#define PFALL 0
        if (PFALL || !prod) {
            if (lw == 3) {
                u64 pc; asm volatile("s_getpc_b64 %0" : "=s"(pc));
                const char* a = (const char*)(pc & ~255ull) - CODEPF_BACK + lane * 128;
                asm volatile("global_load_dword %0, %1, off" : "+v"(pf0) : "v"(a) : "memory");
                const char* b = a + 8192;
                if (lane < CODEPF_N2) asm volatile("global_load_dword %0, %1, off" : "+v"(pf1) : "v"(b) : "memory");
            }
            if (lw == 2) {
                const char* p = lane == 0 ? (const char*)wd : lane == 1 ? (const char*)wd + (F_ * D_ * 2 - 4) : lane == 2 ? (const char*)out : lane == 3 ? (const char*)out + (M_ * D_ * 2 - 4)
                              : lane == 8 ? (const char*)wg + (F_ * D_ * 2 - 4) : lane == 9 ? (const char*)wu + (F_ * D_ * 2 - 4) : lane == 10 ? (const char*)x + (M_ * D_ * 2 - 4)
                              : (const char*)h + (size_t)((lane - 4) & 3) * (2u << 20) + (2u << 20) - 128;
                if (lane < 11) asm volatile("global_load_dword %0, %1, off" : "+v"(pf2) : "v"(p) : "memory");
            }
        }
        u64 e0;
        asm volatile("global_load_dwordx2 %0, %1, off sc1" : "=&v"(e0) : "v"(epoch) : "memory");
        const unsigned r8 = lane >> 3, c8 = lane & 7;
        const unsigned swx = (c8 ^ (r8 & 6)) * 16;
        const unsigned sww0 = (c8 ^ (((r8 >> 1) & 1) << 1)) * 16;
        const unsigned sww1 = (c8 ^ ((((r8 >> 1) & 1) | 2) << 1)) * 16;
        const u32x4 rwd = mksrd(wd, F_ * D_ * 2);
        constexpr unsigned DX = 8 * XROW - 1024, DW = 8 * WROW - 1024;
        const unsigned vd0 = r8 * XROW + sww0, vd1 = r8 * XROW + sww1 + DX, vd2 = vd0 + 2 * DX, vd3 = vd1 + 2 * DX;
        auto wd_batch = [&](int b) {
            int tr = 6 * b + rot; if (tr >= 27) tr -= 27;
            unsigned so = (tr * 64 + lw * 16) * XROW + nt * 128, ld = lds0 + 6 * b * WD_SLOT + lw * 2048;
#pragma unroll 1
            for (int t = 0; t < 6; ++t) {
                dma2(ld, rwd, so, vd0, vd1);
                ld += WD_SLOT; so += 64 * XROW; if (++tr == 27) { tr = 0; so -= 27 * 64 * XROW; }
            }
        };
        if (prod) {
            const u32x4 rx = mksrd(x, M_ * D_ * 2), rg = mksrd(wg, D_ * F_ * 2), ru = mksrd((const char*)wu - 2048, D_ * F_ * 2 + 2048);
            const unsigned vx0 = r8 * XROW + swx, vx1 = vx0 + DX, vx2 = vx0 + 2 * DX, vx3 = vx0 + 3 * DX;
            const unsigned vg0 = r8 * WROW + sww0, vg1 = r8 * WROW + sww1 + DW;
#define ROT1 0
            int kt = ROT1 ? ((j >> 2) + 3 * (j & 3)) % 10 : 0;
            unsigned sx = (pm * 256 + lw * 64) * XROW + kt * 128, sw = (kt * 64 + lw * 16) * WROW + fs * 128;
            auto stage = [&](unsigned slot) {
                const unsigned b = lds0 + slot;
                dma22(b + 32768 + lw * 4096, rg, ru, sw, vg0, vg1);
                dma4(b + lw * 8192, rx, sx, vx0, vx1, vx2, vx3);
                dma4(b + lw * 8192 + 4096, rx, sx + 32 * XROW, vx0, vx1, vx2, vx3);
                sx += 128; sw += 64 * WROW; if (++kt == 10) { kt = 0; sx -= 1280; sw -= 640 * WROW; }
            };
            stage(2 * P1_SLOT); stage(0); stage(P1_SLOT);
            WAIT_V(24);
            asm volatile("" :: "v"(pf0), "v"(pf1), "v"(pf2));
            BAR();
            WAIT_V(12); BAR();
            unsigned sl = 2 * P1_SLOT;
#pragma unroll 1
            for (int t = 1; t < 8; ++t) {
                stage(sl);
                sl = sl == 2 * P1_SLOT ? 0 : sl + P1_SLOT;
                WAIT_V(12); BAR();
            }
            wd_batch(0);
            WAIT_V(12); BAR();
            wd_batch(1);
            if (lw == 0) ST(6);
            BAR();
        } else {
            {
                const int sp = (j >= 28 ? xcd * 4 + (j - 28) : 32 + (xcd >> 2) * 4 + (j & 3)) * 4 + lw;
                const u32x4 sx_ = mksrd(x, M_ * D_ * 2), sg_ = mksrd(wg, D_ * F_ * 2), su_ = mksrd(wu, D_ * F_ * 2);
                unsigned dummy = 0;
#pragma unroll 1
                for (int g = sp; g < 9 * 86; g += 160) {
                    const int kt = g / 86 + 1, r = g % 86;
                    if (r < 32) asm volatile("buffer_load_dword %0, %1, %2, %3 offen" : "+v"(dummy) : "v"(lane * XROW), "s"(sx_), "s"((unsigned)(r * 64 * XROW + kt * 128)) : "memory");
                    else if (r < 59) asm volatile("buffer_load_dword %0, %1, %2, %3 offen" : "+v"(dummy) : "v"(lane * WROW), "s"(sg_), "s"((unsigned)(kt * 64 * WROW + (r - 32) * 128)) : "memory");
                    else asm volatile("buffer_load_dword %0, %1, %2, %3 offen" : "+v"(dummy) : "v"(lane * WROW), "s"(su_), "s"((unsigned)(kt * 64 * WROW + (r - 59) * 128)) : "memory");
                }
#pragma unroll 1
                for (int g = sp; g < 270; g += 160) {
                    const int kt = g / 10, sl = g - kt * 10;
                    asm volatile("buffer_load_dword %0, %1, %2, %3 offen" : "+v"(dummy) : "v"(lane * XROW), "s"(rwd), "s"((unsigned)(kt * 64 * XROW + sl * 128)) : "memory");
                }
                WAIT_V(0);
                asm volatile("" :: "v"(dummy));
            }
            if (cons) { wd_batch(0); wd_batch(1); }
            WAIT_V(0);
            asm volatile("" :: "v"(pf0), "v"(pf1), "v"(pf2));
        }
        if (cons) {
            WAIT_V(0);
            if (lw == 0) ST(7);
            BAR();
            BAR();
            {
                int tr = WD_SLOTS + rot; if (tr >= 27) tr -= 27;
                unsigned so = (tr * 64 + lw * 16) * XROW + nt * 128, ld = lds0 + lw * 2048;
#pragma unroll 1
                for (int t = 0; t < WREG; ++t) {
                    dma2(ld, rwd, so, vd0, vd1);
                    ld += WD_SLOT; so += 64 * XROW; if (++tr == 27) { tr = 0; so -= 27 * 64 * XROW; }
                }
            }
            if (lw < 2) {
                if (lw == 0) ST(11);
                int tr = WD_SLOTS + WREG + rot; if (tr >= 27) tr -= 27;
                unsigned so = (tr * 64 + lw * 32) * XROW + nt * 128, ld = WREG * WD_SLOT;
                WAIT_V(0);
#pragma unroll 1
                for (int t = 0; t < 12; ++t) BAR();
#pragma unroll 1
                for (int t = 12; t < 17; ++t) {
                    BAR();
                    dma4(lds0 + ld + lw * 4096, rwd, so, vd0, vd1, vd2, vd3);
                    ld = ld == (WD_SLOTS - 1) * WD_SLOT ? 0 : ld + WD_SLOT; so += 64 * XROW; if (++tr == 27) { tr = 0; so -= 27 * 64 * XROW; }
                }
                BAR(); BAR(); BAR(); BAR(); BAR();
                WAIT_V(16); BAR(); WAIT_V(12); BAR(); WAIT_V(8); BAR(); WAIT_V(4); BAR(); WAIT_V(0); BAR();
            } else {
                WAIT_V(0);
                asm volatile("" : "+v"(e0));
                const u64 tag = e0 * LCG_A + LCG_C;
                GAS u64* pf = flags + (xcd * NREP + nt % NREP) * 32 + (lane < 27 ? lane : 0);
                const u64 t0 = __builtin_amdgcn_s_memrealtime();
                for (;;) {
                    const u64 v = __hip_atomic_load(pf, __ATOMIC_RELAXED, __HIP_MEMORY_SCOPE_AGENT);
                    if (__all(v == tag)) break;
                    if (__builtin_amdgcn_s_memrealtime() - t0 > 5000000ull) break;
                    __builtin_amdgcn_s_sleep(PSLEEP);
                }
                if (lw == 2) ST(5);
                const u32x4 rh = mksrd(h, 8u << 20);
                const unsigned v0 = r8 * WROW + swx, v1 = v0 + DW, v2 = v0 + 2 * DW, v3 = v0 + 3 * DW;
                const int hl = lw - 2;
                if (ntok == 96) h_stream<6>(rh, (xcd >> 1) * (2u << 20) + ((xcd & 1) * 256 + mt * 96 + hl * 48) * WROW + rot * 128, lds0 + H_BASE + hl * 6144, rot, v0, v1, v2, v3);
                else h_stream<4>(rh, (xcd >> 1) * (2u << 20) + ((xcd & 1) * 256 + mt * 96 + hl * 32) * WROW + rot * 128, lds0 + H_BASE + hl * 4096, rot, v0, v1, v2, v3);
            }
        }
        if (blockIdx.x == 0 && lw == 0) {
            WAIT_V(0);
            asm volatile("" : "+v"(e0));
            const u64 tag = e0 * LCG_A + LCG_C;
            const u64 t0 = __builtin_amdgcn_s_memrealtime();
            for (;;) {
                bool ok = true;
#pragma unroll
                for (int i = 0; i < 4; ++i) ok &= __hip_atomic_load(arrive + i * 64 + lane, __ATOMIC_RELAXED, __HIP_MEMORY_SCOPE_AGENT) == tag;
                if (__all(ok)) break;
                if (__builtin_amdgcn_s_memrealtime() - t0 > 5000000ull) break;
                __builtin_amdgcn_s_sleep(1);
            }
            if (lane == 0) __hip_atomic_store(epoch, tag, __ATOMIC_RELAXED, __HIP_MEMORY_SCOPE_AGENT);
        }
    }
}

extern "C" void kernel_launch(void* const* d_in, const int* in_sizes, int n_in, void* d_out, int out_size, void* d_ws, size_t ws_size, hipStream_t stream) {
    (void)in_sizes; (void)n_in; (void)out_size; (void)ws_size;
    (void)hipFuncSetAttribute((const void*)geglu_mlp, hipFuncAttributeMaxDynamicSharedMemorySize, LDS_BYTES);
    geglu_mlp<<<256, 512, LDS_BYTES, stream>>>(d_in[0], d_in[1], d_in[2], d_in[3], d_out, (char*)d_ws);
}
```

```cpp
#define PROBE_A 3
#define PROBE_B 4
#define PROBE_BLK 9
#define CODEPF
#define CODEPF_BACK 0
#define CODEPF_N2 60
#define TLBPF
#define OUTAUX 16
#include <hip/hip_runtime.h>
#include <hip/hip_bf16.h>

#define LAS __attribute__((address_space(3)))
#define GAS __attribute__((address_space(1)))
typedef __bf16 bf16x2 __attribute__((ext_vector_type(2)));
typedef float f32x2 __attribute__((ext_vector_type(2)));
typedef float f32x4 __attribute__((ext_vector_type(4)));
typedef unsigned u32x4 __attribute__((ext_vector_type(4)));
typedef unsigned long long u64;

#define HOFFF 0
constexpr int M_ = 2048, D_ = 640, F_ = 1728;
constexpr unsigned XROW = D_ * 2, WROW = F_ * 2;
constexpr int P1_SLOT = 49152;
constexpr int WD_SLOTS = 12, WD_SLOT = 8192, H_BASE = 98304, H_SLOTS = 5, H_SLOT = 12288;
constexpr int MISC = H_BASE + H_SLOTS * H_SLOT;
constexpr int LDS_BYTES = 160 * 1024;
constexpr size_t WS_FLAGS = 8u << 20;
#define PSLEEP 1
#define NREP 10
constexpr u64 LCG_A = 6364136223846793005ull, LCG_C = 1442695040888963407ull;

#define WAIT_V(n) asm volatile("s_waitcnt vmcnt(" #n ")" ::: "memory")
#define WAIT_VI(n) asm volatile("s_waitcnt vmcnt(%0)" :: "i"(n) : "memory")
#define WAIT_L(n) asm volatile("s_waitcnt lgkmcnt(" #n ")" ::: "memory")
#define BAR() asm volatile("s_barrier" ::: "memory")
#define DI __device__ __forceinline__

#define ST(i) do { if ((i) == PROBE_A) tpa = __builtin_amdgcn_s_memrealtime(); if ((i) == PROBE_B) tpb = __builtin_amdgcn_s_memrealtime(); } while (0)

#define FCLOB "v160","v161","v162","v163","v164","v165","v166","v167","v168","v169","v170","v171","v172","v173","v174","v175","v176","v177","v178","v179","v180","v181","v182","v183","v184","v185","v186","v187","v188","v189","v190","v191", \
  "v192","v193","v194","v195","v196","v197","v198","v199","v200","v201","v202","v203","v204","v205","v206","v207","v208","v209","v210","v211","v212","v213","v214","v215","v216","v217","v218","v219","v220","v221","v222","v223", \
  "v224","v225","v226","v227","v228","v229","v230","v231","v232","v233","v234","v235","v236","v237","v238","v239","v240","v241","v242","v243","v244","v245","v246","v247","v248","v249","v250","v251","v252","v253","v254","v255"
#define OWN_FRAGS() asm volatile("" ::: FCLOB)
constexpr int FR = 160;

DI int lane_id() { int l; asm volatile("v_mbcnt_lo_u32_b32 %0, -1, 0\n\tv_mbcnt_hi_u32_b32 %0, -1, %0" : "=v"(l)); return l; }
DI unsigned rfl(unsigned v) { return __builtin_amdgcn_readfirstlane(v); }
DI u32x4 mksrd(const void* p, unsigned bytes) {
    const u64 a = (u64)p;
    return (u32x4){(unsigned)a, (unsigned)(a >> 32) & 0xffffu, bytes, 0x00020000u};
}
#define DMA_HEAD "s_mov_b32 m0, %0\n\ts_nop 0\n\t"
DI void dma4(unsigned dst, u32x4 r, unsigned soff, unsigned v0, unsigned v1, unsigned v2, unsigned v3) {
    asm volatile(DMA_HEAD "buffer_load_dwordx4 %3, %1, %2 offen lds\n\tbuffer_load_dwordx4 %4, %1, %2 offen offset:1024 lds\n\t"
                 "buffer_load_dwordx4 %5, %1, %2 offen offset:2048 lds\n\tbuffer_load_dwordx4 %6, %1, %2 offen offset:3072 lds"
                 :: "s"(dst), "s"(r), "s"(soff), "v"(v0), "v"(v1), "v"(v2), "v"(v3) : "memory", "m0");
}
DI void dma2(unsigned dst, u32x4 r, unsigned soff, unsigned v0, unsigned v1) {
    asm volatile(DMA_HEAD "buffer_load_dwordx4 %3, %1, %2 offen lds\n\tbuffer_load_dwordx4 %4, %1, %2 offen offset:1024 lds"
                 :: "s"(dst), "s"(r), "s"(soff), "v"(v0), "v"(v1) : "memory", "m0");
}
DI void dma22(unsigned dst, u32x4 ra, u32x4 rb, unsigned soff, unsigned v0, unsigned v1) {
    asm volatile(DMA_HEAD "buffer_load_dwordx4 %4, %1, %3 offen lds\n\tbuffer_load_dwordx4 %5, %1, %3 offen offset:1024 lds\n\t"
                 "buffer_load_dwordx4 %4, %2, %3 offen offset:2048 lds\n\tbuffer_load_dwordx4 %5, %2, %3 offen offset:3072 lds"
                 :: "s"(dst), "s"(ra), "s"(rb), "s"(soff), "v"(v0), "v"(v1) : "memory", "m0");
}
DI void dma4_sc1(unsigned dst, u32x4 r, unsigned soff, unsigned v0, unsigned v1, unsigned v2, unsigned v3) {
    asm volatile(DMA_HEAD "buffer_load_dwordx4 %3, %1, %2 offen sc1 lds\n\tbuffer_load_dwordx4 %4, %1, %2 offen offset:1024 sc1 lds\n\t"
                 "buffer_load_dwordx4 %5, %1, %2 offen offset:2048 sc1 lds\n\tbuffer_load_dwordx4 %6, %1, %2 offen offset:3072 sc1 lds"
                 :: "s"(dst), "s"(r), "s"(soff), "v"(v0), "v"(v1), "v"(v2), "v"(v3) : "memory", "m0");
}
DI void dma2_sc1(unsigned dst, u32x4 r, unsigned soff, unsigned v0, unsigned v1) {
    asm volatile(DMA_HEAD "buffer_load_dwordx4 %3, %1, %2 offen sc1 lds\n\tbuffer_load_dwordx4 %4, %1, %2 offen offset:1024 sc1 lds"
                 :: "s"(dst), "s"(r), "s"(soff), "v"(v0), "v"(v1) : "memory", "m0");
}
template <int R, int OFF> DI void rd128a(unsigned addr) { asm volatile("ds_read_b128 v[%c1:%c2], %0 offset:%c3" :: "v"(addr), "i"(R), "i"(R + 3), "i"(OFF)); }
template <int R, int OFF> DI void rdtra(unsigned addr) { asm volatile("ds_read_b64_tr_b16 v[%c1:%c2], %0 offset:%c3" :: "v"(addr), "i"(R), "i"(R + 1), "i"(OFF)); }
template <int RA, int RB> DI void mfmaa(f32x4& acc) {
    asm volatile("v_mfma_f32_16x16x32_bf16 %0, v[%c1:%c2], v[%c3:%c4], %0" : "+v"(acc) : "i"(RA), "i"(RA + 3), "i"(RB), "i"(RB + 3));
}
template <int R, int OFF> DI void rd128(unsigned addr) { asm volatile("ds_read_b128 v[%c1:%c2], %0 offset:%c3" :: "v"(addr), "i"(FR + R), "i"(FR + R + 3), "i"(OFF)); }
template <int R, int OFF> DI void rdtr(unsigned addr) { asm volatile("ds_read_b64_tr_b16 v[%c1:%c2], %0 offset:%c3" :: "v"(addr), "i"(FR + R), "i"(FR + R + 1), "i"(OFF)); }
template <int RA, int RB> DI void mfma(f32x4& acc) {
    asm volatile("v_mfma_f32_16x16x32_bf16 %0, v[%c1:%c2], v[%c3:%c4], %0" : "+v"(acc) : "i"(FR + RA), "i"(FR + RA + 3), "i"(FR + RB), "i"(FR + RB + 3));
}
DI unsigned pk(float a, float b) { bf16x2 v = {(__bf16)a, (__bf16)b}; return __builtin_bit_cast(unsigned, v); }
DI unsigned geglu2(f32x2 g, f32x2 u) {
    const f32x2 c1 = {-2.302208198f, -2.302208198f}, c2 = {-0.1029432397f, -0.1029432397f};
    const f32x2 a = g * __builtin_elementwise_fma(g * g, c2, c1);
    const f32x2 d = (f32x2){__builtin_amdgcn_exp2f(a.x), __builtin_amdgcn_exp2f(a.y)} + 1.0f;
    const f32x2 r = (g * u) * (f32x2){__builtin_amdgcn_rcpf(d.x), __builtin_amdgcn_rcpf(d.y)};
    return pk(r.x, r.y);
}

template <int S, int KS, int I> DI void p1_read(unsigned ax, unsigned aw0, unsigned aw1) {
    constexpr int B = 48 * S;
    if constexpr (I < 8) { constexpr int n = I >> 2, up = (I >> 1) & 1, t = I & 1;
        rdtr<B + 32 + 8 * up + 4 * n + 2 * t, 32768 + KS * 8192 + up * 2048 + t * 512>(n ? aw1 : aw0); }
    else rd128<B + 4 * (I - 8), (I - 8) * 2048>(ax);
}
template <int S, int I> DI void p1_mfma(f32x4 (&ag)[8][2], f32x4 (&au)[8][2]) {
    constexpr int B = 48 * S, m = I >> 2, n = (I >> 1) & 1, up = I & 1;
    if constexpr (up) mfma<B + 40 + 4 * n, B + 4 * m>(au[m][n]); else mfma<B + 32 + 4 * n, B + 4 * m>(ag[m][n]);
}
template <int SR, int KS, int I> struct P1Stage {
    static DI void run(f32x4 (&ag)[8][2], f32x4 (&au)[8][2], unsigned ax, unsigned aw0, unsigned aw1) {
        p1_mfma<SR ^ 1, I>(ag, au);
        if constexpr (I % 3 != 2 && (I / 3) * 2 + I % 3 < 16) p1_read<SR, KS, (I / 3) * 2 + I % 3>(ax, aw0, aw1);
        if constexpr (I + 1 < 32) P1Stage<SR, KS, I + 1>::run(ag, au, ax, aw0, aw1);
    }
};
template <int S, int KS, int I> DI void p1_reads_only(unsigned ax, unsigned aw0, unsigned aw1) {
    p1_read<S, KS, I>(ax, aw0, aw1);
    if constexpr (I + 1 < 16) p1_reads_only<S, KS, I + 1>(ax, aw0, aw1);
}

constexpr int WREG = 10, H0 = 48, W0 = 96;
struct P2A { unsigned h0, h1, w0, w1, w2, w3; };
template <int S, int I> DI void p2_rdh(const P2A& a) {
    constexpr int ks = I / 3, m = I % 3;
    rd128a<H0 + 24 * (S & 1) + 12 * ks + 4 * m, (S % H_SLOTS) * H_SLOT + m * 2048>(ks ? a.h1 : a.h0);
}
template <int S, int R, int I> DI void p2_rdw(const P2A& a) {
    constexpr int ks = I >> 2, n = (I >> 1) & 1, t = I & 1, sl = S % WD_SLOTS;
    rdtra<W0 + 16 * R + 8 * ks + 4 * n + 2 * t, (sl % 6) * WD_SLOT + ks * 4096 + t * 512>(sl < 6 ? (n ? a.w1 : a.w0) : (n ? a.w3 : a.w2));
}
template <int S, int I> DI void p2_mfma(f32x4 (&acc)[3][2]) {
    constexpr int ks = I / 6, m = (I % 6) >> 1, n = I & 1, R = S < WREG ? S : (S & 1);
    mfmaa<W0 + 16 * R + 8 * ks + 4 * n, H0 + 24 * (S & 1) + 12 * ks + 4 * m>(acc[m][n]);
}
template <int S, int I> DI void p2_read(const P2A& a) {
    if constexpr (I < 6) p2_rdh<S, I>(a);
    else p2_rdw<S, S & 1, I - 6>(a);
}
template <int S> constexpr int p2_nreads() { return S >= 27 ? 0 : S < WREG ? 6 : 14; }
template <int S, int I> DI void p2_body(f32x4 (&acc)[3][2], const P2A& a) {
    if constexpr (S > 0) p2_mfma<S - 1, I>(acc);
    constexpr int N = p2_nreads<S>();
    if constexpr (N == 6) { if constexpr (I < 6) p2_read<S, I>(a); }
    else if constexpr (N == 14) { if constexpr (I < 7) { p2_read<S, 2 * I>(a); p2_read<S, 2 * I + 1>(a); } }
    if constexpr (I + 1 < 12) p2_body<S, I + 1>(acc, a);
}
#define FCLOB2 "v48","v49","v50","v51","v52","v53","v54","v55","v56","v57","v58","v59","v60","v61","v62","v63","v64","v65","v66","v67","v68","v69","v70","v71","v72","v73","v74","v75","v76","v77","v78","v79", \
  "v80","v81","v82","v83","v84","v85","v86","v87","v88","v89","v90","v91","v92","v93","v94","v95","v96","v97","v98","v99","v100","v101","v102","v103","v104","v105","v106","v107","v108","v109","v110","v111", \
  "v112","v113","v114","v115","v116","v117","v118","v119","v120","v121","v122","v123","v124","v125","v126","v127","v128","v129","v130","v131","v132","v133","v134","v135","v136","v137","v138","v139","v140","v141","v142","v143", \
  "v144","v145","v146","v147","v148","v149","v150","v151","v152","v153","v154","v155","v156","v157","v158","v159", FCLOB
#define OWN_FRAGS2() asm volatile("" ::: FCLOB2)
template <int S> DI void p2_steps(f32x4 (&acc)[3][2], const P2A& a) {
    OWN_FRAGS2();
    if constexpr (S < 27) BAR();
    if constexpr (S > 0) WAIT_L(0);
    p2_body<S, 0>(acc, a);
    if constexpr (S < 26) { if constexpr (S < WREG) WAIT_L(0); else WAIT_L(8); }
    if constexpr (S < 27) p2_steps<S + 1>(acc, a);
}
template <int S, int I> DI void p2_wload(const P2A& a) {
    p2_rdw<S, S, I>(a);
    if constexpr (I + 1 < 8) p2_wload<S, I + 1>(a); else if constexpr (S + 1 < WREG) p2_wload<S + 1, 0>(a);
}

template <int NH> DI void h_stream(u32x4 rh, unsigned so, unsigned lh0, int tr, unsigned v0, unsigned v1, unsigned v2, unsigned v3) {
    auto tile = [&](unsigned slot) {
        dma4_sc1(lh0 + slot, rh, so, v0, v1, v2, v3);
        if constexpr (NH == 6) dma2_sc1(lh0 + slot + 4096, rh, so + 32 * WROW, v0, v1);
        so += 128; if (++tr == 27) { tr = 0; so -= 27 * 128; }
    };
    tile(0); tile(H_SLOT); tile(2 * H_SLOT);
    WAIT_VI(2 * NH); BAR();
    tile(3 * H_SLOT); tile(4 * H_SLOT);
    unsigned hs = 0;
#pragma unroll 1
    for (int t = 1; t < 23; ++t) {
        WAIT_VI(3 * NH); BAR();
        tile(hs); hs = hs == (H_SLOTS - 1) * H_SLOT ? 0 : hs + H_SLOT;
    }
    WAIT_VI(3 * NH); BAR();
    WAIT_VI(2 * NH); BAR();
    WAIT_VI(NH); BAR();
    WAIT_V(0); BAR();
}

__global__ __launch_bounds__(512, 2) void geglu_mlp(const void* __restrict__ x, const void* __restrict__ wg, const void* __restrict__ wu,
                                                    const void* __restrict__ wd, void* __restrict__ out, char* __restrict__ ws) {
    u64 t_entry;
    asm volatile("s_memrealtime %0\n\ts_waitcnt lgkmcnt(0)" : "=s"(t_entry));
    extern __shared__ __attribute__((aligned(16))) char smem_[];
    LAS char* smem = (LAS char*)smem_;
    const unsigned lds0 = (unsigned)(size_t)smem;
    const int lane = threadIdx.x & 63, w = rfl(threadIdx.x >> 6);
    const int xcd = blockIdx.x & 7, j = blockIdx.x >> 3;
    const int fs = (xcd & 3) * 7 + (j >> 2), pm = (xcd >> 2) * 4 + (j & 3);
    const bool prod = j < 28 && fs < 27, cons = j < 30;
    const int mt = j / 10, nt = j - mt * 10;
    const int row2 = xcd * 256 + mt * 96, ntok = mt == 2 ? 64 : 96;
    const int rot = (nt * 27) / 10;
    GAS u64* flags = (GAS u64*)(ws + ((size_t)HOFFF << 20) + WS_FLAGS);
    GAS u64* arrive = flags + 8 * NREP * 32;
    GAS u64* epoch = arrive + 256;
#define HOFF 0
    void* h = ws + ((size_t)HOFF << 20);
    u64 tpa = 0, tpb = 0;
    if (PROBE_A == 99) tpa = t_entry;
    if (w < 4) {
        if (w == 0) ST(0);
        const u64 e0v = __hip_atomic_load(epoch, __ATOMIC_RELAXED, __HIP_MEMORY_SCOPE_AGENT);
        const u64 tag = (((u64)rfl((unsigned)(e0v >> 32)) << 32) | rfl((unsigned)e0v)) * LCG_A + LCG_C;
        if (threadIdx.x == 0) __hip_atomic_store(arrive + blockIdx.x, tag, __ATOMIC_RELAXED, __HIP_MEMORY_SCOPE_AGENT);
        const int wm = w >> 1, wn = w & 1, q4 = lane >> 4;
        const int tq = (lane & 15) >> 2, tp = lane & 3;
        const unsigned tsw = (((tq >> 1) & 1) | ((q4 & 1) << 1)) << 1;
        const unsigned awc = (((wn * 4 + (tp >> 1)) ^ tsw) * 16) + 8 * (tp & 1);
        const unsigned xsw = (q4 ^ (lane & 6)) * 16;
        if (prod) {
            f32x4 ag[8][2], au[8][2];
#pragma unroll
            for (int m = 0; m < 8; ++m)
#pragma unroll
                for (int n = 0; n < 2; ++n) { ag[m][n] = (f32x4){0.f, 0.f, 0.f, 0.f}; au[m][n] = (f32x4){0.f, 0.f, 0.f, 0.f}; }
#pragma unroll
            for (int m = 0; m < 8; ++m) asm volatile("" : "+v"(ag[m][0]), "+v"(ag[m][1]), "+v"(au[m][0]), "+v"(au[m][1]));
            if (threadIdx.x == 0) *(LAS unsigned*)(smem + MISC) = 0u;
            const unsigned axb = lds0 + (wm * 128 + (lane & 15)) * 128 + xsw;
            const unsigned awb = lds0 + (q4 >> 1) * 4096 + (8 * (q4 & 1) + tq) * 128 + awc;
            WAIT_L(0);
            unsigned sl = 2 * P1_SLOT;
            OWN_FRAGS();
            BAR();
            if (w == 0) ST(1);
            p1_reads_only<0, 0, 0>(axb + sl, awb + sl, (awb ^ 32) + sl);
            WAIT_L(0);
#pragma unroll 1
            for (int t = 0; t < 10; ++t) {
                OWN_FRAGS();
                P1Stage<1, 1, 0>::run(ag, au, (axb ^ 64) + sl, awb + sl, (awb ^ 32) + sl);
                WAIT_L(0);
                BAR();
                sl = sl == 2 * P1_SLOT ? 0 : sl + P1_SLOT;
                P1Stage<0, 0, 0>::run(ag, au, axb + sl, awb + sl, (awb ^ 32) + sl);
                WAIT_L(0);
            }
            asm volatile("s_nop 7\n\ts_nop 7" ::: "memory");
            __builtin_amdgcn_sched_barrier(0);
            if (w == 0) ST(2);
            const __amdgpu_buffer_rsrc_t rh = __builtin_amdgcn_make_buffer_rsrc(h, 0, 8u << 20, 0x00020000);
            const int le = lane_id();
            const unsigned ho = (pm >> 1) * (2u << 20) + ((pm & 1) * 256 + wm * 128 + (le & 15)) * WROW + (fs * 64 + wn * 32 + ((le >> 4) & 1) * 16 + (le >> 5) * 8) * 2;
#pragma unroll
            for (int m = 0; m < 8; ++m) {
                unsigned d[2][2];
#pragma unroll
                for (int n = 0; n < 2; ++n) {
                    d[n][0] = geglu2(ag[m][n].xy, au[m][n].xy);
                    d[n][1] = geglu2(ag[m][n].zw, au[m][n].zw);
                }
                const auto s0 = __builtin_amdgcn_permlane16_swap(d[0][0], d[1][0], false, false);
                const auto s1 = __builtin_amdgcn_permlane16_swap(d[0][1], d[1][1], false, false);
                const u32x4 v = {s0[0], s1[0], s0[1], s1[1]};
                __builtin_amdgcn_raw_buffer_store_b128(v, rh, ho + m * 16 * WROW, 0, 16);
            }
            if (w == 0) ST(3);
        }
        const int tw = ntok >> 1;
        P2A a2;
        {
            const int l2 = lane_id(), g2 = l2 >> 4, r2 = (l2 & 15) >> 2, p2 = l2 & 3;
            const unsigned sw2 = (((r2 >> 1) & 1) | ((g2 & 1) << 1)) << 1;
            a2.h0 = lds0 + H_BASE + (wm * tw + (l2 & 15)) * 128 + (g2 ^ (l2 & 6)) * 16; a2.h1 = a2.h0 ^ 64;
            a2.w0 = lds0 + (8 * g2 + r2) * 128 + (((wn * 4 + (p2 >> 1)) ^ sw2) * 16) + 8 * (p2 & 1);
            a2.w1 = a2.w0 ^ 32; a2.w2 = a2.w0 + 6 * WD_SLOT; a2.w3 = a2.w1 + 6 * WD_SLOT;
        }
        if (cons) {
            OWN_FRAGS2();
            BAR();
            p2_wload<0, 0>(a2);
        }
        if (prod) {
            WAIT_V(0);
            if (w == 0) ST(4);
            const int lf = lane_id();
            unsigned old = 0;
            if (lf == 0) old = __hip_atomic_fetch_add((LAS unsigned*)(smem + MISC), 1u, __ATOMIC_RELAXED, __HIP_MEMORY_SCOPE_WORKGROUP);
            if (rfl(old) == 3 && lf < NREP) __hip_atomic_store(flags + (pm * NREP + lf) * 32 + fs, tag, __ATOMIC_RELAXED, __HIP_MEMORY_SCOPE_AGENT);
        }
        if (cons) {
            f32x4 acc[3][2];
#pragma unroll
            for (int m = 0; m < 3; ++m) { acc[m][0] = (f32x4){0.f, 0.f, 0.f, 0.f}; acc[m][1] = (f32x4){0.f, 0.f, 0.f, 0.f}; }
            asm volatile("" : "+v"(acc[0][0]), "+v"(acc[0][1]), "+v"(acc[1][0]), "+v"(acc[1][1]), "+v"(acc[2][0]), "+v"(acc[2][1]));
            OWN_FRAGS2();
            WAIT_L(0);
            BAR();
            if (w == 0) ST(8);
            OWN_FRAGS2();
            BAR();
            if (w == 0) ST(12);
            p2_body<0, 0>(acc, a2);
            WAIT_L(0);
            p2_steps<1>(acc, a2);
            asm volatile("s_nop 7\n\ts_nop 7" ::: "memory");
            __builtin_amdgcn_sched_barrier(0);
            if (w == 0) ST(9);
            const __amdgpu_buffer_rsrc_t ro = __builtin_amdgcn_make_buffer_rsrc(out, 0, M_ * D_ * 2, 0x00020000);
            const int lo = lane_id();
            const unsigned oo = (row2 + wm * tw + (lo & 15)) * XROW + (nt * 64 + wn * 32 + ((lo >> 4) & 1) * 16 + (lo >> 5) * 8) * 2;
#pragma unroll
            for (int m = 0; m < 3; ++m) {
                const auto s0 = __builtin_amdgcn_permlane16_swap(pk(acc[m][0][0], acc[m][0][1]), pk(acc[m][1][0], acc[m][1][1]), false, false);
                const auto s1 = __builtin_amdgcn_permlane16_swap(pk(acc[m][0][2], acc[m][0][3]), pk(acc[m][1][2], acc[m][1][3]), false, false);
                const u32x4 v = {s0[0], s1[0], s0[1], s1[1]};
                if (m * 16 < tw) __builtin_amdgcn_raw_buffer_store_b128(v, ro, oo + m * 16 * XROW, 0, OUTAUX);
            }
            if (w == 0) ST(10);
            if (w == 0 && blockIdx.x == PROBE_BLK) { const u64 tend = __builtin_amdgcn_s_memrealtime() + 4 * (tpb - tpa); while (__builtin_amdgcn_s_memrealtime() < tend) __builtin_amdgcn_s_sleep(1); }
        }
    } else {
        const int lw = w - 4;
        unsigned pf0 = 0, pf1 = 0, pf2 = 0;
#define PFALL 0
        if (PFALL || !prod) {
            if (lw == 3) {
                u64 pc; asm volatile("s_getpc_b64 %0" : "=s"(pc));
                const char* a = (const char*)(pc & ~255ull) - CODEPF_BACK + lane * 128;
                asm volatile("global_load_dword %0, %1, off" : "+v"(pf0) : "v"(a) : "memory");
                const char* b = a + 8192;
                if (lane < CODEPF_N2) asm volatile("global_load_dword %0, %1, off" : "+v"(pf1) : "v"(b) : "memory");
            }
            if (lw == 2) {
                const char* p = lane == 0 ? (const char*)wd : lane == 1 ? (const char*)wd + (F_ * D_ * 2 - 4) : lane == 2 ? (const char*)out : lane == 3 ? (const char*)out + (M_ * D_ * 2 - 4)
                              : lane == 8 ? (const char*)wg + (F_ * D_ * 2 - 4) : lane == 9 ? (const char*)wu + (F_ * D_ * 2 - 4) : lane == 10 ? (const char*)x + (M_ * D_ * 2 - 4)
                              : (const char*)h + (size_t)((lane - 4) & 3) * (2u << 20) + (2u << 20) - 128;
                if (lane < 11) asm volatile("global_load_dword %0, %1, off" : "+v"(pf2) : "v"(p) : "memory");
            }
        }
        u64 e0;
        asm volatile("global_load_dwordx2 %0, %1, off sc1" : "=&v"(e0) : "v"(epoch) : "memory");
        const unsigned r8 = lane >> 3, c8 = lane & 7;
        const unsigned swx = (c8 ^ (r8 & 6)) * 16;
        const unsigned sww0 = (c8 ^ (((r8 >> 1) & 1) << 1)) * 16;
        const unsigned sww1 = (c8 ^ ((((r8 >> 1) & 1) | 2) << 1)) * 16;
        const u32x4 rwd = mksrd(wd, F_ * D_ * 2);
        constexpr unsigned DX = 8 * XROW - 1024, DW = 8 * WROW - 1024;
        const unsigned vd0 = r8 * XROW + sww0, vd1 = r8 * XROW + sww1 + DX, vd2 = vd0 + 2 * DX, vd3 = vd1 + 2 * DX;
        auto wd_batch = [&](int b) {
            int tr = 6 * b + rot; if (tr >= 27) tr -= 27;
            unsigned so = (tr * 64 + lw * 16) * XROW + nt * 128, ld = lds0 + 6 * b * WD_SLOT + lw * 2048;
#pragma unroll 1
            for (int t = 0; t < 6; ++t) {
                dma2(ld, rwd, so, vd0, vd1);
                ld += WD_SLOT; so += 64 * XROW; if (++tr == 27) { tr = 0; so -= 27 * 64 * XROW; }
            }
        };
        if (prod) {
            const u32x4 rx = mksrd(x, M_ * D_ * 2), rg = mksrd(wg, D_ * F_ * 2), ru = mksrd((const char*)wu - 2048, D_ * F_ * 2 + 2048);
            const unsigned vx0 = r8 * XROW + swx, vx1 = vx0 + DX, vx2 = vx0 + 2 * DX, vx3 = vx0 + 3 * DX;
            const unsigned vg0 = r8 * WROW + sww0, vg1 = r8 * WROW + sww1 + DW;
#define ROT1 0
            int kt = ROT1 ? ((j >> 2) + 3 * (j & 3)) % 10 : 0;
            unsigned sx = (pm * 256 + lw * 64) * XROW + kt * 128, sw = (kt * 64 + lw * 16) * WROW + fs * 128;
            auto stage = [&](unsigned slot) {
                const unsigned b = lds0 + slot;
                dma22(b + 32768 + lw * 4096, rg, ru, sw, vg0, vg1);
                dma4(b + lw * 8192, rx, sx, vx0, vx1, vx2, vx3);
                dma4(b + lw * 8192 + 4096, rx, sx + 32 * XROW, vx0, vx1, vx2, vx3);
                sx += 128; sw += 64 * WROW; if (++kt == 10) { kt = 0; sx -= 1280; sw -= 640 * WROW; }
            };
            stage(2 * P1_SLOT); stage(0); stage(P1_SLOT);
            WAIT_V(24);
            asm volatile("" :: "v"(pf0), "v"(pf1), "v"(pf2));
            BAR();
            WAIT_V(12); BAR();
            unsigned sl = 2 * P1_SLOT;
#pragma unroll 1
            for (int t = 1; t < 8; ++t) {
                stage(sl);
                sl = sl == 2 * P1_SLOT ? 0 : sl + P1_SLOT;
                WAIT_V(12); BAR();
            }
            wd_batch(0);
            WAIT_V(12); BAR();
            wd_batch(1);
            if (lw == 0) ST(6);
            BAR();
        } else {
            {
                const int sp = (j >= 28 ? xcd * 4 + (j - 28) : 32 + (xcd >> 2) * 4 + (j & 3)) * 4 + lw;
                const u32x4 sx_ = mksrd(x, M_ * D_ * 2), sg_ = mksrd(wg, D_ * F_ * 2), su_ = mksrd(wu, D_ * F_ * 2);
                unsigned dummy = 0;
#pragma unroll 1
                for (int g = sp; g < 9 * 86; g += 160) {
                    const int kt = g / 86 + 1, r = g % 86;
                    if (r < 32) asm volatile("buffer_load_dword %0, %1, %2, %3 offen" : "+v"(dummy) : "v"(lane * XROW), "s"(sx_), "s"((unsigned)(r * 64 * XROW + kt * 128)) : "memory");
                    else if (r < 59) asm volatile("buffer_load_dword %0, %1, %2, %3 offen" : "+v"(dummy) : "v"(lane * WROW), "s"(sg_), "s"((unsigned)(kt * 64 * WROW + (r - 32) * 128)) : "memory");
                    else asm volatile("buffer_load_dword %0, %1, %2, %3 offen" : "+v"(dummy) : "v"(lane * WROW), "s"(su_), "s"((unsigned)(kt * 64 * WROW + (r - 59) * 128)) : "memory");
                }
#pragma unroll 1
                for (int g = sp; g < 270; g += 160) {
                    const int kt = g / 10, sl = g - kt * 10;
                    asm volatile("buffer_load_dword %0, %1, %2, %3 offen" : "+v"(dummy) : "v"(lane * XROW), "s"(rwd), "s"((unsigned)(kt * 64 * XROW + sl * 128)) : "memory");
                }
                WAIT_V(0);
                asm volatile("" :: "v"(dummy));
            }
            if (cons) { wd_batch(0); wd_batch(1); }
            WAIT_V(0);
            asm volatile("" :: "v"(pf0), "v"(pf1), "v"(pf2));
        }
        if (cons) {
            WAIT_V(0);
            if (lw == 0) ST(7);
            BAR();
            BAR();
            {
                int tr = WD_SLOTS + rot; if (tr >= 27) tr -= 27;
                unsigned so = (tr * 64 + lw * 16) * XROW + nt * 128, ld = lds0 + lw * 2048;
#pragma unroll 1
                for (int t = 0; t < WREG; ++t) {
                    dma2(ld, rwd, so, vd0, vd1);
                    ld += WD_SLOT; so += 64 * XROW; if (++tr == 27) { tr = 0; so -= 27 * 64 * XROW; }
                }
            }
            if (lw < 2) {
                if (lw == 0) ST(11);
                int tr = WD_SLOTS + WREG + rot; if (tr >= 27) tr -= 27;
                unsigned so = (tr * 64 + lw * 32) * XROW + nt * 128, ld = WREG * WD_SLOT;
                WAIT_V(0);
#pragma unroll 1
                for (int t = 0; t < 12; ++t) BAR();
#pragma unroll 1
                for (int t = 12; t < 17; ++t) {
                    BAR();
                    dma4(lds0 + ld + lw * 4096, rwd, so, vd0, vd1, vd2, vd3);
                    ld = ld == (WD_SLOTS - 1) * WD_SLOT ? 0 : ld + WD_SLOT; so += 64 * XROW; if (++tr == 27) { tr = 0; so -= 27 * 64 * XROW; }
                }
                BAR(); BAR(); BAR(); BAR(); BAR();
                WAIT_V(16); BAR(); WAIT_V(12); BAR(); WAIT_V(8); BAR(); WAIT_V(4); BAR(); WAIT_V(0); BAR();
            } else {
                WAIT_V(0);
                asm volatile("" : "+v"(e0));
                const u64 tag = e0 * LCG_A + LCG_C;
                GAS u64* pf = flags + (xcd * NREP + nt % NREP) * 32 + (lane < 27 ? lane : 0);
                const u64 t0 = __builtin_amdgcn_s_memrealtime();
                for (;;) {
                    const u64 v = __hip_atomic_load(pf, __ATOMIC_RELAXED, __HIP_MEMORY_SCOPE_AGENT);
                    if (__all(v == tag)) break;
                    if (__builtin_amdgcn_s_memrealtime() - t0 > 5000000ull) break;
                    __builtin_amdgcn_s_sleep(PSLEEP);
                }
                if (lw == 2) ST(5);
                const u32x4 rh = mksrd(h, 8u << 20);
                const unsigned v0 = r8 * WROW + swx, v1 = v0 + DW, v2 = v0 + 2 * DW, v3 = v0 + 3 * DW;
                const int hl = lw - 2;
                if (ntok == 96) h_stream<6>(rh, (xcd >> 1) * (2u << 20) + ((xcd & 1) * 256 + mt * 96 + hl * 48) * WROW + rot * 128, lds0 + H_BASE + hl * 6144, rot, v0, v1, v2, v3);
                else h_stream<4>(rh, (xcd >> 1) * (2u << 20) + ((xcd & 1) * 256 + mt * 96 + hl * 32) * WROW + rot * 128, lds0 + H_BASE + hl * 4096, rot, v0, v1, v2, v3);
            }
        }
        if (blockIdx.x == 0 && lw == 0) {
            WAIT_V(0);
            asm volatile("" : "+v"(e0));
            const u64 tag = e0 * LCG_A + LCG_C;
            const u64 t0 = __builtin_amdgcn_s_memrealtime();
            for (;;) {
                bool ok = true;
#pragma unroll
                for (int i = 0; i < 4; ++i) ok &= __hip_atomic_load(arrive + i * 64 + lane, __ATOMIC_RELAXED, __HIP_MEMORY_SCOPE_AGENT) == tag;
                if (__all(ok)) break;
                if (__builtin_amdgcn_s_memrealtime() - t0 > 5000000ull) break;
                __builtin_amdgcn_s_sleep(1);
            }
            if (lane == 0) __hip_atomic_store(epoch, tag, __ATOMIC_RELAXED, __HIP_MEMORY_SCOPE_AGENT);
        }
    }
}

extern "C" void kernel_launch(void* const* d_in, const int* in_sizes, int n_in, void* d_out, int out_size, void* d_ws, size_t ws_size, hipStream_t stream) {
    (void)in_sizes; (void)n_in; (void)out_size; (void)ws_size;
    (void)hipFuncSetAttribute((const void*)geglu_mlp, hipFuncAttributeMaxDynamicSharedMemorySize, LDS_BYTES);
    geglu_mlp<<<256, 512, LDS_BYTES, stream>>>(d_in[0], d_in[1], d_in[2], d_in[3], d_out, (char*)d_ws);
}
```

```cpp
#include <hip/hip_runtime.h>
#include <hip/hip_bf16.h>

#define LAS __attribute__((address_space(3)))
#define GAS __attribute__((address_space(1)))
#define DI __device__ __forceinline__
typedef __bf16 bf16x2 __attribute__((ext_vector_type(2)));
typedef float f32x2 __attribute__((ext_vector_type(2)));
typedef float f32x4 __attribute__((ext_vector_type(4)));
typedef unsigned u32x4 __attribute__((ext_vector_type(4)));
typedef unsigned long long u64;

constexpr int M_ = 2048, D_ = 640, F_ = 1728;
constexpr unsigned XROW = D_ * 2, WROW = F_ * 2;
constexpr unsigned HPAGE = 2u << 20;
constexpr size_t WS_FLAGS = 8u << 20;
constexpr u64 LCG_A = 6364136223846793005ull, LCG_C = 1442695040888963407ull;
constexpr unsigned P1_SLOT = 49152;
constexpr unsigned WD_SLOT = 8192, H_BASE = 11 * WD_SLOT, H_SLOT = 12288;
constexpr unsigned MISC = 160 * 1024 - 64;
constexpr int LDS_BYTES = 160 * 1024;
constexpr unsigned TLB_STEP = 128u << 10;
constexpr int CODE_LINES = 150;

#define WAIT_V(n) asm volatile("s_waitcnt vmcnt(" #n ")" ::: "memory")
#define WAIT_L(n) asm volatile("s_waitcnt lgkmcnt(" #n ")" ::: "memory")
#define BAR() asm volatile("s_barrier" ::: "memory")

DI unsigned rfl(unsigned v) { return __builtin_amdgcn_readfirstlane(v); }
DI int lane_id() { int l; asm volatile("v_mbcnt_lo_u32_b32 %0, -1, 0\n\tv_mbcnt_hi_u32_b32 %0, -1, %0" : "=v"(l)); return l; }
DI u32x4 mksrd(const void* p, unsigned bytes) {
    const u64 a = (u64)p;
    return (u32x4){(unsigned)a, (unsigned)(a >> 32) & 0xffffu, bytes, 0x00020000u};
}
DI unsigned pk(float a, float b) { bf16x2 v = {(__bf16)a, (__bf16)b}; return __builtin_bit_cast(unsigned, v); }

#define DMA_HEAD "s_mov_b32 m0, %0\n\ts_nop 0\n\t"
DI void dma4(unsigned dst, u32x4 r, unsigned soff, unsigned v0, unsigned v1, unsigned v2, unsigned v3) {
    asm volatile(DMA_HEAD "buffer_load_dwordx4 %3, %1, %2 offen lds\n\tbuffer_load_dwordx4 %4, %1, %2 offen offset:1024 lds\n\t"
                 "buffer_load_dwordx4 %5, %1, %2 offen offset:2048 lds\n\tbuffer_load_dwordx4 %6, %1, %2 offen offset:3072 lds"
                 :: "s"(dst), "s"(r), "s"(soff), "v"(v0), "v"(v1), "v"(v2), "v"(v3) : "memory", "m0");
}
DI void dma2(unsigned dst, u32x4 r, unsigned soff, unsigned v0, unsigned v1) {
    asm volatile(DMA_HEAD "buffer_load_dwordx4 %3, %1, %2 offen lds\n\tbuffer_load_dwordx4 %4, %1, %2 offen offset:1024 lds"
                 :: "s"(dst), "s"(r), "s"(soff), "v"(v0), "v"(v1) : "memory", "m0");
}
DI void dma22(unsigned dst, u32x4 ra, u32x4 rb, unsigned soff, unsigned v0, unsigned v1) {
    asm volatile(DMA_HEAD "buffer_load_dwordx4 %4, %1, %3 offen lds\n\tbuffer_load_dwordx4 %5, %1, %3 offen offset:1024 lds\n\t"
                 "buffer_load_dwordx4 %4, %2, %3 offen offset:2048 lds\n\tbuffer_load_dwordx4 %5, %2, %3 offen offset:3072 lds"
                 :: "s"(dst), "s"(ra), "s"(rb), "s"(soff), "v"(v0), "v"(v1) : "memory", "m0");
}

#define P1_INIT \
    "v_mov_b64 v[0:1], 0\n\t" \
    "v_mov_b64 v[2:3], 0\n\t" \
    "v_mov_b64 v[4:5], 0\n\t" \
    "v_mov_b64 v[6:7], 0\n\t" \
    "v_mov_b64 v[8:9], 0\n\t" \
    "v_mov_b64 v[10:11], 0\n\t" \
    "v_mov_b64 v[12:13], 0\n\t" \
    "v_mov_b64 v[14:15], 0\n\t" \
    "v_mov_b64 v[16:17], 0\n\t" \
    "v_mov_b64 v[18:19], 0\n\t" \
    "v_mov_b64 v[20:21], 0\n\t" \
    "v_mov_b64 v[22:23], 0\n\t" \
    "v_mov_b64 v[24:25], 0\n\t" \
    "v_mov_b64 v[26:27], 0\n\t" \
    "v_mov_b64 v[28:29], 0\n\t" \
    "v_mov_b64 v[30:31], 0\n\t" \
    "v_mov_b64 v[32:33], 0\n\t" \
    "v_mov_b64 v[34:35], 0\n\t" \
    "v_mov_b64 v[36:37], 0\n\t" \
    "v_mov_b64 v[38:39], 0\n\t" \
    "v_mov_b64 v[40:41], 0\n\t" \
    "v_mov_b64 v[42:43], 0\n\t" \
    "v_mov_b64 v[44:45], 0\n\t" \
    "v_mov_b64 v[46:47], 0\n\t" \
    "v_mov_b64 v[48:49], 0\n\t" \
    "v_mov_b64 v[50:51], 0\n\t" \
    "v_mov_b64 v[52:53], 0\n\t" \
    "v_mov_b64 v[54:55], 0\n\t" \
    "v_mov_b64 v[56:57], 0\n\t" \
    "v_mov_b64 v[58:59], 0\n\t" \
    "v_mov_b64 v[60:61], 0\n\t" \
    "v_mov_b64 v[62:63], 0\n\t" \
    "v_mov_b64 v[64:65], 0\n\t" \
    "v_mov_b64 v[66:67], 0\n\t" \
    "v_mov_b64 v[68:69], 0\n\t" \
    "v_mov_b64 v[70:71], 0\n\t" \
    "v_mov_b64 v[72:73], 0\n\t" \
    "v_mov_b64 v[74:75], 0\n\t" \
    "v_mov_b64 v[76:77], 0\n\t" \
    "v_mov_b64 v[78:79], 0\n\t" \
    "v_mov_b64 v[80:81], 0\n\t" \
    "v_mov_b64 v[82:83], 0\n\t" \
    "v_mov_b64 v[84:85], 0\n\t" \
    "v_mov_b64 v[86:87], 0\n\t" \
    "v_mov_b64 v[88:89], 0\n\t" \
    "v_mov_b64 v[90:91], 0\n\t" \
    "v_mov_b64 v[92:93], 0\n\t" \
    "v_mov_b64 v[94:95], 0\n\t" \
    "v_mov_b64 v[96:97], 0\n\t" \
    "v_mov_b64 v[98:99], 0\n\t" \
    "v_mov_b64 v[100:101], 0\n\t" \
    "v_mov_b64 v[102:103], 0\n\t" \
    "v_mov_b64 v[104:105], 0\n\t" \
    "v_mov_b64 v[106:107], 0\n\t" \
    "v_mov_b64 v[108:109], 0\n\t" \
    "v_mov_b64 v[110:111], 0\n\t" \
    "v_mov_b64 v[112:113], 0\n\t" \
    "v_mov_b64 v[114:115], 0\n\t" \
    "v_mov_b64 v[116:117], 0\n\t" \
    "v_mov_b64 v[118:119], 0\n\t" \
    "v_mov_b64 v[120:121], 0\n\t" \
    "v_mov_b64 v[122:123], 0\n\t" \
    "v_mov_b64 v[124:125], 0\n\t" \
    "v_mov_b64 v[126:127], 0\n\t"
#define P1_LOOP \
    "s_barrier\n\t" \
    "ds_read_b64_tr_b16 v[192:193], %11 offset:32768\n\t" \
    "ds_read_b64_tr_b16 v[194:195], %11 offset:33280\n\t" \
    "ds_read_b64_tr_b16 v[200:201], %11 offset:34816\n\t" \
    "ds_read_b64_tr_b16 v[202:203], %11 offset:35328\n\t" \
    "ds_read_b64_tr_b16 v[196:197], %12 offset:32768\n\t" \
    "ds_read_b64_tr_b16 v[198:199], %12 offset:33280\n\t" \
    "ds_read_b64_tr_b16 v[204:205], %12 offset:34816\n\t" \
    "ds_read_b64_tr_b16 v[206:207], %12 offset:35328\n\t" \
    "ds_read_b128 v[160:163], %5 offset:0\n\t" \
    "ds_read_b128 v[164:167], %5 offset:2048\n\t" \
    "ds_read_b128 v[168:171], %5 offset:4096\n\t" \
    "ds_read_b128 v[172:175], %5 offset:6144\n\t" \
    "ds_read_b128 v[176:179], %5 offset:8192\n\t" \
    "ds_read_b128 v[180:183], %5 offset:10240\n\t" \
    "ds_read_b128 v[184:187], %5 offset:12288\n\t" \
    "ds_read_b128 v[188:191], %5 offset:14336\n\t" \
    "s_waitcnt lgkmcnt(0)\n\t" \
    "s_mov_b32 %0, 3\n\t" \
    "LP1_%=:\n\t" \
    "v_mfma_f32_16x16x32_bf16 v[0:3], v[192:195], v[160:163], v[0:3]\n\t" \
    "ds_read_b64_tr_b16 v[240:241], %11 offset:40960\n\t" \
    "v_mfma_f32_16x16x32_bf16 v[4:7], v[200:203], v[160:163], v[4:7]\n\t" \
    "ds_read_b64_tr_b16 v[242:243], %11 offset:41472\n\t" \
    "v_mfma_f32_16x16x32_bf16 v[8:11], v[196:199], v[160:163], v[8:11]\n\t" \
    "v_mfma_f32_16x16x32_bf16 v[12:15], v[204:207], v[160:163], v[12:15]\n\t" \
    "ds_read_b64_tr_b16 v[248:249], %11 offset:43008\n\t" \
    "v_mfma_f32_16x16x32_bf16 v[16:19], v[192:195], v[164:167], v[16:19]\n\t" \
    "ds_read_b64_tr_b16 v[250:251], %11 offset:43520\n\t" \
    "v_mfma_f32_16x16x32_bf16 v[20:23], v[200:203], v[164:167], v[20:23]\n\t" \
    "v_mfma_f32_16x16x32_bf16 v[24:27], v[196:199], v[164:167], v[24:27]\n\t" \
    "ds_read_b64_tr_b16 v[244:245], %12 offset:40960\n\t" \
    "v_mfma_f32_16x16x32_bf16 v[28:31], v[204:207], v[164:167], v[28:31]\n\t" \
    "ds_read_b64_tr_b16 v[246:247], %12 offset:41472\n\t" \
    "v_mfma_f32_16x16x32_bf16 v[32:35], v[192:195], v[168:171], v[32:35]\n\t" \
    "v_mfma_f32_16x16x32_bf16 v[36:39], v[200:203], v[168:171], v[36:39]\n\t" \
    "ds_read_b64_tr_b16 v[252:253], %12 offset:43008\n\t" \
    "v_mfma_f32_16x16x32_bf16 v[40:43], v[196:199], v[168:171], v[40:43]\n\t" \
    "ds_read_b64_tr_b16 v[254:255], %12 offset:43520\n\t" \
    "v_mfma_f32_16x16x32_bf16 v[44:47], v[204:207], v[168:171], v[44:47]\n\t" \
    "v_mfma_f32_16x16x32_bf16 v[48:51], v[192:195], v[172:175], v[48:51]\n\t" \
    "ds_read_b128 v[208:211], %6 offset:0\n\t" \
    "v_mfma_f32_16x16x32_bf16 v[52:55], v[200:203], v[172:175], v[52:55]\n\t" \
    "ds_read_b128 v[212:215], %6 offset:2048\n\t" \
    "v_mfma_f32_16x16x32_bf16 v[56:59], v[196:199], v[172:175], v[56:59]\n\t" \
    "v_mfma_f32_16x16x32_bf16 v[60:63], v[204:207], v[172:175], v[60:63]\n\t" \
    "ds_read_b128 v[216:219], %6 offset:4096\n\t" \
    "v_mfma_f32_16x16x32_bf16 v[64:67], v[192:195], v[176:179], v[64:67]\n\t" \
    "ds_read_b128 v[220:223], %6 offset:6144\n\t" \
    "v_mfma_f32_16x16x32_bf16 v[68:71], v[200:203], v[176:179], v[68:71]\n\t" \
    "v_mfma_f32_16x16x32_bf16 v[72:75], v[196:199], v[176:179], v[72:75]\n\t" \
    "ds_read_b128 v[224:227], %6 offset:8192\n\t" \
    "v_mfma_f32_16x16x32_bf16 v[76:79], v[204:207], v[176:179], v[76:79]\n\t" \
    "ds_read_b128 v[228:231], %6 offset:10240\n\t" \
    "v_mfma_f32_16x16x32_bf16 v[80:83], v[192:195], v[180:183], v[80:83]\n\t" \
    "v_mfma_f32_16x16x32_bf16 v[84:87], v[200:203], v[180:183], v[84:87]\n\t" \
    "ds_read_b128 v[232:235], %6 offset:12288\n\t" \
    "v_mfma_f32_16x16x32_bf16 v[88:91], v[196:199], v[180:183], v[88:91]\n\t" \
    "ds_read_b128 v[236:239], %6 offset:14336\n\t" \
    "v_mfma_f32_16x16x32_bf16 v[92:95], v[204:207], v[180:183], v[92:95]\n\t" \
    "v_mfma_f32_16x16x32_bf16 v[96:99], v[192:195], v[184:187], v[96:99]\n\t" \
    "v_mfma_f32_16x16x32_bf16 v[100:103], v[200:203], v[184:187], v[100:103]\n\t" \
    "v_mfma_f32_16x16x32_bf16 v[104:107], v[196:199], v[184:187], v[104:107]\n\t" \
    "v_mfma_f32_16x16x32_bf16 v[108:111], v[204:207], v[184:187], v[108:111]\n\t" \
    "v_mfma_f32_16x16x32_bf16 v[112:115], v[192:195], v[188:191], v[112:115]\n\t" \
    "v_mfma_f32_16x16x32_bf16 v[116:119], v[200:203], v[188:191], v[116:119]\n\t" \
    "v_mfma_f32_16x16x32_bf16 v[120:123], v[196:199], v[188:191], v[120:123]\n\t" \
    "v_mfma_f32_16x16x32_bf16 v[124:127], v[204:207], v[188:191], v[124:127]\n\t" \
    "s_waitcnt lgkmcnt(0)\n\t" \
    "s_barrier\n\t" \
    "v_mfma_f32_16x16x32_bf16 v[0:3], v[240:243], v[208:211], v[0:3]\n\t" \
    "ds_read_b64_tr_b16 v[192:193], %7 offset:32768\n\t" \
    "v_mfma_f32_16x16x32_bf16 v[4:7], v[248:251], v[208:211], v[4:7]\n\t" \
    "ds_read_b64_tr_b16 v[194:195], %7 offset:33280\n\t" \
    "v_mfma_f32_16x16x32_bf16 v[8:11], v[244:247], v[208:211], v[8:11]\n\t" \
    "v_mfma_f32_16x16x32_bf16 v[12:15], v[252:255], v[208:211], v[12:15]\n\t" \
    "ds_read_b64_tr_b16 v[200:201], %7 offset:34816\n\t" \
    "v_mfma_f32_16x16x32_bf16 v[16:19], v[240:243], v[212:215], v[16:19]\n\t" \
    "ds_read_b64_tr_b16 v[202:203], %7 offset:35328\n\t" \
    "v_mfma_f32_16x16x32_bf16 v[20:23], v[248:251], v[212:215], v[20:23]\n\t" \
    "v_mfma_f32_16x16x32_bf16 v[24:27], v[244:247], v[212:215], v[24:27]\n\t" \
    "ds_read_b64_tr_b16 v[196:197], %8 offset:32768\n\t" \
    "v_mfma_f32_16x16x32_bf16 v[28:31], v[252:255], v[212:215], v[28:31]\n\t" \
    "ds_read_b64_tr_b16 v[198:199], %8 offset:33280\n\t" \
    "v_mfma_f32_16x16x32_bf16 v[32:35], v[240:243], v[216:219], v[32:35]\n\t" \
    "v_mfma_f32_16x16x32_bf16 v[36:39], v[248:251], v[216:219], v[36:39]\n\t" \
    "ds_read_b64_tr_b16 v[204:205], %8 offset:34816\n\t" \
    "v_mfma_f32_16x16x32_bf16 v[40:43], v[244:247], v[216:219], v[40:43]\n\t" \
    "ds_read_b64_tr_b16 v[206:207], %8 offset:35328\n\t" \
    "v_mfma_f32_16x16x32_bf16 v[44:47], v[252:255], v[216:219], v[44:47]\n\t" \
    "v_mfma_f32_16x16x32_bf16 v[48:51], v[240:243], v[220:223], v[48:51]\n\t" \
    "ds_read_b128 v[160:163], %1 offset:0\n\t" \
    "v_mfma_f32_16x16x32_bf16 v[52:55], v[248:251], v[220:223], v[52:55]\n\t" \
    "ds_read_b128 v[164:167], %1 offset:2048\n\t" \
    "v_mfma_f32_16x16x32_bf16 v[56:59], v[244:247], v[220:223], v[56:59]\n\t" \
    "v_mfma_f32_16x16x32_bf16 v[60:63], v[252:255], v[220:223], v[60:63]\n\t" \
    "ds_read_b128 v[168:171], %1 offset:4096\n\t" \
    "v_mfma_f32_16x16x32_bf16 v[64:67], v[240:243], v[224:227], v[64:67]\n\t" \
    "ds_read_b128 v[172:175], %1 offset:6144\n\t" \
    "v_mfma_f32_16x16x32_bf16 v[68:71], v[248:251], v[224:227], v[68:71]\n\t" \
    "v_mfma_f32_16x16x32_bf16 v[72:75], v[244:247], v[224:227], v[72:75]\n\t" \
    "ds_read_b128 v[176:179], %1 offset:8192\n\t" \
    "v_mfma_f32_16x16x32_bf16 v[76:79], v[252:255], v[224:227], v[76:79]\n\t" \
    "ds_read_b128 v[180:183], %1 offset:10240\n\t" \
    "v_mfma_f32_16x16x32_bf16 v[80:83], v[240:243], v[228:231], v[80:83]\n\t" \
    "v_mfma_f32_16x16x32_bf16 v[84:87], v[248:251], v[228:231], v[84:87]\n\t" \
    "ds_read_b128 v[184:187], %1 offset:12288\n\t" \
    "v_mfma_f32_16x16x32_bf16 v[88:91], v[244:247], v[228:231], v[88:91]\n\t" \
    "ds_read_b128 v[188:191], %1 offset:14336\n\t" \
    "v_mfma_f32_16x16x32_bf16 v[92:95], v[252:255], v[228:231], v[92:95]\n\t" \
    "v_mfma_f32_16x16x32_bf16 v[96:99], v[240:243], v[232:235], v[96:99]\n\t" \
    "v_mfma_f32_16x16x32_bf16 v[100:103], v[248:251], v[232:235], v[100:103]\n\t" \
    "v_mfma_f32_16x16x32_bf16 v[104:107], v[244:247], v[232:235], v[104:107]\n\t" \
    "v_mfma_f32_16x16x32_bf16 v[108:111], v[252:255], v[232:235], v[108:111]\n\t" \
    "v_mfma_f32_16x16x32_bf16 v[112:115], v[240:243], v[236:239], v[112:115]\n\t" \
    "v_mfma_f32_16x16x32_bf16 v[116:119], v[248:251], v[236:239], v[116:119]\n\t" \
    "v_mfma_f32_16x16x32_bf16 v[120:123], v[244:247], v[236:239], v[120:123]\n\t" \
    "v_mfma_f32_16x16x32_bf16 v[124:127], v[252:255], v[236:239], v[124:127]\n\t" \
    "s_waitcnt lgkmcnt(0)\n\t" \
    "v_mfma_f32_16x16x32_bf16 v[0:3], v[192:195], v[160:163], v[0:3]\n\t" \
    "ds_read_b64_tr_b16 v[240:241], %7 offset:40960\n\t" \
    "v_mfma_f32_16x16x32_bf16 v[4:7], v[200:203], v[160:163], v[4:7]\n\t" \
    "ds_read_b64_tr_b16 v[242:243], %7 offset:41472\n\t" \
    "v_mfma_f32_16x16x32_bf16 v[8:11], v[196:199], v[160:163], v[8:11]\n\t" \
    "v_mfma_f32_16x16x32_bf16 v[12:15], v[204:207], v[160:163], v[12:15]\n\t" \
    "ds_read_b64_tr_b16 v[248:249], %7 offset:43008\n\t" \
    "v_mfma_f32_16x16x32_bf16 v[16:19], v[192:195], v[164:167], v[16:19]\n\t" \
    "ds_read_b64_tr_b16 v[250:251], %7 offset:43520\n\t" \
    "v_mfma_f32_16x16x32_bf16 v[20:23], v[200:203], v[164:167], v[20:23]\n\t" \
    "v_mfma_f32_16x16x32_bf16 v[24:27], v[196:199], v[164:167], v[24:27]\n\t" \
    "ds_read_b64_tr_b16 v[244:245], %8 offset:40960\n\t" \
    "v_mfma_f32_16x16x32_bf16 v[28:31], v[204:207], v[164:167], v[28:31]\n\t" \
    "ds_read_b64_tr_b16 v[246:247], %8 offset:41472\n\t" \
    "v_mfma_f32_16x16x32_bf16 v[32:35], v[192:195], v[168:171], v[32:35]\n\t" \
    "v_mfma_f32_16x16x32_bf16 v[36:39], v[200:203], v[168:171], v[36:39]\n\t" \
    "ds_read_b64_tr_b16 v[252:253], %8 offset:43008\n\t" \
    "v_mfma_f32_16x16x32_bf16 v[40:43], v[196:199], v[168:171], v[40:43]\n\t" \
    "ds_read_b64_tr_b16 v[254:255], %8 offset:43520\n\t" \
    "v_mfma_f32_16x16x32_bf16 v[44:47], v[204:207], v[168:171], v[44:47]\n\t" \
    "v_mfma_f32_16x16x32_bf16 v[48:51], v[192:195], v[172:175], v[48:51]\n\t" \
    "ds_read_b128 v[208:211], %2 offset:0\n\t" \
    "v_mfma_f32_16x16x32_bf16 v[52:55], v[200:203], v[172:175], v[52:55]\n\t" \
    "ds_read_b128 v[212:215], %2 offset:2048\n\t" \
    "v_mfma_f32_16x16x32_bf16 v[56:59], v[196:199], v[172:175], v[56:59]\n\t" \
    "v_mfma_f32_16x16x32_bf16 v[60:63], v[204:207], v[172:175], v[60:63]\n\t" \
    "ds_read_b128 v[216:219], %2 offset:4096\n\t" \
    "v_mfma_f32_16x16x32_bf16 v[64:67], v[192:195], v[176:179], v[64:67]\n\t" \
    "ds_read_b128 v[220:223], %2 offset:6144\n\t" \
    "v_mfma_f32_16x16x32_bf16 v[68:71], v[200:203], v[176:179], v[68:71]\n\t" \
    "v_mfma_f32_16x16x32_bf16 v[72:75], v[196:199], v[176:179], v[72:75]\n\t" \
    "ds_read_b128 v[224:227], %2 offset:8192\n\t" \
    "v_mfma_f32_16x16x32_bf16 v[76:79], v[204:207], v[176:179], v[76:79]\n\t" \
    "ds_read_b128 v[228:231], %2 offset:10240\n\t" \
    "v_mfma_f32_16x16x32_bf16 v[80:83], v[192:195], v[180:183], v[80:83]\n\t" \
    "v_mfma_f32_16x16x32_bf16 v[84:87], v[200:203], v[180:183], v[84:87]\n\t" \
    "ds_read_b128 v[232:235], %2 offset:12288\n\t" \
    "v_mfma_f32_16x16x32_bf16 v[88:91], v[196:199], v[180:183], v[88:91]\n\t" \
    "ds_read_b128 v[236:239], %2 offset:14336\n\t" \
    "v_mfma_f32_16x16x32_bf16 v[92:95], v[204:207], v[180:183], v[92:95]\n\t" \
    "v_mfma_f32_16x16x32_bf16 v[96:99], v[192:195], v[184:187], v[96:99]\n\t" \
    "v_mfma_f32_16x16x32_bf16 v[100:103], v[200:203], v[184:187], v[100:103]\n\t" \
    "v_mfma_f32_16x16x32_bf16 v[104:107], v[196:199], v[184:187], v[104:107]\n\t" \
    "v_mfma_f32_16x16x32_bf16 v[108:111], v[204:207], v[184:187], v[108:111]\n\t" \
    "v_mfma_f32_16x16x32_bf16 v[112:115], v[192:195], v[188:191], v[112:115]\n\t" \
    "v_mfma_f32_16x16x32_bf16 v[116:119], v[200:203], v[188:191], v[116:119]\n\t" \
    "v_mfma_f32_16x16x32_bf16 v[120:123], v[196:199], v[188:191], v[120:123]\n\t" \
    "v_mfma_f32_16x16x32_bf16 v[124:127], v[204:207], v[188:191], v[124:127]\n\t" \
    "s_waitcnt lgkmcnt(0)\n\t" \
    "s_barrier\n\t" \
    "v_mfma_f32_16x16x32_bf16 v[0:3], v[240:243], v[208:211], v[0:3]\n\t" \
    "ds_read_b64_tr_b16 v[192:193], %9 offset:32768\n\t" \
    "v_mfma_f32_16x16x32_bf16 v[4:7], v[248:251], v[208:211], v[4:7]\n\t" \
    "ds_read_b64_tr_b16 v[194:195], %9 offset:33280\n\t" \
    "v_mfma_f32_16x16x32_bf16 v[8:11], v[244:247], v[208:211], v[8:11]\n\t" \
    "v_mfma_f32_16x16x32_bf16 v[12:15], v[252:255], v[208:211], v[12:15]\n\t" \
    "ds_read_b64_tr_b16 v[200:201], %9 offset:34816\n\t" \
    "v_mfma_f32_16x16x32_bf16 v[16:19], v[240:243], v[212:215], v[16:19]\n\t" \
    "ds_read_b64_tr_b16 v[202:203], %9 offset:35328\n\t" \
    "v_mfma_f32_16x16x32_bf16 v[20:23], v[248:251], v[212:215], v[20:23]\n\t" \
    "v_mfma_f32_16x16x32_bf16 v[24:27], v[244:247], v[212:215], v[24:27]\n\t" \
    "ds_read_b64_tr_b16 v[196:197], %10 offset:32768\n\t" \
    "v_mfma_f32_16x16x32_bf16 v[28:31], v[252:255], v[212:215], v[28:31]\n\t" \
    "ds_read_b64_tr_b16 v[198:199], %10 offset:33280\n\t" \
    "v_mfma_f32_16x16x32_bf16 v[32:35], v[240:243], v[216:219], v[32:35]\n\t" \
    "v_mfma_f32_16x16x32_bf16 v[36:39], v[248:251], v[216:219], v[36:39]\n\t" \
    "ds_read_b64_tr_b16 v[204:205], %10 offset:34816\n\t" \
    "v_mfma_f32_16x16x32_bf16 v[40:43], v[244:247], v[216:219], v[40:43]\n\t" \
    "ds_read_b64_tr_b16 v[206:207], %10 offset:35328\n\t" \
    "v_mfma_f32_16x16x32_bf16 v[44:47], v[252:255], v[216:219], v[44:47]\n\t" \
    "v_mfma_f32_16x16x32_bf16 v[48:51], v[240:243], v[220:223], v[48:51]\n\t" \
    "ds_read_b128 v[160:163], %3 offset:0\n\t" \
    "v_mfma_f32_16x16x32_bf16 v[52:55], v[248:251], v[220:223], v[52:55]\n\t" \
    "ds_read_b128 v[164:167], %3 offset:2048\n\t" \
    "v_mfma_f32_16x16x32_bf16 v[56:59], v[244:247], v[220:223], v[56:59]\n\t" \
    "v_mfma_f32_16x16x32_bf16 v[60:63], v[252:255], v[220:223], v[60:63]\n\t" \
    "ds_read_b128 v[168:171], %3 offset:4096\n\t" \
    "v_mfma_f32_16x16x32_bf16 v[64:67], v[240:243], v[224:227], v[64:67]\n\t" \
    "ds_read_b128 v[172:175], %3 offset:6144\n\t" \
    "v_mfma_f32_16x16x32_bf16 v[68:71], v[248:251], v[224:227], v[68:71]\n\t" \
    "v_mfma_f32_16x16x32_bf16 v[72:75], v[244:247], v[224:227], v[72:75]\n\t" \
    "ds_read_b128 v[176:179], %3 offset:8192\n\t" \
    "v_mfma_f32_16x16x32_bf16 v[76:79], v[252:255], v[224:227], v[76:79]\n\t" \
    "ds_read_b128 v[180:183], %3 offset:10240\n\t" \
    "v_mfma_f32_16x16x32_bf16 v[80:83], v[240:243], v[228:231], v[80:83]\n\t" \
    "v_mfma_f32_16x16x32_bf16 v[84:87], v[248:251], v[228:231], v[84:87]\n\t" \
    "ds_read_b128 v[184:187], %3 offset:12288\n\t" \
    "v_mfma_f32_16x16x32_bf16 v[88:91], v[244:247], v[228:231], v[88:91]\n\t" \
    "ds_read_b128 v[188:191], %3 offset:14336\n\t" \
    "v_mfma_f32_16x16x32_bf16 v[92:95], v[252:255], v[228:231], v[92:95]\n\t" \
    "v_mfma_f32_16x16x32_bf16 v[96:99], v[240:243], v[232:235], v[96:99]\n\t" \
    "v_mfma_f32_16x16x32_bf16 v[100:103], v[248:251], v[232:235], v[100:103]\n\t" \
    "v_mfma_f32_16x16x32_bf16 v[104:107], v[244:247], v[232:235], v[104:107]\n\t" \
    "v_mfma_f32_16x16x32_bf16 v[108:111], v[252:255], v[232:235], v[108:111]\n\t" \
    "v_mfma_f32_16x16x32_bf16 v[112:115], v[240:243], v[236:239], v[112:115]\n\t" \
    "v_mfma_f32_16x16x32_bf16 v[116:119], v[248:251], v[236:239], v[116:119]\n\t" \
    "v_mfma_f32_16x16x32_bf16 v[120:123], v[244:247], v[236:239], v[120:123]\n\t" \
    "v_mfma_f32_16x16x32_bf16 v[124:127], v[252:255], v[236:239], v[124:127]\n\t" \
    "s_waitcnt lgkmcnt(0)\n\t" \
    "v_mfma_f32_16x16x32_bf16 v[0:3], v[192:195], v[160:163], v[0:3]\n\t" \
    "ds_read_b64_tr_b16 v[240:241], %9 offset:40960\n\t" \
    "v_mfma_f32_16x16x32_bf16 v[4:7], v[200:203], v[160:163], v[4:7]\n\t" \
    "ds_read_b64_tr_b16 v[242:243], %9 offset:41472\n\t" \
    "v_mfma_f32_16x16x32_bf16 v[8:11], v[196:199], v[160:163], v[8:11]\n\t" \
    "v_mfma_f32_16x16x32_bf16 v[12:15], v[204:207], v[160:163], v[12:15]\n\t" \
    "ds_read_b64_tr_b16 v[248:249], %9 offset:43008\n\t" \
    "v_mfma_f32_16x16x32_bf16 v[16:19], v[192:195], v[164:167], v[16:19]\n\t" \
    "ds_read_b64_tr_b16 v[250:251], %9 offset:43520\n\t" \
    "v_mfma_f32_16x16x32_bf16 v[20:23], v[200:203], v[164:167], v[20:23]\n\t" \
    "v_mfma_f32_16x16x32_bf16 v[24:27], v[196:199], v[164:167], v[24:27]\n\t" \
    "ds_read_b64_tr_b16 v[244:245], %10 offset:40960\n\t" \
    "v_mfma_f32_16x16x32_bf16 v[28:31], v[204:207], v[164:167], v[28:31]\n\t" \
    "ds_read_b64_tr_b16 v[246:247], %10 offset:41472\n\t" \
    "v_mfma_f32_16x16x32_bf16 v[32:35], v[192:195], v[168:171], v[32:35]\n\t" \
    "v_mfma_f32_16x16x32_bf16 v[36:39], v[200:203], v[168:171], v[36:39]\n\t" \
    "ds_read_b64_tr_b16 v[252:253], %10 offset:43008\n\t" \
    "v_mfma_f32_16x16x32_bf16 v[40:43], v[196:199], v[168:171], v[40:43]\n\t" \
    "ds_read_b64_tr_b16 v[254:255], %10 offset:43520\n\t" \
    "v_mfma_f32_16x16x32_bf16 v[44:47], v[204:207], v[168:171], v[44:47]\n\t" \
    "v_mfma_f32_16x16x32_bf16 v[48:51], v[192:195], v[172:175], v[48:51]\n\t" \
    "ds_read_b128 v[208:211], %4 offset:0\n\t" \
    "v_mfma_f32_16x16x32_bf16 v[52:55], v[200:203], v[172:175], v[52:55]\n\t" \
    "ds_read_b128 v[212:215], %4 offset:2048\n\t" \
    "v_mfma_f32_16x16x32_bf16 v[56:59], v[196:199], v[172:175], v[56:59]\n\t" \
    "v_mfma_f32_16x16x32_bf16 v[60:63], v[204:207], v[172:175], v[60:63]\n\t" \
    "ds_read_b128 v[216:219], %4 offset:4096\n\t" \
    "v_mfma_f32_16x16x32_bf16 v[64:67], v[192:195], v[176:179], v[64:67]\n\t" \
    "ds_read_b128 v[220:223], %4 offset:6144\n\t" \
    "v_mfma_f32_16x16x32_bf16 v[68:71], v[200:203], v[176:179], v[68:71]\n\t" \
    "v_mfma_f32_16x16x32_bf16 v[72:75], v[196:199], v[176:179], v[72:75]\n\t" \
    "ds_read_b128 v[224:227], %4 offset:8192\n\t" \
    "v_mfma_f32_16x16x32_bf16 v[76:79], v[204:207], v[176:179], v[76:79]\n\t" \
    "ds_read_b128 v[228:231], %4 offset:10240\n\t" \
    "v_mfma_f32_16x16x32_bf16 v[80:83], v[192:195], v[180:183], v[80:83]\n\t" \
    "v_mfma_f32_16x16x32_bf16 v[84:87], v[200:203], v[180:183], v[84:87]\n\t" \
    "ds_read_b128 v[232:235], %4 offset:12288\n\t" \
    "v_mfma_f32_16x16x32_bf16 v[88:91], v[196:199], v[180:183], v[88:91]\n\t" \
    "ds_read_b128 v[236:239], %4 offset:14336\n\t" \
    "v_mfma_f32_16x16x32_bf16 v[92:95], v[204:207], v[180:183], v[92:95]\n\t" \
    "v_mfma_f32_16x16x32_bf16 v[96:99], v[192:195], v[184:187], v[96:99]\n\t" \
    "v_mfma_f32_16x16x32_bf16 v[100:103], v[200:203], v[184:187], v[100:103]\n\t" \
    "v_mfma_f32_16x16x32_bf16 v[104:107], v[196:199], v[184:187], v[104:107]\n\t" \
    "v_mfma_f32_16x16x32_bf16 v[108:111], v[204:207], v[184:187], v[108:111]\n\t" \
    "v_mfma_f32_16x16x32_bf16 v[112:115], v[192:195], v[188:191], v[112:115]\n\t" \
    "v_mfma_f32_16x16x32_bf16 v[116:119], v[200:203], v[188:191], v[116:119]\n\t" \
    "v_mfma_f32_16x16x32_bf16 v[120:123], v[196:199], v[188:191], v[120:123]\n\t" \
    "v_mfma_f32_16x16x32_bf16 v[124:127], v[204:207], v[188:191], v[124:127]\n\t" \
    "s_waitcnt lgkmcnt(0)\n\t" \
    "s_barrier\n\t" \
    "v_mfma_f32_16x16x32_bf16 v[0:3], v[240:243], v[208:211], v[0:3]\n\t" \
    "ds_read_b64_tr_b16 v[192:193], %11 offset:32768\n\t" \
    "v_mfma_f32_16x16x32_bf16 v[4:7], v[248:251], v[208:211], v[4:7]\n\t" \
    "ds_read_b64_tr_b16 v[194:195], %11 offset:33280\n\t" \
    "v_mfma_f32_16x16x32_bf16 v[8:11], v[244:247], v[208:211], v[8:11]\n\t" \
    "v_mfma_f32_16x16x32_bf16 v[12:15], v[252:255], v[208:211], v[12:15]\n\t" \
    "ds_read_b64_tr_b16 v[200:201], %11 offset:34816\n\t" \
    "v_mfma_f32_16x16x32_bf16 v[16:19], v[240:243], v[212:215], v[16:19]\n\t" \
    "ds_read_b64_tr_b16 v[202:203], %11 offset:35328\n\t" \
    "v_mfma_f32_16x16x32_bf16 v[20:23], v[248:251], v[212:215], v[20:23]\n\t" \
    "v_mfma_f32_16x16x32_bf16 v[24:27], v[244:247], v[212:215], v[24:27]\n\t" \
    "ds_read_b64_tr_b16 v[196:197], %12 offset:32768\n\t" \
    "v_mfma_f32_16x16x32_bf16 v[28:31], v[252:255], v[212:215], v[28:31]\n\t" \
    "ds_read_b64_tr_b16 v[198:199], %12 offset:33280\n\t" \
    "v_mfma_f32_16x16x32_bf16 v[32:35], v[240:243], v[216:219], v[32:35]\n\t" \
    "v_mfma_f32_16x16x32_bf16 v[36:39], v[248:251], v[216:219], v[36:39]\n\t" \
    "ds_read_b64_tr_b16 v[204:205], %12 offset:34816\n\t" \
    "v_mfma_f32_16x16x32_bf16 v[40:43], v[244:247], v[216:219], v[40:43]\n\t" \
    "ds_read_b64_tr_b16 v[206:207], %12 offset:35328\n\t" \
    "v_mfma_f32_16x16x32_bf16 v[44:47], v[252:255], v[216:219], v[44:47]\n\t" \
    "v_mfma_f32_16x16x32_bf16 v[48:51], v[240:243], v[220:223], v[48:51]\n\t" \
    "ds_read_b128 v[160:163], %5 offset:0\n\t" \
    "v_mfma_f32_16x16x32_bf16 v[52:55], v[248:251], v[220:223], v[52:55]\n\t" \
    "ds_read_b128 v[164:167], %5 offset:2048\n\t" \
    "v_mfma_f32_16x16x32_bf16 v[56:59], v[244:247], v[220:223], v[56:59]\n\t" \
    "v_mfma_f32_16x16x32_bf16 v[60:63], v[252:255], v[220:223], v[60:63]\n\t" \
    "ds_read_b128 v[168:171], %5 offset:4096\n\t" \
    "v_mfma_f32_16x16x32_bf16 v[64:67], v[240:243], v[224:227], v[64:67]\n\t" \
    "ds_read_b128 v[172:175], %5 offset:6144\n\t" \
    "v_mfma_f32_16x16x32_bf16 v[68:71], v[248:251], v[224:227], v[68:71]\n\t" \
    "v_mfma_f32_16x16x32_bf16 v[72:75], v[244:247], v[224:227], v[72:75]\n\t" \
    "ds_read_b128 v[176:179], %5 offset:8192\n\t" \
    "v_mfma_f32_16x16x32_bf16 v[76:79], v[252:255], v[224:227], v[76:79]\n\t" \
    "ds_read_b128 v[180:183], %5 offset:10240\n\t" \
    "v_mfma_f32_16x16x32_bf16 v[80:83], v[240:243], v[228:231], v[80:83]\n\t" \
    "v_mfma_f32_16x16x32_bf16 v[84:87], v[248:251], v[228:231], v[84:87]\n\t" \
    "ds_read_b128 v[184:187], %5 offset:12288\n\t" \
    "v_mfma_f32_16x16x32_bf16 v[88:91], v[244:247], v[228:231], v[88:91]\n\t" \
    "ds_read_b128 v[188:191], %5 offset:14336\n\t" \
    "v_mfma_f32_16x16x32_bf16 v[92:95], v[252:255], v[228:231], v[92:95]\n\t" \
    "v_mfma_f32_16x16x32_bf16 v[96:99], v[240:243], v[232:235], v[96:99]\n\t" \
    "v_mfma_f32_16x16x32_bf16 v[100:103], v[248:251], v[232:235], v[100:103]\n\t" \
    "v_mfma_f32_16x16x32_bf16 v[104:107], v[244:247], v[232:235], v[104:107]\n\t" \
    "v_mfma_f32_16x16x32_bf16 v[108:111], v[252:255], v[232:235], v[108:111]\n\t" \
    "v_mfma_f32_16x16x32_bf16 v[112:115], v[240:243], v[236:239], v[112:115]\n\t" \
    "v_mfma_f32_16x16x32_bf16 v[116:119], v[248:251], v[236:239], v[116:119]\n\t" \
    "v_mfma_f32_16x16x32_bf16 v[120:123], v[244:247], v[236:239], v[120:123]\n\t" \
    "v_mfma_f32_16x16x32_bf16 v[124:127], v[252:255], v[236:239], v[124:127]\n\t" \
    "s_waitcnt lgkmcnt(0)\n\t" \
    "s_sub_u32 %0, %0, 1\n\t" \
    "s_cmp_lg_u32 %0, 0\n\t" \
    "s_cbranch_scc1 LP1_%=\n\t" \
    "v_mfma_f32_16x16x32_bf16 v[0:3], v[192:195], v[160:163], v[0:3]\n\t" \
    "ds_read_b64_tr_b16 v[240:241], %11 offset:40960\n\t" \
    "v_mfma_f32_16x16x32_bf16 v[4:7], v[200:203], v[160:163], v[4:7]\n\t" \
    "ds_read_b64_tr_b16 v[242:243], %11 offset:41472\n\t" \
    "v_mfma_f32_16x16x32_bf16 v[8:11], v[196:199], v[160:163], v[8:11]\n\t" \
    "v_mfma_f32_16x16x32_bf16 v[12:15], v[204:207], v[160:163], v[12:15]\n\t" \
    "ds_read_b64_tr_b16 v[248:249], %11 offset:43008\n\t" \
    "v_mfma_f32_16x16x32_bf16 v[16:19], v[192:195], v[164:167], v[16:19]\n\t" \
    "ds_read_b64_tr_b16 v[250:251], %11 offset:43520\n\t" \
    "v_mfma_f32_16x16x32_bf16 v[20:23], v[200:203], v[164:167], v[20:23]\n\t" \
    "v_mfma_f32_16x16x32_bf16 v[24:27], v[196:199], v[164:167], v[24:27]\n\t" \
    "ds_read_b64_tr_b16 v[244:245], %12 offset:40960\n\t" \
    "v_mfma_f32_16x16x32_bf16 v[28:31], v[204:207], v[164:167], v[28:31]\n\t" \
    "ds_read_b64_tr_b16 v[246:247], %12 offset:41472\n\t" \
    "v_mfma_f32_16x16x32_bf16 v[32:35], v[192:195], v[168:171], v[32:35]\n\t" \
    "v_mfma_f32_16x16x32_bf16 v[36:39], v[200:203], v[168:171], v[36:39]\n\t" \
    "ds_read_b64_tr_b16 v[252:253], %12 offset:43008\n\t" \
    "v_mfma_f32_16x16x32_bf16 v[40:43], v[196:199], v[168:171], v[40:43]\n\t" \
    "ds_read_b64_tr_b16 v[254:255], %12 offset:43520\n\t" \
    "v_mfma_f32_16x16x32_bf16 v[44:47], v[204:207], v[168:171], v[44:47]\n\t" \
    "v_mfma_f32_16x16x32_bf16 v[48:51], v[192:195], v[172:175], v[48:51]\n\t" \
    "ds_read_b128 v[208:211], %6 offset:0\n\t" \
    "v_mfma_f32_16x16x32_bf16 v[52:55], v[200:203], v[172:175], v[52:55]\n\t" \
    "ds_read_b128 v[212:215], %6 offset:2048\n\t" \
    "v_mfma_f32_16x16x32_bf16 v[56:59], v[196:199], v[172:175], v[56:59]\n\t" \
    "v_mfma_f32_16x16x32_bf16 v[60:63], v[204:207], v[172:175], v[60:63]\n\t" \
    "ds_read_b128 v[216:219], %6 offset:4096\n\t" \
    "v_mfma_f32_16x16x32_bf16 v[64:67], v[192:195], v[176:179], v[64:67]\n\t" \
    "ds_read_b128 v[220:223], %6 offset:6144\n\t" \
    "v_mfma_f32_16x16x32_bf16 v[68:71], v[200:203], v[176:179], v[68:71]\n\t" \
    "v_mfma_f32_16x16x32_bf16 v[72:75], v[196:199], v[176:179], v[72:75]\n\t" \
    "ds_read_b128 v[224:227], %6 offset:8192\n\t" \
    "v_mfma_f32_16x16x32_bf16 v[76:79], v[204:207], v[176:179], v[76:79]\n\t" \
    "ds_read_b128 v[228:231], %6 offset:10240\n\t" \
    "v_mfma_f32_16x16x32_bf16 v[80:83], v[192:195], v[180:183], v[80:83]\n\t" \
    "v_mfma_f32_16x16x32_bf16 v[84:87], v[200:203], v[180:183], v[84:87]\n\t" \
    "ds_read_b128 v[232:235], %6 offset:12288\n\t" \
    "v_mfma_f32_16x16x32_bf16 v[88:91], v[196:199], v[180:183], v[88:91]\n\t" \
    "ds_read_b128 v[236:239], %6 offset:14336\n\t" \
    "v_mfma_f32_16x16x32_bf16 v[92:95], v[204:207], v[180:183], v[92:95]\n\t" \
    "v_mfma_f32_16x16x32_bf16 v[96:99], v[192:195], v[184:187], v[96:99]\n\t" \
    "v_mfma_f32_16x16x32_bf16 v[100:103], v[200:203], v[184:187], v[100:103]\n\t" \
    "v_mfma_f32_16x16x32_bf16 v[104:107], v[196:199], v[184:187], v[104:107]\n\t" \
    "v_mfma_f32_16x16x32_bf16 v[108:111], v[204:207], v[184:187], v[108:111]\n\t" \
    "v_mfma_f32_16x16x32_bf16 v[112:115], v[192:195], v[188:191], v[112:115]\n\t" \
    "v_mfma_f32_16x16x32_bf16 v[116:119], v[200:203], v[188:191], v[116:119]\n\t" \
    "v_mfma_f32_16x16x32_bf16 v[120:123], v[196:199], v[188:191], v[120:123]\n\t" \
    "v_mfma_f32_16x16x32_bf16 v[124:127], v[204:207], v[188:191], v[124:127]\n\t" \
    "s_waitcnt lgkmcnt(0)\n\t" \
    "s_barrier\n\t"
#define P1_TAIL \
    "v_mfma_f32_16x16x32_bf16 v[0:3], v[240:243], v[208:211], v[0:3]\n\t" \
    "v_mfma_f32_16x16x32_bf16 v[4:7], v[248:251], v[208:211], v[4:7]\n\t" \
    "v_mfma_f32_16x16x32_bf16 v[8:11], v[244:247], v[208:211], v[8:11]\n\t" \
    "v_mfma_f32_16x16x32_bf16 v[12:15], v[252:255], v[208:211], v[12:15]\n\t" \
    "v_mfma_f32_16x16x32_bf16 v[16:19], v[240:243], v[212:215], v[16:19]\n\t" \
    "v_mfma_f32_16x16x32_bf16 v[20:23], v[248:251], v[212:215], v[20:23]\n\t" \
    "v_mfma_f32_16x16x32_bf16 v[24:27], v[244:247], v[212:215], v[24:27]\n\t" \
    "v_mfma_f32_16x16x32_bf16 v[28:31], v[252:255], v[212:215], v[28:31]\n\t" \
    "v_mfma_f32_16x16x32_bf16 v[32:35], v[240:243], v[216:219], v[32:35]\n\t" \
    "v_mfma_f32_16x16x32_bf16 v[36:39], v[248:251], v[216:219], v[36:39]\n\t" \
    "v_mfma_f32_16x16x32_bf16 v[40:43], v[244:247], v[216:219], v[40:43]\n\t" \
    "v_mfma_f32_16x16x32_bf16 v[44:47], v[252:255], v[216:219], v[44:47]\n\t" \
    "v_mfma_f32_16x16x32_bf16 v[48:51], v[240:243], v[220:223], v[48:51]\n\t" \
    "v_mfma_f32_16x16x32_bf16 v[52:55], v[248:251], v[220:223], v[52:55]\n\t" \
    "v_mfma_f32_16x16x32_bf16 v[56:59], v[244:247], v[220:223], v[56:59]\n\t" \
    "v_mfma_f32_16x16x32_bf16 v[60:63], v[252:255], v[220:223], v[60:63]\n\t" \
    "v_mfma_f32_16x16x32_bf16 v[64:67], v[240:243], v[224:227], v[64:67]\n\t" \
    "v_mfma_f32_16x16x32_bf16 v[68:71], v[248:251], v[224:227], v[68:71]\n\t" \
    "v_mfma_f32_16x16x32_bf16 v[72:75], v[244:247], v[224:227], v[72:75]\n\t" \
    "v_mfma_f32_16x16x32_bf16 v[76:79], v[252:255], v[224:227], v[76:79]\n\t" \
    "v_mfma_f32_16x16x32_bf16 v[80:83], v[240:243], v[228:231], v[80:83]\n\t" \
    "v_mfma_f32_16x16x32_bf16 v[84:87], v[248:251], v[228:231], v[84:87]\n\t" \
    "v_mfma_f32_16x16x32_bf16 v[88:91], v[244:247], v[228:231], v[88:91]\n\t" \
    "v_mfma_f32_16x16x32_bf16 v[92:95], v[252:255], v[228:231], v[92:95]\n\t" \
    "v_mfma_f32_16x16x32_bf16 v[96:99], v[240:243], v[232:235], v[96:99]\n\t" \
    "v_mfma_f32_16x16x32_bf16 v[100:103], v[248:251], v[232:235], v[100:103]\n\t" \
    "v_mfma_f32_16x16x32_bf16 v[104:107], v[244:247], v[232:235], v[104:107]\n\t" \
    "v_mfma_f32_16x16x32_bf16 v[108:111], v[252:255], v[232:235], v[108:111]\n\t" \
    "v_mfma_f32_16x16x32_bf16 v[112:115], v[240:243], v[236:239], v[112:115]\n\t" \
    "v_mfma_f32_16x16x32_bf16 v[116:119], v[248:251], v[236:239], v[116:119]\n\t" \
    "v_mfma_f32_16x16x32_bf16 v[120:123], v[244:247], v[236:239], v[120:123]\n\t" \
    "v_mfma_f32_16x16x32_bf16 v[124:127], v[252:255], v[236:239], v[124:127]\n\t" \
    "s_nop 7\n\t" \
    "s_nop 1\n\t" \
    "v_pk_mul_f32 v[160:161], v[0:1], v[0:1]\n\t" \
    "v_pk_mul_f32 v[162:163], v[2:3], v[2:3]\n\t" \
    "v_pk_mul_f32 v[164:165], v[8:9], v[8:9]\n\t" \
    "v_pk_mul_f32 v[166:167], v[10:11], v[10:11]\n\t" \
    "v_pk_fma_f32 v[160:161], v[160:161], %3, %2 op_sel_hi:[1,0,0]\n\t" \
    "v_pk_fma_f32 v[162:163], v[162:163], %3, %2 op_sel_hi:[1,0,0]\n\t" \
    "v_pk_fma_f32 v[164:165], v[164:165], %3, %2 op_sel_hi:[1,0,0]\n\t" \
    "v_pk_fma_f32 v[166:167], v[166:167], %3, %2 op_sel_hi:[1,0,0]\n\t" \
    "v_pk_mul_f32 v[160:161], v[160:161], v[0:1]\n\t" \
    "v_pk_mul_f32 v[162:163], v[162:163], v[2:3]\n\t" \
    "v_pk_mul_f32 v[164:165], v[164:165], v[8:9]\n\t" \
    "v_pk_mul_f32 v[166:167], v[166:167], v[10:11]\n\t" \
    "v_pk_mul_f32 v[4:5], v[0:1], v[4:5]\n\t" \
    "v_pk_mul_f32 v[6:7], v[2:3], v[6:7]\n\t" \
    "v_pk_mul_f32 v[12:13], v[8:9], v[12:13]\n\t" \
    "v_pk_mul_f32 v[14:15], v[10:11], v[14:15]\n\t" \
    "v_exp_f32 v160, v160\n\t" \
    "v_exp_f32 v161, v161\n\t" \
    "v_exp_f32 v162, v162\n\t" \
    "v_exp_f32 v163, v163\n\t" \
    "v_exp_f32 v164, v164\n\t" \
    "v_exp_f32 v165, v165\n\t" \
    "v_exp_f32 v166, v166\n\t" \
    "v_exp_f32 v167, v167\n\t" \
    "v_pk_add_f32 v[160:161], v[160:161], 1.0 op_sel_hi:[1,0]\n\t" \
    "v_pk_add_f32 v[162:163], v[162:163], 1.0 op_sel_hi:[1,0]\n\t" \
    "v_pk_add_f32 v[164:165], v[164:165], 1.0 op_sel_hi:[1,0]\n\t" \
    "v_pk_add_f32 v[166:167], v[166:167], 1.0 op_sel_hi:[1,0]\n\t" \
    "v_rcp_f32 v160, v160\n\t" \
    "v_rcp_f32 v161, v161\n\t" \
    "v_rcp_f32 v162, v162\n\t" \
    "v_rcp_f32 v163, v163\n\t" \
    "v_rcp_f32 v164, v164\n\t" \
    "v_rcp_f32 v165, v165\n\t" \
    "v_rcp_f32 v166, v166\n\t" \
    "v_rcp_f32 v167, v167\n\t" \
    "v_pk_mul_f32 v[4:5], v[4:5], v[160:161]\n\t" \
    "v_pk_mul_f32 v[6:7], v[6:7], v[162:163]\n\t" \
    "v_pk_mul_f32 v[12:13], v[12:13], v[164:165]\n\t" \
    "v_pk_mul_f32 v[14:15], v[14:15], v[166:167]\n\t" \
    "v_cvt_pk_bf16_f32 v168, v4, v5\n\t" \
    "v_cvt_pk_bf16_f32 v169, v6, v7\n\t" \
    "v_cvt_pk_bf16_f32 v170, v12, v13\n\t" \
    "v_cvt_pk_bf16_f32 v171, v14, v15\n\t" \
    "v_pk_mul_f32 v[160:161], v[16:17], v[16:17]\n\t" \
    "v_pk_mul_f32 v[162:163], v[18:19], v[18:19]\n\t" \
    "v_permlane16_swap_b32 v168, v170\n\t" \
    "v_permlane16_swap_b32 v169, v171\n\t" \
    "buffer_store_dwordx4 v[168:171], %1, %4, %0 offen sc1\n\t" \
    "s_add_u32 %0, %0, 55296\n\t" \
    "v_pk_mul_f32 v[164:165], v[24:25], v[24:25]\n\t" \
    "v_pk_mul_f32 v[166:167], v[26:27], v[26:27]\n\t" \
    "v_pk_fma_f32 v[160:161], v[160:161], %3, %2 op_sel_hi:[1,0,0]\n\t" \
    "v_pk_fma_f32 v[162:163], v[162:163], %3, %2 op_sel_hi:[1,0,0]\n\t" \
    "v_pk_fma_f32 v[164:165], v[164:165], %3, %2 op_sel_hi:[1,0,0]\n\t" \
    "v_pk_fma_f32 v[166:167], v[166:167], %3, %2 op_sel_hi:[1,0,0]\n\t" \
    "v_pk_mul_f32 v[160:161], v[160:161], v[16:17]\n\t" \
    "v_pk_mul_f32 v[162:163], v[162:163], v[18:19]\n\t" \
    "v_pk_mul_f32 v[164:165], v[164:165], v[24:25]\n\t" \
    "v_pk_mul_f32 v[166:167], v[166:167], v[26:27]\n\t" \
    "v_pk_mul_f32 v[20:21], v[16:17], v[20:21]\n\t" \
    "v_pk_mul_f32 v[22:23], v[18:19], v[22:23]\n\t" \
    "v_pk_mul_f32 v[28:29], v[24:25], v[28:29]\n\t" \
    "v_pk_mul_f32 v[30:31], v[26:27], v[30:31]\n\t" \
    "v_exp_f32 v160, v160\n\t" \
    "v_exp_f32 v161, v161\n\t" \
    "v_exp_f32 v162, v162\n\t" \
    "v_exp_f32 v163, v163\n\t" \
    "v_exp_f32 v164, v164\n\t" \
    "v_exp_f32 v165, v165\n\t" \
    "v_exp_f32 v166, v166\n\t" \
    "v_exp_f32 v167, v167\n\t" \
    "v_pk_add_f32 v[160:161], v[160:161], 1.0 op_sel_hi:[1,0]\n\t" \
    "v_pk_add_f32 v[162:163], v[162:163], 1.0 op_sel_hi:[1,0]\n\t" \
    "v_pk_add_f32 v[164:165], v[164:165], 1.0 op_sel_hi:[1,0]\n\t" \
    "v_pk_add_f32 v[166:167], v[166:167], 1.0 op_sel_hi:[1,0]\n\t" \
    "v_rcp_f32 v160, v160\n\t" \
    "v_rcp_f32 v161, v161\n\t" \
    "v_rcp_f32 v162, v162\n\t" \
    "v_rcp_f32 v163, v163\n\t" \
    "v_rcp_f32 v164, v164\n\t" \
    "v_rcp_f32 v165, v165\n\t" \
    "v_rcp_f32 v166, v166\n\t" \
    "v_rcp_f32 v167, v167\n\t" \
    "v_pk_mul_f32 v[20:21], v[20:21], v[160:161]\n\t" \
    "v_pk_mul_f32 v[22:23], v[22:23], v[162:163]\n\t" \
    "v_pk_mul_f32 v[28:29], v[28:29], v[164:165]\n\t" \
    "v_pk_mul_f32 v[30:31], v[30:31], v[166:167]\n\t" \
    "v_cvt_pk_bf16_f32 v172, v20, v21\n\t" \
    "v_cvt_pk_bf16_f32 v173, v22, v23\n\t" \
    "v_cvt_pk_bf16_f32 v174, v28, v29\n\t" \
    "v_cvt_pk_bf16_f32 v175, v30, v31\n\t" \
    "v_pk_mul_f32 v[160:161], v[32:33], v[32:33]\n\t" \
    "v_pk_mul_f32 v[162:163], v[34:35], v[34:35]\n\t" \
    "v_permlane16_swap_b32 v172, v174\n\t" \
    "v_permlane16_swap_b32 v173, v175\n\t" \
    "buffer_store_dwordx4 v[172:175], %1, %4, %0 offen sc1\n\t" \
    "s_add_u32 %0, %0, 55296\n\t" \
    "v_pk_mul_f32 v[164:165], v[40:41], v[40:41]\n\t" \
    "v_pk_mul_f32 v[166:167], v[42:43], v[42:43]\n\t" \
    "v_pk_fma_f32 v[160:161], v[160:161], %3, %2 op_sel_hi:[1,0,0]\n\t" \
    "v_pk_fma_f32 v[162:163], v[162:163], %3, %2 op_sel_hi:[1,0,0]\n\t" \
    "v_pk_fma_f32 v[164:165], v[164:165], %3, %2 op_sel_hi:[1,0,0]\n\t" \
    "v_pk_fma_f32 v[166:167], v[166:167], %3, %2 op_sel_hi:[1,0,0]\n\t" \
    "v_pk_mul_f32 v[160:161], v[160:161], v[32:33]\n\t" \
    "v_pk_mul_f32 v[162:163], v[162:163], v[34:35]\n\t" \
    "v_pk_mul_f32 v[164:165], v[164:165], v[40:41]\n\t" \
    "v_pk_mul_f32 v[166:167], v[166:167], v[42:43]\n\t" \
    "v_pk_mul_f32 v[36:37], v[32:33], v[36:37]\n\t" \
    "v_pk_mul_f32 v[38:39], v[34:35], v[38:39]\n\t" \
    "v_pk_mul_f32 v[44:45], v[40:41], v[44:45]\n\t" \
    "v_pk_mul_f32 v[46:47], v[42:43], v[46:47]\n\t" \
    "v_exp_f32 v160, v160\n\t" \
    "v_exp_f32 v161, v161\n\t" \
    "v_exp_f32 v162, v162\n\t" \
    "v_exp_f32 v163, v163\n\t" \
    "v_exp_f32 v164, v164\n\t" \
    "v_exp_f32 v165, v165\n\t" \
    "v_exp_f32 v166, v166\n\t" \
    "v_exp_f32 v167, v167\n\t" \
    "v_pk_add_f32 v[160:161], v[160:161], 1.0 op_sel_hi:[1,0]\n\t" \
    "v_pk_add_f32 v[162:163], v[162:163], 1.0 op_sel_hi:[1,0]\n\t" \
    "v_pk_add_f32 v[164:165], v[164:165], 1.0 op_sel_hi:[1,0]\n\t" \
    "v_pk_add_f32 v[166:167], v[166:167], 1.0 op_sel_hi:[1,0]\n\t" \
    "v_rcp_f32 v160, v160\n\t" \
    "v_rcp_f32 v161, v161\n\t" \
    "v_rcp_f32 v162, v162\n\t" \
    "v_rcp_f32 v163, v163\n\t" \
    "v_rcp_f32 v164, v164\n\t" \
    "v_rcp_f32 v165, v165\n\t" \
    "v_rcp_f32 v166, v166\n\t" \
    "v_rcp_f32 v167, v167\n\t" \
    "v_pk_mul_f32 v[36:37], v[36:37], v[160:161]\n\t" \
    "v_pk_mul_f32 v[38:39], v[38:39], v[162:163]\n\t" \
    "v_pk_mul_f32 v[44:45], v[44:45], v[164:165]\n\t" \
    "v_pk_mul_f32 v[46:47], v[46:47], v[166:167]\n\t" \
    "v_cvt_pk_bf16_f32 v168, v36, v37\n\t" \
    "v_cvt_pk_bf16_f32 v169, v38, v39\n\t" \
    "v_cvt_pk_bf16_f32 v170, v44, v45\n\t" \
    "v_cvt_pk_bf16_f32 v171, v46, v47\n\t" \
    "v_pk_mul_f32 v[160:161], v[48:49], v[48:49]\n\t" \
    "v_pk_mul_f32 v[162:163], v[50:51], v[50:51]\n\t" \
    "v_permlane16_swap_b32 v168, v170\n\t" \
    "v_permlane16_swap_b32 v169, v171\n\t" \
    "buffer_store_dwordx4 v[168:171], %1, %4, %0 offen sc1\n\t" \
    "s_add_u32 %0, %0, 55296\n\t" \
    "v_pk_mul_f32 v[164:165], v[56:57], v[56:57]\n\t" \
    "v_pk_mul_f32 v[166:167], v[58:59], v[58:59]\n\t" \
    "v_pk_fma_f32 v[160:161], v[160:161], %3, %2 op_sel_hi:[1,0,0]\n\t" \
    "v_pk_fma_f32 v[162:163], v[162:163], %3, %2 op_sel_hi:[1,0,0]\n\t" \
    "v_pk_fma_f32 v[164:165], v[164:165], %3, %2 op_sel_hi:[1,0,0]\n\t" \
    "v_pk_fma_f32 v[166:167], v[166:167], %3, %2 op_sel_hi:[1,0,0]\n\t" \
    "v_pk_mul_f32 v[160:161], v[160:161], v[48:49]\n\t" \
    "v_pk_mul_f32 v[162:163], v[162:163], v[50:51]\n\t" \
    "v_pk_mul_f32 v[164:165], v[164:165], v[56:57]\n\t" \
    "v_pk_mul_f32 v[166:167], v[166:167], v[58:59]\n\t" \
    "v_pk_mul_f32 v[52:53], v[48:49], v[52:53]\n\t" \
    "v_pk_mul_f32 v[54:55], v[50:51], v[54:55]\n\t" \
    "v_pk_mul_f32 v[60:61], v[56:57], v[60:61]\n\t" \
    "v_pk_mul_f32 v[62:63], v[58:59], v[62:63]\n\t" \
    "v_exp_f32 v160, v160\n\t" \
    "v_exp_f32 v161, v161\n\t" \
    "v_exp_f32 v162, v162\n\t" \
    "v_exp_f32 v163, v163\n\t" \
    "v_exp_f32 v164, v164\n\t" \
    "v_exp_f32 v165, v165\n\t" \
    "v_exp_f32 v166, v166\n\t" \
    "v_exp_f32 v167, v167\n\t" \
    "v_pk_add_f32 v[160:161], v[160:161], 1.0 op_sel_hi:[1,0]\n\t" \
    "v_pk_add_f32 v[162:163], v[162:163], 1.0 op_sel_hi:[1,0]\n\t" \
    "v_pk_add_f32 v[164:165], v[164:165], 1.0 op_sel_hi:[1,0]\n\t" \
    "v_pk_add_f32 v[166:167], v[166:167], 1.0 op_sel_hi:[1,0]\n\t" \
    "v_rcp_f32 v160, v160\n\t" \
    "v_rcp_f32 v161, v161\n\t" \
    "v_rcp_f32 v162, v162\n\t" \
    "v_rcp_f32 v163, v163\n\t" \
    "v_rcp_f32 v164, v164\n\t" \
    "v_rcp_f32 v165, v165\n\t" \
    "v_rcp_f32 v166, v166\n\t" \
    "v_rcp_f32 v167, v167\n\t" \
    "v_pk_mul_f32 v[52:53], v[52:53], v[160:161]\n\t" \
    "v_pk_mul_f32 v[54:55], v[54:55], v[162:163]\n\t" \
    "v_pk_mul_f32 v[60:61], v[60:61], v[164:165]\n\t" \
    "v_pk_mul_f32 v[62:63], v[62:63], v[166:167]\n\t" \
    "v_cvt_pk_bf16_f32 v172, v52, v53\n\t" \
    "v_cvt_pk_bf16_f32 v173, v54, v55\n\t" \
    "v_cvt_pk_bf16_f32 v174, v60, v61\n\t" \
    "v_cvt_pk_bf16_f32 v175, v62, v63\n\t" \
    "v_pk_mul_f32 v[160:161], v[64:65], v[64:65]\n\t" \
    "v_pk_mul_f32 v[162:163], v[66:67], v[66:67]\n\t" \
    "v_permlane16_swap_b32 v172, v174\n\t" \
    "v_permlane16_swap_b32 v173, v175\n\t" \
    "buffer_store_dwordx4 v[172:175], %1, %4, %0 offen sc1\n\t" \
    "s_add_u32 %0, %0, 55296\n\t" \
    "v_pk_mul_f32 v[164:165], v[72:73], v[72:73]\n\t" \
    "v_pk_mul_f32 v[166:167], v[74:75], v[74:75]\n\t" \
    "v_pk_fma_f32 v[160:161], v[160:161], %3, %2 op_sel_hi:[1,0,0]\n\t" \
    "v_pk_fma_f32 v[162:163], v[162:163], %3, %2 op_sel_hi:[1,0,0]\n\t" \
    "v_pk_fma_f32 v[164:165], v[164:165], %3, %2 op_sel_hi:[1,0,0]\n\t" \
    "v_pk_fma_f32 v[166:167], v[166:167], %3, %2 op_sel_hi:[1,0,0]\n\t" \
    "v_pk_mul_f32 v[160:161], v[160:161], v[64:65]\n\t" \
    "v_pk_mul_f32 v[162:163], v[162:163], v[66:67]\n\t" \
    "v_pk_mul_f32 v[164:165], v[164:165], v[72:73]\n\t" \
    "v_pk_mul_f32 v[166:167], v[166:167], v[74:75]\n\t" \
    "v_pk_mul_f32 v[68:69], v[64:65], v[68:69]\n\t" \
    "v_pk_mul_f32 v[70:71], v[66:67], v[70:71]\n\t" \
    "v_pk_mul_f32 v[76:77], v[72:73], v[76:77]\n\t" \
    "v_pk_mul_f32 v[78:79], v[74:75], v[78:79]\n\t" \
    "v_exp_f32 v160, v160\n\t" \
    "v_exp_f32 v161, v161\n\t" \
    "v_exp_f32 v162, v162\n\t" \
    "v_exp_f32 v163, v163\n\t" \
    "v_exp_f32 v164, v164\n\t" \
    "v_exp_f32 v165, v165\n\t" \
    "v_exp_f32 v166, v166\n\t" \
    "v_exp_f32 v167, v167\n\t" \
    "v_pk_add_f32 v[160:161], v[160:161], 1.0 op_sel_hi:[1,0]\n\t" \
    "v_pk_add_f32 v[162:163], v[162:163], 1.0 op_sel_hi:[1,0]\n\t" \
    "v_pk_add_f32 v[164:165], v[164:165], 1.0 op_sel_hi:[1,0]\n\t" \
    "v_pk_add_f32 v[166:167], v[166:167], 1.0 op_sel_hi:[1,0]\n\t" \
    "v_rcp_f32 v160, v160\n\t" \
    "v_rcp_f32 v161, v161\n\t" \
    "v_rcp_f32 v162, v162\n\t" \
    "v_rcp_f32 v163, v163\n\t" \
    "v_rcp_f32 v164, v164\n\t" \
    "v_rcp_f32 v165, v165\n\t" \
    "v_rcp_f32 v166, v166\n\t" \
    "v_rcp_f32 v167, v167\n\t" \
    "v_pk_mul_f32 v[68:69], v[68:69], v[160:161]\n\t" \
    "v_pk_mul_f32 v[70:71], v[70:71], v[162:163]\n\t" \
    "v_pk_mul_f32 v[76:77], v[76:77], v[164:165]\n\t" \
    "v_pk_mul_f32 v[78:79], v[78:79], v[166:167]\n\t" \
    "v_cvt_pk_bf16_f32 v168, v68, v69\n\t" \
    "v_cvt_pk_bf16_f32 v169, v70, v71\n\t" \
    "v_cvt_pk_bf16_f32 v170, v76, v77\n\t" \
    "v_cvt_pk_bf16_f32 v171, v78, v79\n\t" \
    "v_pk_mul_f32 v[160:161], v[80:81], v[80:81]\n\t" \
    "v_pk_mul_f32 v[162:163], v[82:83], v[82:83]\n\t" \
    "v_permlane16_swap_b32 v168, v170\n\t" \
    "v_permlane16_swap_b32 v169, v171\n\t" \
    "buffer_store_dwordx4 v[168:171], %1, %4, %0 offen sc1\n\t" \
    "s_add_u32 %0, %0, 55296\n\t" \
    "v_pk_mul_f32 v[164:165], v[88:89], v[88:89]\n\t" \
    "v_pk_mul_f32 v[166:167], v[90:91], v[90:91]\n\t" \
    "v_pk_fma_f32 v[160:161], v[160:161], %3, %2 op_sel_hi:[1,0,0]\n\t" \
    "v_pk_fma_f32 v[162:163], v[162:163], %3, %2 op_sel_hi:[1,0,0]\n\t" \
    "v_pk_fma_f32 v[164:165], v[164:165], %3, %2 op_sel_hi:[1,0,0]\n\t" \
    "v_pk_fma_f32 v[166:167], v[166:167], %3, %2 op_sel_hi:[1,0,0]\n\t" \
    "v_pk_mul_f32 v[160:161], v[160:161], v[80:81]\n\t" \
    "v_pk_mul_f32 v[162:163], v[162:163], v[82:83]\n\t" \
    "v_pk_mul_f32 v[164:165], v[164:165], v[88:89]\n\t" \
    "v_pk_mul_f32 v[166:167], v[166:167], v[90:91]\n\t" \
    "v_pk_mul_f32 v[84:85], v[80:81], v[84:85]\n\t" \
    "v_pk_mul_f32 v[86:87], v[82:83], v[86:87]\n\t" \
    "v_pk_mul_f32 v[92:93], v[88:89], v[92:93]\n\t" \
    "v_pk_mul_f32 v[94:95], v[90:91], v[94:95]\n\t" \
    "v_exp_f32 v160, v160\n\t" \
    "v_exp_f32 v161, v161\n\t" \
    "v_exp_f32 v162, v162\n\t" \
    "v_exp_f32 v163, v163\n\t" \
    "v_exp_f32 v164, v164\n\t" \
    "v_exp_f32 v165, v165\n\t" \
    "v_exp_f32 v166, v166\n\t" \
    "v_exp_f32 v167, v167\n\t" \
    "v_pk_add_f32 v[160:161], v[160:161], 1.0 op_sel_hi:[1,0]\n\t" \
    "v_pk_add_f32 v[162:163], v[162:163], 1.0 op_sel_hi:[1,0]\n\t" \
    "v_pk_add_f32 v[164:165], v[164:165], 1.0 op_sel_hi:[1,0]\n\t" \
    "v_pk_add_f32 v[166:167], v[166:167], 1.0 op_sel_hi:[1,0]\n\t" \
    "v_rcp_f32 v160, v160\n\t" \
    "v_rcp_f32 v161, v161\n\t" \
    "v_rcp_f32 v162, v162\n\t" \
    "v_rcp_f32 v163, v163\n\t" \
    "v_rcp_f32 v164, v164\n\t" \
    "v_rcp_f32 v165, v165\n\t" \
    "v_rcp_f32 v166, v166\n\t" \
    "v_rcp_f32 v167, v167\n\t" \
    "v_pk_mul_f32 v[84:85], v[84:85], v[160:161]\n\t" \
    "v_pk_mul_f32 v[86:87], v[86:87], v[162:163]\n\t" \
    "v_pk_mul_f32 v[92:93], v[92:93], v[164:165]\n\t" \
    "v_pk_mul_f32 v[94:95], v[94:95], v[166:167]\n\t" \
    "v_cvt_pk_bf16_f32 v172, v84, v85\n\t" \
    "v_cvt_pk_bf16_f32 v173, v86, v87\n\t" \
    "v_cvt_pk_bf16_f32 v174, v92, v93\n\t" \
    "v_cvt_pk_bf16_f32 v175, v94, v95\n\t" \
    "v_pk_mul_f32 v[160:161], v[96:97], v[96:97]\n\t" \
    "v_pk_mul_f32 v[162:163], v[98:99], v[98:99]\n\t" \
    "v_permlane16_swap_b32 v172, v174\n\t" \
    "v_permlane16_swap_b32 v173, v175\n\t" \
    "buffer_store_dwordx4 v[172:175], %1, %4, %0 offen sc1\n\t" \
    "s_add_u32 %0, %0, 55296\n\t" \
    "v_pk_mul_f32 v[164:165], v[104:105], v[104:105]\n\t" \
    "v_pk_mul_f32 v[166:167], v[106:107], v[106:107]\n\t" \
    "v_pk_fma_f32 v[160:161], v[160:161], %3, %2 op_sel_hi:[1,0,0]\n\t" \
    "v_pk_fma_f32 v[162:163], v[162:163], %3, %2 op_sel_hi:[1,0,0]\n\t" \
    "v_pk_fma_f32 v[164:165], v[164:165], %3, %2 op_sel_hi:[1,0,0]\n\t" \
    "v_pk_fma_f32 v[166:167], v[166:167], %3, %2 op_sel_hi:[1,0,0]\n\t" \
    "v_pk_mul_f32 v[160:161], v[160:161], v[96:97]\n\t" \
    "v_pk_mul_f32 v[162:163], v[162:163], v[98:99]\n\t" \
    "v_pk_mul_f32 v[164:165], v[164:165], v[104:105]\n\t" \
    "v_pk_mul_f32 v[166:167], v[166:167], v[106:107]\n\t" \
    "v_pk_mul_f32 v[100:101], v[96:97], v[100:101]\n\t" \
    "v_pk_mul_f32 v[102:103], v[98:99], v[102:103]\n\t" \
    "v_pk_mul_f32 v[108:109], v[104:105], v[108:109]\n\t" \
    "v_pk_mul_f32 v[110:111], v[106:107], v[110:111]\n\t" \
    "v_exp_f32 v160, v160\n\t" \
    "v_exp_f32 v161, v161\n\t" \
    "v_exp_f32 v162, v162\n\t" \
    "v_exp_f32 v163, v163\n\t" \
    "v_exp_f32 v164, v164\n\t" \
    "v_exp_f32 v165, v165\n\t" \
    "v_exp_f32 v166, v166\n\t" \
    "v_exp_f32 v167, v167\n\t" \
    "v_pk_add_f32 v[160:161], v[160:161], 1.0 op_sel_hi:[1,0]\n\t" \
    "v_pk_add_f32 v[162:163], v[162:163], 1.0 op_sel_hi:[1,0]\n\t" \
    "v_pk_add_f32 v[164:165], v[164:165], 1.0 op_sel_hi:[1,0]\n\t" \
    "v_pk_add_f32 v[166:167], v[166:167], 1.0 op_sel_hi:[1,0]\n\t" \
    "v_rcp_f32 v160, v160\n\t" \
    "v_rcp_f32 v161, v161\n\t" \
    "v_rcp_f32 v162, v162\n\t" \
    "v_rcp_f32 v163, v163\n\t" \
    "v_rcp_f32 v164, v164\n\t" \
    "v_rcp_f32 v165, v165\n\t" \
    "v_rcp_f32 v166, v166\n\t" \
    "v_rcp_f32 v167, v167\n\t" \
    "v_pk_mul_f32 v[100:101], v[100:101], v[160:161]\n\t" \
    "v_pk_mul_f32 v[102:103], v[102:103], v[162:163]\n\t" \
    "v_pk_mul_f32 v[108:109], v[108:109], v[164:165]\n\t" \
    "v_pk_mul_f32 v[110:111], v[110:111], v[166:167]\n\t" \
    "v_cvt_pk_bf16_f32 v168, v100, v101\n\t" \
    "v_cvt_pk_bf16_f32 v169, v102, v103\n\t" \
    "v_cvt_pk_bf16_f32 v170, v108, v109\n\t" \
    "v_cvt_pk_bf16_f32 v171, v110, v111\n\t" \
    "v_pk_mul_f32 v[160:161], v[112:113], v[112:113]\n\t" \
    "v_pk_mul_f32 v[162:163], v[114:115], v[114:115]\n\t" \
    "v_permlane16_swap_b32 v168, v170\n\t" \
    "v_permlane16_swap_b32 v169, v171\n\t" \
    "buffer_store_dwordx4 v[168:171], %1, %4, %0 offen sc1\n\t" \
    "s_add_u32 %0, %0, 55296\n\t" \
    "v_pk_mul_f32 v[164:165], v[120:121], v[120:121]\n\t" \
    "v_pk_mul_f32 v[166:167], v[122:123], v[122:123]\n\t" \
    "v_pk_fma_f32 v[160:161], v[160:161], %3, %2 op_sel_hi:[1,0,0]\n\t" \
    "v_pk_fma_f32 v[162:163], v[162:163], %3, %2 op_sel_hi:[1,0,0]\n\t" \
    "v_pk_fma_f32 v[164:165], v[164:165], %3, %2 op_sel_hi:[1,0,0]\n\t" \
    "v_pk_fma_f32 v[166:167], v[166:167], %3, %2 op_sel_hi:[1,0,0]\n\t" \
    "v_pk_mul_f32 v[160:161], v[160:161], v[112:113]\n\t" \
    "v_pk_mul_f32 v[162:163], v[162:163], v[114:115]\n\t" \
    "v_pk_mul_f32 v[164:165], v[164:165], v[120:121]\n\t" \
    "v_pk_mul_f32 v[166:167], v[166:167], v[122:123]\n\t" \
    "v_pk_mul_f32 v[116:117], v[112:113], v[116:117]\n\t" \
    "v_pk_mul_f32 v[118:119], v[114:115], v[118:119]\n\t" \
    "v_pk_mul_f32 v[124:125], v[120:121], v[124:125]\n\t" \
    "v_pk_mul_f32 v[126:127], v[122:123], v[126:127]\n\t" \
    "v_exp_f32 v160, v160\n\t" \
    "v_exp_f32 v161, v161\n\t" \
    "v_exp_f32 v162, v162\n\t" \
    "v_exp_f32 v163, v163\n\t" \
    "v_exp_f32 v164, v164\n\t" \
    "v_exp_f32 v165, v165\n\t" \
    "v_exp_f32 v166, v166\n\t" \
    "v_exp_f32 v167, v167\n\t" \
    "v_pk_add_f32 v[160:161], v[160:161], 1.0 op_sel_hi:[1,0]\n\t" \
    "v_pk_add_f32 v[162:163], v[162:163], 1.0 op_sel_hi:[1,0]\n\t" \
    "v_pk_add_f32 v[164:165], v[164:165], 1.0 op_sel_hi:[1,0]\n\t" \
    "v_pk_add_f32 v[166:167], v[166:167], 1.0 op_sel_hi:[1,0]\n\t" \
    "v_rcp_f32 v160, v160\n\t" \
    "v_rcp_f32 v161, v161\n\t" \
    "v_rcp_f32 v162, v162\n\t" \
    "v_rcp_f32 v163, v163\n\t" \
    "v_rcp_f32 v164, v164\n\t" \
    "v_rcp_f32 v165, v165\n\t" \
    "v_rcp_f32 v166, v166\n\t" \
    "v_rcp_f32 v167, v167\n\t" \
    "v_pk_mul_f32 v[116:117], v[116:117], v[160:161]\n\t" \
    "v_pk_mul_f32 v[118:119], v[118:119], v[162:163]\n\t" \
    "v_pk_mul_f32 v[124:125], v[124:125], v[164:165]\n\t" \
    "v_pk_mul_f32 v[126:127], v[126:127], v[166:167]\n\t" \
    "v_cvt_pk_bf16_f32 v172, v116, v117\n\t" \
    "v_cvt_pk_bf16_f32 v173, v118, v119\n\t" \
    "v_cvt_pk_bf16_f32 v174, v124, v125\n\t" \
    "v_cvt_pk_bf16_f32 v175, v126, v127\n\t" \
    "s_nop 1\n\t" \
    "v_permlane16_swap_b32 v172, v174\n\t" \
    "v_permlane16_swap_b32 v173, v175\n\t" \
    "buffer_store_dwordx4 v[172:175], %1, %4, %0 offen sc1\n\t" \
    "s_add_u32 %0, %0, 55296\n\t"
#define P2_WLOAD \
    "ds_read_b64_tr_b16 v[96:97], %0 offset:0\n\t" \
    "ds_read_b64_tr_b16 v[98:99], %0 offset:512\n\t" \
    "ds_read_b64_tr_b16 v[100:101], %1 offset:0\n\t" \
    "ds_read_b64_tr_b16 v[102:103], %1 offset:512\n\t" \
    "ds_read_b64_tr_b16 v[104:105], %0 offset:4096\n\t" \
    "ds_read_b64_tr_b16 v[106:107], %0 offset:4608\n\t" \
    "ds_read_b64_tr_b16 v[108:109], %1 offset:4096\n\t" \
    "ds_read_b64_tr_b16 v[110:111], %1 offset:4608\n\t" \
    "ds_read_b64_tr_b16 v[112:113], %0 offset:8192\n\t" \
    "ds_read_b64_tr_b16 v[114:115], %0 offset:8704\n\t" \
    "ds_read_b64_tr_b16 v[116:117], %1 offset:8192\n\t" \
    "ds_read_b64_tr_b16 v[118:119], %1 offset:8704\n\t" \
    "ds_read_b64_tr_b16 v[120:121], %0 offset:12288\n\t" \
    "ds_read_b64_tr_b16 v[122:123], %0 offset:12800\n\t" \
    "ds_read_b64_tr_b16 v[124:125], %1 offset:12288\n\t" \
    "ds_read_b64_tr_b16 v[126:127], %1 offset:12800\n\t" \
    "ds_read_b64_tr_b16 v[128:129], %0 offset:16384\n\t" \
    "ds_read_b64_tr_b16 v[130:131], %0 offset:16896\n\t" \
    "ds_read_b64_tr_b16 v[132:133], %1 offset:16384\n\t" \
    "ds_read_b64_tr_b16 v[134:135], %1 offset:16896\n\t" \
    "ds_read_b64_tr_b16 v[136:137], %0 offset:20480\n\t" \
    "ds_read_b64_tr_b16 v[138:139], %0 offset:20992\n\t" \
    "ds_read_b64_tr_b16 v[140:141], %1 offset:20480\n\t" \
    "ds_read_b64_tr_b16 v[142:143], %1 offset:20992\n\t" \
    "ds_read_b64_tr_b16 v[144:145], %0 offset:24576\n\t" \
    "ds_read_b64_tr_b16 v[146:147], %0 offset:25088\n\t" \
    "ds_read_b64_tr_b16 v[148:149], %1 offset:24576\n\t" \
    "ds_read_b64_tr_b16 v[150:151], %1 offset:25088\n\t" \
    "ds_read_b64_tr_b16 v[152:153], %0 offset:28672\n\t" \
    "ds_read_b64_tr_b16 v[154:155], %0 offset:29184\n\t" \
    "ds_read_b64_tr_b16 v[156:157], %1 offset:28672\n\t" \
    "ds_read_b64_tr_b16 v[158:159], %1 offset:29184\n\t" \
    "ds_read_b64_tr_b16 v[160:161], %0 offset:32768\n\t" \
    "ds_read_b64_tr_b16 v[162:163], %0 offset:33280\n\t" \
    "ds_read_b64_tr_b16 v[164:165], %1 offset:32768\n\t" \
    "ds_read_b64_tr_b16 v[166:167], %1 offset:33280\n\t" \
    "ds_read_b64_tr_b16 v[168:169], %0 offset:36864\n\t" \
    "ds_read_b64_tr_b16 v[170:171], %0 offset:37376\n\t" \
    "ds_read_b64_tr_b16 v[172:173], %1 offset:36864\n\t" \
    "ds_read_b64_tr_b16 v[174:175], %1 offset:37376\n\t" \
    "ds_read_b64_tr_b16 v[176:177], %0 offset:40960\n\t" \
    "ds_read_b64_tr_b16 v[178:179], %0 offset:41472\n\t" \
    "ds_read_b64_tr_b16 v[180:181], %1 offset:40960\n\t" \
    "ds_read_b64_tr_b16 v[182:183], %1 offset:41472\n\t" \
    "ds_read_b64_tr_b16 v[184:185], %0 offset:45056\n\t" \
    "ds_read_b64_tr_b16 v[186:187], %0 offset:45568\n\t" \
    "ds_read_b64_tr_b16 v[188:189], %1 offset:45056\n\t" \
    "ds_read_b64_tr_b16 v[190:191], %1 offset:45568\n\t" \
    "ds_read_b64_tr_b16 v[192:193], %0 offset:49152\n\t" \
    "ds_read_b64_tr_b16 v[194:195], %0 offset:49664\n\t" \
    "ds_read_b64_tr_b16 v[196:197], %1 offset:49152\n\t" \
    "ds_read_b64_tr_b16 v[198:199], %1 offset:49664\n\t" \
    "ds_read_b64_tr_b16 v[200:201], %0 offset:53248\n\t" \
    "ds_read_b64_tr_b16 v[202:203], %0 offset:53760\n\t" \
    "ds_read_b64_tr_b16 v[204:205], %1 offset:53248\n\t" \
    "ds_read_b64_tr_b16 v[206:207], %1 offset:53760\n\t" \
    "ds_read_b64_tr_b16 v[208:209], %0 offset:57344\n\t" \
    "ds_read_b64_tr_b16 v[210:211], %0 offset:57856\n\t" \
    "ds_read_b64_tr_b16 v[212:213], %1 offset:57344\n\t" \
    "ds_read_b64_tr_b16 v[214:215], %1 offset:57856\n\t" \
    "ds_read_b64_tr_b16 v[216:217], %0 offset:61440\n\t" \
    "ds_read_b64_tr_b16 v[218:219], %0 offset:61952\n\t" \
    "ds_read_b64_tr_b16 v[220:221], %1 offset:61440\n\t" \
    "ds_read_b64_tr_b16 v[222:223], %1 offset:61952\n\t" \
    "ds_read_b64_tr_b16 v[224:225], %2 offset:24576\n\t" \
    "ds_read_b64_tr_b16 v[226:227], %2 offset:25088\n\t" \
    "ds_read_b64_tr_b16 v[228:229], %3 offset:24576\n\t" \
    "ds_read_b64_tr_b16 v[230:231], %3 offset:25088\n\t" \
    "ds_read_b64_tr_b16 v[232:233], %2 offset:28672\n\t" \
    "ds_read_b64_tr_b16 v[234:235], %2 offset:29184\n\t" \
    "ds_read_b64_tr_b16 v[236:237], %3 offset:28672\n\t" \
    "ds_read_b64_tr_b16 v[238:239], %3 offset:29184\n\t" \
    "ds_read_b64_tr_b16 v[240:241], %2 offset:32768\n\t" \
    "ds_read_b64_tr_b16 v[242:243], %2 offset:33280\n\t" \
    "ds_read_b64_tr_b16 v[244:245], %3 offset:32768\n\t" \
    "ds_read_b64_tr_b16 v[246:247], %3 offset:33280\n\t" \
    "ds_read_b64_tr_b16 v[248:249], %2 offset:36864\n\t" \
    "ds_read_b64_tr_b16 v[250:251], %2 offset:37376\n\t" \
    "ds_read_b64_tr_b16 v[252:253], %3 offset:36864\n\t" \
    "ds_read_b64_tr_b16 v[254:255], %3 offset:37376\n\t"
#define P2_MFMA \
    "s_barrier\n\t" \
    "ds_read_b128 v[48:51], %6 offset:0\n\t" \
    "ds_read_b128 v[52:55], %6 offset:2048\n\t" \
    "ds_read_b128 v[56:59], %6 offset:4096\n\t" \
    "ds_read_b128 v[60:63], %7 offset:0\n\t" \
    "ds_read_b128 v[64:67], %7 offset:2048\n\t" \
    "ds_read_b128 v[68:71], %7 offset:4096\n\t" \
    "s_waitcnt lgkmcnt(0)\n\t" \
    "v_mfma_f32_16x16x32_bf16 %0, v[96:99], v[48:51], %0\n\t" \
    "ds_read_b128 v[72:75], %6 offset:12288\n\t" \
    "ds_read_b128 v[76:79], %6 offset:14336\n\t" \
    "v_mfma_f32_16x16x32_bf16 %1, v[100:103], v[48:51], %1\n\t" \
    "ds_read_b128 v[80:83], %6 offset:16384\n\t" \
    "ds_read_b128 v[84:87], %7 offset:12288\n\t" \
    "v_mfma_f32_16x16x32_bf16 %2, v[96:99], v[52:55], %2\n\t" \
    "ds_read_b128 v[88:91], %7 offset:14336\n\t" \
    "ds_read_b128 v[92:95], %7 offset:16384\n\t" \
    "v_mfma_f32_16x16x32_bf16 %3, v[100:103], v[52:55], %3\n\t" \
    "v_mfma_f32_16x16x32_bf16 %4, v[96:99], v[56:59], %4\n\t" \
    "v_mfma_f32_16x16x32_bf16 %5, v[100:103], v[56:59], %5\n\t" \
    "v_mfma_f32_16x16x32_bf16 %0, v[104:107], v[60:63], %0\n\t" \
    "v_mfma_f32_16x16x32_bf16 %1, v[108:111], v[60:63], %1\n\t" \
    "v_mfma_f32_16x16x32_bf16 %2, v[104:107], v[64:67], %2\n\t" \
    "v_mfma_f32_16x16x32_bf16 %3, v[108:111], v[64:67], %3\n\t" \
    "v_mfma_f32_16x16x32_bf16 %4, v[104:107], v[68:71], %4\n\t" \
    "v_mfma_f32_16x16x32_bf16 %5, v[108:111], v[68:71], %5\n\t" \
    "s_waitcnt lgkmcnt(0)\n\t" \
    "s_barrier\n\t" \
    "v_mfma_f32_16x16x32_bf16 %0, v[112:115], v[72:75], %0\n\t" \
    "ds_read_b128 v[48:51], %6 offset:24576\n\t" \
    "ds_read_b128 v[52:55], %6 offset:26624\n\t" \
    "v_mfma_f32_16x16x32_bf16 %1, v[116:119], v[72:75], %1\n\t" \
    "ds_read_b128 v[56:59], %6 offset:28672\n\t" \
    "ds_read_b128 v[60:63], %7 offset:24576\n\t" \
    "v_mfma_f32_16x16x32_bf16 %2, v[112:115], v[76:79], %2\n\t" \
    "ds_read_b128 v[64:67], %7 offset:26624\n\t" \
    "ds_read_b128 v[68:71], %7 offset:28672\n\t" \
    "v_mfma_f32_16x16x32_bf16 %3, v[116:119], v[76:79], %3\n\t" \
    "v_mfma_f32_16x16x32_bf16 %4, v[112:115], v[80:83], %4\n\t" \
    "v_mfma_f32_16x16x32_bf16 %5, v[116:119], v[80:83], %5\n\t" \
    "v_mfma_f32_16x16x32_bf16 %0, v[120:123], v[84:87], %0\n\t" \
    "v_mfma_f32_16x16x32_bf16 %1, v[124:127], v[84:87], %1\n\t" \
    "v_mfma_f32_16x16x32_bf16 %2, v[120:123], v[88:91], %2\n\t" \
    "v_mfma_f32_16x16x32_bf16 %3, v[124:127], v[88:91], %3\n\t" \
    "v_mfma_f32_16x16x32_bf16 %4, v[120:123], v[92:95], %4\n\t" \
    "v_mfma_f32_16x16x32_bf16 %5, v[124:127], v[92:95], %5\n\t" \
    "s_waitcnt lgkmcnt(0)\n\t" \
    "v_mfma_f32_16x16x32_bf16 %0, v[128:131], v[48:51], %0\n\t" \
    "ds_read_b128 v[72:75], %8 offset:0\n\t" \
    "ds_read_b128 v[76:79], %8 offset:2048\n\t" \
    "v_mfma_f32_16x16x32_bf16 %1, v[132:135], v[48:51], %1\n\t" \
    "ds_read_b128 v[80:83], %8 offset:4096\n\t" \
    "ds_read_b128 v[84:87], %9 offset:0\n\t" \
    "v_mfma_f32_16x16x32_bf16 %2, v[128:131], v[52:55], %2\n\t" \
    "ds_read_b128 v[88:91], %9 offset:2048\n\t" \
    "ds_read_b128 v[92:95], %9 offset:4096\n\t" \
    "v_mfma_f32_16x16x32_bf16 %3, v[132:135], v[52:55], %3\n\t" \
    "v_mfma_f32_16x16x32_bf16 %4, v[128:131], v[56:59], %4\n\t" \
    "v_mfma_f32_16x16x32_bf16 %5, v[132:135], v[56:59], %5\n\t" \
    "v_mfma_f32_16x16x32_bf16 %0, v[136:139], v[60:63], %0\n\t" \
    "v_mfma_f32_16x16x32_bf16 %1, v[140:143], v[60:63], %1\n\t" \
    "v_mfma_f32_16x16x32_bf16 %2, v[136:139], v[64:67], %2\n\t" \
    "v_mfma_f32_16x16x32_bf16 %3, v[140:143], v[64:67], %3\n\t" \
    "v_mfma_f32_16x16x32_bf16 %4, v[136:139], v[68:71], %4\n\t" \
    "v_mfma_f32_16x16x32_bf16 %5, v[140:143], v[68:71], %5\n\t" \
    "s_waitcnt lgkmcnt(0)\n\t" \
    "s_barrier\n\t" \
    "v_mfma_f32_16x16x32_bf16 %0, v[144:147], v[72:75], %0\n\t" \
    "ds_read_b128 v[48:51], %8 offset:12288\n\t" \
    "ds_read_b128 v[52:55], %8 offset:14336\n\t" \
    "v_mfma_f32_16x16x32_bf16 %1, v[148:151], v[72:75], %1\n\t" \
    "ds_read_b128 v[56:59], %8 offset:16384\n\t" \
    "ds_read_b128 v[60:63], %9 offset:12288\n\t" \
    "v_mfma_f32_16x16x32_bf16 %2, v[144:147], v[76:79], %2\n\t" \
    "ds_read_b128 v[64:67], %9 offset:14336\n\t" \
    "ds_read_b128 v[68:71], %9 offset:16384\n\t" \
    "v_mfma_f32_16x16x32_bf16 %3, v[148:151], v[76:79], %3\n\t" \
    "v_mfma_f32_16x16x32_bf16 %4, v[144:147], v[80:83], %4\n\t" \
    "v_mfma_f32_16x16x32_bf16 %5, v[148:151], v[80:83], %5\n\t" \
    "v_mfma_f32_16x16x32_bf16 %0, v[152:155], v[84:87], %0\n\t" \
    "v_mfma_f32_16x16x32_bf16 %1, v[156:159], v[84:87], %1\n\t" \
    "v_mfma_f32_16x16x32_bf16 %2, v[152:155], v[88:91], %2\n\t" \
    "v_mfma_f32_16x16x32_bf16 %3, v[156:159], v[88:91], %3\n\t" \
    "v_mfma_f32_16x16x32_bf16 %4, v[152:155], v[92:95], %4\n\t" \
    "v_mfma_f32_16x16x32_bf16 %5, v[156:159], v[92:95], %5\n\t" \
    "s_waitcnt lgkmcnt(0)\n\t" \
    "v_mfma_f32_16x16x32_bf16 %0, v[160:163], v[48:51], %0\n\t" \
    "ds_read_b128 v[72:75], %8 offset:24576\n\t" \
    "ds_read_b128 v[76:79], %8 offset:26624\n\t" \
    "v_mfma_f32_16x16x32_bf16 %1, v[164:167], v[48:51], %1\n\t" \
    "ds_read_b128 v[80:83], %8 offset:28672\n\t" \
    "ds_read_b128 v[84:87], %9 offset:24576\n\t" \
    "v_mfma_f32_16x16x32_bf16 %2, v[160:163], v[52:55], %2\n\t" \
    "ds_read_b128 v[88:91], %9 offset:26624\n\t" \
    "ds_read_b128 v[92:95], %9 offset:28672\n\t" \
    "v_mfma_f32_16x16x32_bf16 %3, v[164:167], v[52:55], %3\n\t" \
    "v_mfma_f32_16x16x32_bf16 %4, v[160:163], v[56:59], %4\n\t" \
    "v_mfma_f32_16x16x32_bf16 %5, v[164:167], v[56:59], %5\n\t" \
    "v_mfma_f32_16x16x32_bf16 %0, v[168:171], v[60:63], %0\n\t" \
    "v_mfma_f32_16x16x32_bf16 %1, v[172:175], v[60:63], %1\n\t" \
    "v_mfma_f32_16x16x32_bf16 %2, v[168:171], v[64:67], %2\n\t" \
    "v_mfma_f32_16x16x32_bf16 %3, v[172:175], v[64:67], %3\n\t" \
    "v_mfma_f32_16x16x32_bf16 %4, v[168:171], v[68:71], %4\n\t" \
    "v_mfma_f32_16x16x32_bf16 %5, v[172:175], v[68:71], %5\n\t" \
    "s_waitcnt lgkmcnt(0)\n\t" \
    "s_barrier\n\t" \
    "v_mfma_f32_16x16x32_bf16 %0, v[176:179], v[72:75], %0\n\t" \
    "ds_read_b128 v[48:51], %6 offset:0\n\t" \
    "ds_read_b128 v[52:55], %6 offset:2048\n\t" \
    "v_mfma_f32_16x16x32_bf16 %1, v[180:183], v[72:75], %1\n\t" \
    "ds_read_b128 v[56:59], %6 offset:4096\n\t" \
    "ds_read_b128 v[60:63], %7 offset:0\n\t" \
    "v_mfma_f32_16x16x32_bf16 %2, v[176:179], v[76:79], %2\n\t" \
    "ds_read_b128 v[64:67], %7 offset:2048\n\t" \
    "ds_read_b128 v[68:71], %7 offset:4096\n\t" \
    "v_mfma_f32_16x16x32_bf16 %3, v[180:183], v[76:79], %3\n\t" \
    "v_mfma_f32_16x16x32_bf16 %4, v[176:179], v[80:83], %4\n\t" \
    "v_mfma_f32_16x16x32_bf16 %5, v[180:183], v[80:83], %5\n\t" \
    "v_mfma_f32_16x16x32_bf16 %0, v[184:187], v[84:87], %0\n\t" \
    "v_mfma_f32_16x16x32_bf16 %1, v[188:191], v[84:87], %1\n\t" \
    "v_mfma_f32_16x16x32_bf16 %2, v[184:187], v[88:91], %2\n\t" \
    "v_mfma_f32_16x16x32_bf16 %3, v[188:191], v[88:91], %3\n\t" \
    "v_mfma_f32_16x16x32_bf16 %4, v[184:187], v[92:95], %4\n\t" \
    "v_mfma_f32_16x16x32_bf16 %5, v[188:191], v[92:95], %5\n\t" \
    "s_waitcnt lgkmcnt(0)\n\t" \
    "v_mfma_f32_16x16x32_bf16 %0, v[192:195], v[48:51], %0\n\t" \
    "ds_read_b128 v[72:75], %6 offset:12288\n\t" \
    "ds_read_b128 v[76:79], %6 offset:14336\n\t" \
    "v_mfma_f32_16x16x32_bf16 %1, v[196:199], v[48:51], %1\n\t" \
    "ds_read_b128 v[80:83], %6 offset:16384\n\t" \
    "ds_read_b128 v[84:87], %7 offset:12288\n\t" \
    "v_mfma_f32_16x16x32_bf16 %2, v[192:195], v[52:55], %2\n\t" \
    "ds_read_b128 v[88:91], %7 offset:14336\n\t" \
    "ds_read_b128 v[92:95], %7 offset:16384\n\t" \
    "v_mfma_f32_16x16x32_bf16 %3, v[196:199], v[52:55], %3\n\t" \
    "v_mfma_f32_16x16x32_bf16 %4, v[192:195], v[56:59], %4\n\t" \
    "v_mfma_f32_16x16x32_bf16 %5, v[196:199], v[56:59], %5\n\t" \
    "v_mfma_f32_16x16x32_bf16 %0, v[200:203], v[60:63], %0\n\t" \
    "v_mfma_f32_16x16x32_bf16 %1, v[204:207], v[60:63], %1\n\t" \
    "v_mfma_f32_16x16x32_bf16 %2, v[200:203], v[64:67], %2\n\t" \
    "v_mfma_f32_16x16x32_bf16 %3, v[204:207], v[64:67], %3\n\t" \
    "v_mfma_f32_16x16x32_bf16 %4, v[200:203], v[68:71], %4\n\t" \
    "v_mfma_f32_16x16x32_bf16 %5, v[204:207], v[68:71], %5\n\t" \
    "s_waitcnt lgkmcnt(0)\n\t" \
    "s_barrier\n\t" \
    "v_mfma_f32_16x16x32_bf16 %0, v[208:211], v[72:75], %0\n\t" \
    "ds_read_b128 v[48:51], %6 offset:24576\n\t" \
    "ds_read_b128 v[52:55], %6 offset:26624\n\t" \
    "v_mfma_f32_16x16x32_bf16 %1, v[212:215], v[72:75], %1\n\t" \
    "ds_read_b128 v[56:59], %6 offset:28672\n\t" \
    "ds_read_b128 v[60:63], %7 offset:24576\n\t" \
    "v_mfma_f32_16x16x32_bf16 %2, v[208:211], v[76:79], %2\n\t" \
    "ds_read_b128 v[64:67], %7 offset:26624\n\t" \
    "ds_read_b128 v[68:71], %7 offset:28672\n\t" \
    "v_mfma_f32_16x16x32_bf16 %3, v[212:215], v[76:79], %3\n\t" \
    "v_mfma_f32_16x16x32_bf16 %4, v[208:211], v[80:83], %4\n\t" \
    "v_mfma_f32_16x16x32_bf16 %5, v[212:215], v[80:83], %5\n\t" \
    "v_mfma_f32_16x16x32_bf16 %0, v[216:219], v[84:87], %0\n\t" \
    "v_mfma_f32_16x16x32_bf16 %1, v[220:223], v[84:87], %1\n\t" \
    "v_mfma_f32_16x16x32_bf16 %2, v[216:219], v[88:91], %2\n\t" \
    "v_mfma_f32_16x16x32_bf16 %3, v[220:223], v[88:91], %3\n\t" \
    "v_mfma_f32_16x16x32_bf16 %4, v[216:219], v[92:95], %4\n\t" \
    "v_mfma_f32_16x16x32_bf16 %5, v[220:223], v[92:95], %5\n\t" \
    "s_waitcnt lgkmcnt(0)\n\t" \
    "v_mfma_f32_16x16x32_bf16 %0, v[224:227], v[48:51], %0\n\t" \
    "ds_read_b128 v[72:75], %8 offset:0\n\t" \
    "ds_read_b128 v[76:79], %8 offset:2048\n\t" \
    "v_mfma_f32_16x16x32_bf16 %1, v[228:231], v[48:51], %1\n\t" \
    "ds_read_b128 v[80:83], %8 offset:4096\n\t" \
    "ds_read_b128 v[84:87], %9 offset:0\n\t" \
    "v_mfma_f32_16x16x32_bf16 %2, v[224:227], v[52:55], %2\n\t" \
    "ds_read_b128 v[88:91], %9 offset:2048\n\t" \
    "ds_read_b128 v[92:95], %9 offset:4096\n\t" \
    "v_mfma_f32_16x16x32_bf16 %3, v[228:231], v[52:55], %3\n\t" \
    "v_mfma_f32_16x16x32_bf16 %4, v[224:227], v[56:59], %4\n\t" \
    "v_mfma_f32_16x16x32_bf16 %5, v[228:231], v[56:59], %5\n\t" \
    "v_mfma_f32_16x16x32_bf16 %0, v[232:235], v[60:63], %0\n\t" \
    "v_mfma_f32_16x16x32_bf16 %1, v[236:239], v[60:63], %1\n\t" \
    "v_mfma_f32_16x16x32_bf16 %2, v[232:235], v[64:67], %2\n\t" \
    "v_mfma_f32_16x16x32_bf16 %3, v[236:239], v[64:67], %3\n\t" \
    "v_mfma_f32_16x16x32_bf16 %4, v[232:235], v[68:71], %4\n\t" \
    "v_mfma_f32_16x16x32_bf16 %5, v[236:239], v[68:71], %5\n\t" \
    "s_waitcnt lgkmcnt(0)\n\t" \
    "s_barrier\n\t" \
    "v_mfma_f32_16x16x32_bf16 %0, v[240:243], v[72:75], %0\n\t" \
    "ds_read_b128 v[48:51], %8 offset:12288\n\t" \
    "ds_read_b128 v[52:55], %8 offset:14336\n\t" \
    "v_mfma_f32_16x16x32_bf16 %1, v[244:247], v[72:75], %1\n\t" \
    "ds_read_b128 v[56:59], %8 offset:16384\n\t" \
    "ds_read_b128 v[60:63], %9 offset:12288\n\t" \
    "v_mfma_f32_16x16x32_bf16 %2, v[240:243], v[76:79], %2\n\t" \
    "ds_read_b128 v[64:67], %9 offset:14336\n\t" \
    "ds_read_b128 v[68:71], %9 offset:16384\n\t" \
    "v_mfma_f32_16x16x32_bf16 %3, v[244:247], v[76:79], %3\n\t" \
    "ds_read_b64_tr_b16 v[96:97], %10 offset:0\n\t" \
    "v_mfma_f32_16x16x32_bf16 %4, v[240:243], v[80:83], %4\n\t" \
    "ds_read_b64_tr_b16 v[98:99], %10 offset:512\n\t" \
    "v_mfma_f32_16x16x32_bf16 %5, v[244:247], v[80:83], %5\n\t" \
    "ds_read_b64_tr_b16 v[100:101], %11 offset:0\n\t" \
    "v_mfma_f32_16x16x32_bf16 %0, v[248:251], v[84:87], %0\n\t" \
    "ds_read_b64_tr_b16 v[102:103], %11 offset:512\n\t" \
    "v_mfma_f32_16x16x32_bf16 %1, v[252:255], v[84:87], %1\n\t" \
    "ds_read_b64_tr_b16 v[104:105], %10 offset:4096\n\t" \
    "v_mfma_f32_16x16x32_bf16 %2, v[248:251], v[88:91], %2\n\t" \
    "ds_read_b64_tr_b16 v[106:107], %10 offset:4608\n\t" \
    "v_mfma_f32_16x16x32_bf16 %3, v[252:255], v[88:91], %3\n\t" \
    "ds_read_b64_tr_b16 v[108:109], %11 offset:4096\n\t" \
    "v_mfma_f32_16x16x32_bf16 %4, v[248:251], v[92:95], %4\n\t" \
    "ds_read_b64_tr_b16 v[110:111], %11 offset:4608\n\t" \
    "v_mfma_f32_16x16x32_bf16 %5, v[252:255], v[92:95], %5\n\t" \
    "s_waitcnt lgkmcnt(0)\n\t" \
    "v_mfma_f32_16x16x32_bf16 %0, v[96:99], v[48:51], %0\n\t" \
    "ds_read_b128 v[72:75], %8 offset:24576\n\t" \
    "ds_read_b128 v[76:79], %8 offset:26624\n\t" \
    "v_mfma_f32_16x16x32_bf16 %1, v[100:103], v[48:51], %1\n\t" \
    "ds_read_b128 v[80:83], %8 offset:28672\n\t" \
    "ds_read_b128 v[84:87], %9 offset:24576\n\t" \
    "v_mfma_f32_16x16x32_bf16 %2, v[96:99], v[52:55], %2\n\t" \
    "ds_read_b128 v[88:91], %9 offset:26624\n\t" \
    "ds_read_b128 v[92:95], %9 offset:28672\n\t" \
    "v_mfma_f32_16x16x32_bf16 %3, v[100:103], v[52:55], %3\n\t" \
    "ds_read_b64_tr_b16 v[112:113], %10 offset:8192\n\t" \
    "v_mfma_f32_16x16x32_bf16 %4, v[96:99], v[56:59], %4\n\t" \
    "ds_read_b64_tr_b16 v[114:115], %10 offset:8704\n\t" \
    "v_mfma_f32_16x16x32_bf16 %5, v[100:103], v[56:59], %5\n\t" \
    "ds_read_b64_tr_b16 v[116:117], %11 offset:8192\n\t" \
    "v_mfma_f32_16x16x32_bf16 %0, v[104:107], v[60:63], %0\n\t" \
    "ds_read_b64_tr_b16 v[118:119], %11 offset:8704\n\t" \
    "v_mfma_f32_16x16x32_bf16 %1, v[108:111], v[60:63], %1\n\t" \
    "ds_read_b64_tr_b16 v[120:121], %10 offset:12288\n\t" \
    "v_mfma_f32_16x16x32_bf16 %2, v[104:107], v[64:67], %2\n\t" \
    "ds_read_b64_tr_b16 v[122:123], %10 offset:12800\n\t" \
    "v_mfma_f32_16x16x32_bf16 %3, v[108:111], v[64:67], %3\n\t" \
    "ds_read_b64_tr_b16 v[124:125], %11 offset:12288\n\t" \
    "v_mfma_f32_16x16x32_bf16 %4, v[104:107], v[68:71], %4\n\t" \
    "ds_read_b64_tr_b16 v[126:127], %11 offset:12800\n\t" \
    "v_mfma_f32_16x16x32_bf16 %5, v[108:111], v[68:71], %5\n\t" \
    "s_waitcnt lgkmcnt(8)\n\t" \
    "s_barrier\n\t" \
    "s_waitcnt lgkmcnt(0)\n\t" \
    "v_mfma_f32_16x16x32_bf16 %0, v[112:115], v[72:75], %0\n\t" \
    "ds_read_b128 v[48:51], %6 offset:0\n\t" \
    "ds_read_b128 v[52:55], %6 offset:2048\n\t" \
    "v_mfma_f32_16x16x32_bf16 %1, v[116:119], v[72:75], %1\n\t" \
    "ds_read_b128 v[56:59], %6 offset:4096\n\t" \
    "ds_read_b128 v[60:63], %7 offset:0\n\t" \
    "v_mfma_f32_16x16x32_bf16 %2, v[112:115], v[76:79], %2\n\t" \
    "ds_read_b128 v[64:67], %7 offset:2048\n\t" \
    "ds_read_b128 v[68:71], %7 offset:4096\n\t" \
    "v_mfma_f32_16x16x32_bf16 %3, v[116:119], v[76:79], %3\n\t" \
    "ds_read_b64_tr_b16 v[96:97], %10 offset:16384\n\t" \
    "v_mfma_f32_16x16x32_bf16 %4, v[112:115], v[80:83], %4\n\t" \
    "ds_read_b64_tr_b16 v[98:99], %10 offset:16896\n\t" \
    "v_mfma_f32_16x16x32_bf16 %5, v[116:119], v[80:83], %5\n\t" \
    "ds_read_b64_tr_b16 v[100:101], %11 offset:16384\n\t" \
    "v_mfma_f32_16x16x32_bf16 %0, v[120:123], v[84:87], %0\n\t" \
    "ds_read_b64_tr_b16 v[102:103], %11 offset:16896\n\t" \
    "v_mfma_f32_16x16x32_bf16 %1, v[124:127], v[84:87], %1\n\t" \
    "ds_read_b64_tr_b16 v[104:105], %10 offset:20480\n\t" \
    "v_mfma_f32_16x16x32_bf16 %2, v[120:123], v[88:91], %2\n\t" \
    "ds_read_b64_tr_b16 v[106:107], %10 offset:20992\n\t" \
    "v_mfma_f32_16x16x32_bf16 %3, v[124:127], v[88:91], %3\n\t" \
    "ds_read_b64_tr_b16 v[108:109], %11 offset:20480\n\t" \
    "v_mfma_f32_16x16x32_bf16 %4, v[120:123], v[92:95], %4\n\t" \
    "ds_read_b64_tr_b16 v[110:111], %11 offset:20992\n\t" \
    "v_mfma_f32_16x16x32_bf16 %5, v[124:127], v[92:95], %5\n\t" \
    "s_waitcnt lgkmcnt(0)\n\t" \
    "v_mfma_f32_16x16x32_bf16 %0, v[96:99], v[48:51], %0\n\t" \
    "ds_read_b128 v[72:75], %6 offset:12288\n\t" \
    "ds_read_b128 v[76:79], %6 offset:14336\n\t" \
    "v_mfma_f32_16x16x32_bf16 %1, v[100:103], v[48:51], %1\n\t" \
    "ds_read_b128 v[80:83], %6 offset:16384\n\t" \
    "ds_read_b128 v[84:87], %7 offset:12288\n\t" \
    "v_mfma_f32_16x16x32_bf16 %2, v[96:99], v[52:55], %2\n\t" \
    "ds_read_b128 v[88:91], %7 offset:14336\n\t" \
    "ds_read_b128 v[92:95], %7 offset:16384\n\t" \
    "v_mfma_f32_16x16x32_bf16 %3, v[100:103], v[52:55], %3\n\t" \
    "ds_read_b64_tr_b16 v[112:113], %10 offset:24576\n\t" \
    "v_mfma_f32_16x16x32_bf16 %4, v[96:99], v[56:59], %4\n\t" \
    "ds_read_b64_tr_b16 v[114:115], %10 offset:25088\n\t" \
    "v_mfma_f32_16x16x32_bf16 %5, v[100:103], v[56:59], %5\n\t" \
    "ds_read_b64_tr_b16 v[116:117], %11 offset:24576\n\t" \
    "v_mfma_f32_16x16x32_bf16 %0, v[104:107], v[60:63], %0\n\t" \
    "ds_read_b64_tr_b16 v[118:119], %11 offset:25088\n\t" \
    "v_mfma_f32_16x16x32_bf16 %1, v[108:111], v[60:63], %1\n\t" \
    "ds_read_b64_tr_b16 v[120:121], %10 offset:28672\n\t" \
    "v_mfma_f32_16x16x32_bf16 %2, v[104:107], v[64:67], %2\n\t" \
    "ds_read_b64_tr_b16 v[122:123], %10 offset:29184\n\t" \
    "v_mfma_f32_16x16x32_bf16 %3, v[108:111], v[64:67], %3\n\t" \
    "ds_read_b64_tr_b16 v[124:125], %11 offset:28672\n\t" \
    "v_mfma_f32_16x16x32_bf16 %4, v[104:107], v[68:71], %4\n\t" \
    "ds_read_b64_tr_b16 v[126:127], %11 offset:29184\n\t" \
    "v_mfma_f32_16x16x32_bf16 %5, v[108:111], v[68:71], %5\n\t" \
    "s_waitcnt lgkmcnt(8)\n\t" \
    "s_barrier\n\t" \
    "s_waitcnt lgkmcnt(0)\n\t" \
    "v_mfma_f32_16x16x32_bf16 %0, v[112:115], v[72:75], %0\n\t" \
    "ds_read_b128 v[48:51], %6 offset:24576\n\t" \
    "ds_read_b128 v[52:55], %6 offset:26624\n\t" \
    "v_mfma_f32_16x16x32_bf16 %1, v[116:119], v[72:75], %1\n\t" \
    "ds_read_b128 v[56:59], %6 offset:28672\n\t" \
    "ds_read_b128 v[60:63], %7 offset:24576\n\t" \
    "v_mfma_f32_16x16x32_bf16 %2, v[112:115], v[76:79], %2\n\t" \
    "ds_read_b128 v[64:67], %7 offset:26624\n\t" \
    "ds_read_b128 v[68:71], %7 offset:28672\n\t" \
    "v_mfma_f32_16x16x32_bf16 %3, v[116:119], v[76:79], %3\n\t" \
    "ds_read_b64_tr_b16 v[96:97], %10 offset:32768\n\t" \
    "v_mfma_f32_16x16x32_bf16 %4, v[112:115], v[80:83], %4\n\t" \
    "ds_read_b64_tr_b16 v[98:99], %10 offset:33280\n\t" \
    "v_mfma_f32_16x16x32_bf16 %5, v[116:119], v[80:83], %5\n\t" \
    "ds_read_b64_tr_b16 v[100:101], %11 offset:32768\n\t" \
    "v_mfma_f32_16x16x32_bf16 %0, v[120:123], v[84:87], %0\n\t" \
    "ds_read_b64_tr_b16 v[102:103], %11 offset:33280\n\t" \
    "v_mfma_f32_16x16x32_bf16 %1, v[124:127], v[84:87], %1\n\t" \
    "ds_read_b64_tr_b16 v[104:105], %10 offset:36864\n\t" \
    "v_mfma_f32_16x16x32_bf16 %2, v[120:123], v[88:91], %2\n\t" \
    "ds_read_b64_tr_b16 v[106:107], %10 offset:37376\n\t" \
    "v_mfma_f32_16x16x32_bf16 %3, v[124:127], v[88:91], %3\n\t" \
    "ds_read_b64_tr_b16 v[108:109], %11 offset:36864\n\t" \
    "v_mfma_f32_16x16x32_bf16 %4, v[120:123], v[92:95], %4\n\t" \
    "ds_read_b64_tr_b16 v[110:111], %11 offset:37376\n\t" \
    "v_mfma_f32_16x16x32_bf16 %5, v[124:127], v[92:95], %5\n\t" \
    "s_waitcnt lgkmcnt(0)\n\t" \
    "v_mfma_f32_16x16x32_bf16 %0, v[96:99], v[48:51], %0\n\t" \
    "ds_read_b128 v[72:75], %8 offset:0\n\t" \
    "ds_read_b128 v[76:79], %8 offset:2048\n\t" \
    "v_mfma_f32_16x16x32_bf16 %1, v[100:103], v[48:51], %1\n\t" \
    "ds_read_b128 v[80:83], %8 offset:4096\n\t" \
    "ds_read_b128 v[84:87], %9 offset:0\n\t" \
    "v_mfma_f32_16x16x32_bf16 %2, v[96:99], v[52:55], %2\n\t" \
    "ds_read_b128 v[88:91], %9 offset:2048\n\t" \
    "ds_read_b128 v[92:95], %9 offset:4096\n\t" \
    "v_mfma_f32_16x16x32_bf16 %3, v[100:103], v[52:55], %3\n\t" \
    "ds_read_b64_tr_b16 v[112:113], %10 offset:40960\n\t" \
    "v_mfma_f32_16x16x32_bf16 %4, v[96:99], v[56:59], %4\n\t" \
    "ds_read_b64_tr_b16 v[114:115], %10 offset:41472\n\t" \
    "v_mfma_f32_16x16x32_bf16 %5, v[100:103], v[56:59], %5\n\t" \
    "ds_read_b64_tr_b16 v[116:117], %11 offset:40960\n\t" \
    "v_mfma_f32_16x16x32_bf16 %0, v[104:107], v[60:63], %0\n\t" \
    "ds_read_b64_tr_b16 v[118:119], %11 offset:41472\n\t" \
    "v_mfma_f32_16x16x32_bf16 %1, v[108:111], v[60:63], %1\n\t" \
    "ds_read_b64_tr_b16 v[120:121], %10 offset:45056\n\t" \
    "v_mfma_f32_16x16x32_bf16 %2, v[104:107], v[64:67], %2\n\t" \
    "ds_read_b64_tr_b16 v[122:123], %10 offset:45568\n\t" \
    "v_mfma_f32_16x16x32_bf16 %3, v[108:111], v[64:67], %3\n\t" \
    "ds_read_b64_tr_b16 v[124:125], %11 offset:45056\n\t" \
    "v_mfma_f32_16x16x32_bf16 %4, v[104:107], v[68:71], %4\n\t" \
    "ds_read_b64_tr_b16 v[126:127], %11 offset:45568\n\t" \
    "v_mfma_f32_16x16x32_bf16 %5, v[108:111], v[68:71], %5\n\t" \
    "s_waitcnt lgkmcnt(8)\n\t" \
    "s_barrier\n\t" \
    "s_waitcnt lgkmcnt(0)\n\t" \
    "v_mfma_f32_16x16x32_bf16 %0, v[112:115], v[72:75], %0\n\t" \
    "ds_read_b128 v[48:51], %8 offset:12288\n\t" \
    "ds_read_b128 v[52:55], %8 offset:14336\n\t" \
    "v_mfma_f32_16x16x32_bf16 %1, v[116:119], v[72:75], %1\n\t" \
    "ds_read_b128 v[56:59], %8 offset:16384\n\t" \
    "ds_read_b128 v[60:63], %9 offset:12288\n\t" \
    "v_mfma_f32_16x16x32_bf16 %2, v[112:115], v[76:79], %2\n\t" \
    "ds_read_b128 v[64:67], %9 offset:14336\n\t" \
    "ds_read_b128 v[68:71], %9 offset:16384\n\t" \
    "v_mfma_f32_16x16x32_bf16 %3, v[116:119], v[76:79], %3\n\t" \
    "ds_read_b64_tr_b16 v[96:97], %12 offset:0\n\t" \
    "v_mfma_f32_16x16x32_bf16 %4, v[112:115], v[80:83], %4\n\t" \
    "ds_read_b64_tr_b16 v[98:99], %12 offset:512\n\t" \
    "v_mfma_f32_16x16x32_bf16 %5, v[116:119], v[80:83], %5\n\t" \
    "ds_read_b64_tr_b16 v[100:101], %13 offset:0\n\t" \
    "v_mfma_f32_16x16x32_bf16 %0, v[120:123], v[84:87], %0\n\t" \
    "ds_read_b64_tr_b16 v[102:103], %13 offset:512\n\t" \
    "v_mfma_f32_16x16x32_bf16 %1, v[124:127], v[84:87], %1\n\t" \
    "ds_read_b64_tr_b16 v[104:105], %12 offset:4096\n\t" \
    "v_mfma_f32_16x16x32_bf16 %2, v[120:123], v[88:91], %2\n\t" \
    "ds_read_b64_tr_b16 v[106:107], %12 offset:4608\n\t" \
    "v_mfma_f32_16x16x32_bf16 %3, v[124:127], v[88:91], %3\n\t" \
    "ds_read_b64_tr_b16 v[108:109], %13 offset:4096\n\t" \
    "v_mfma_f32_16x16x32_bf16 %4, v[120:123], v[92:95], %4\n\t" \
    "ds_read_b64_tr_b16 v[110:111], %13 offset:4608\n\t" \
    "v_mfma_f32_16x16x32_bf16 %5, v[124:127], v[92:95], %5\n\t" \
    "s_waitcnt lgkmcnt(0)\n\t" \
    "v_mfma_f32_16x16x32_bf16 %0, v[96:99], v[48:51], %0\n\t" \
    "ds_read_b128 v[72:75], %8 offset:24576\n\t" \
    "ds_read_b128 v[76:79], %8 offset:26624\n\t" \
    "v_mfma_f32_16x16x32_bf16 %1, v[100:103], v[48:51], %1\n\t" \
    "ds_read_b128 v[80:83], %8 offset:28672\n\t" \
    "ds_read_b128 v[84:87], %9 offset:24576\n\t" \
    "v_mfma_f32_16x16x32_bf16 %2, v[96:99], v[52:55], %2\n\t" \
    "ds_read_b128 v[88:91], %9 offset:26624\n\t" \
    "ds_read_b128 v[92:95], %9 offset:28672\n\t" \
    "v_mfma_f32_16x16x32_bf16 %3, v[100:103], v[52:55], %3\n\t" \
    "ds_read_b64_tr_b16 v[112:113], %12 offset:8192\n\t" \
    "v_mfma_f32_16x16x32_bf16 %4, v[96:99], v[56:59], %4\n\t" \
    "ds_read_b64_tr_b16 v[114:115], %12 offset:8704\n\t" \
    "v_mfma_f32_16x16x32_bf16 %5, v[100:103], v[56:59], %5\n\t" \
    "ds_read_b64_tr_b16 v[116:117], %13 offset:8192\n\t" \
    "v_mfma_f32_16x16x32_bf16 %0, v[104:107], v[60:63], %0\n\t" \
    "ds_read_b64_tr_b16 v[118:119], %13 offset:8704\n\t" \
    "v_mfma_f32_16x16x32_bf16 %1, v[108:111], v[60:63], %1\n\t" \
    "ds_read_b64_tr_b16 v[120:121], %12 offset:12288\n\t" \
    "v_mfma_f32_16x16x32_bf16 %2, v[104:107], v[64:67], %2\n\t" \
    "ds_read_b64_tr_b16 v[122:123], %12 offset:12800\n\t" \
    "v_mfma_f32_16x16x32_bf16 %3, v[108:111], v[64:67], %3\n\t" \
    "ds_read_b64_tr_b16 v[124:125], %13 offset:12288\n\t" \
    "v_mfma_f32_16x16x32_bf16 %4, v[104:107], v[68:71], %4\n\t" \
    "ds_read_b64_tr_b16 v[126:127], %13 offset:12800\n\t" \
    "v_mfma_f32_16x16x32_bf16 %5, v[108:111], v[68:71], %5\n\t" \
    "s_waitcnt lgkmcnt(8)\n\t" \
    "s_barrier\n\t" \
    "s_waitcnt lgkmcnt(0)\n\t" \
    "v_mfma_f32_16x16x32_bf16 %0, v[112:115], v[72:75], %0\n\t" \
    "ds_read_b128 v[48:51], %6 offset:0\n\t" \
    "ds_read_b128 v[52:55], %6 offset:2048\n\t" \
    "v_mfma_f32_16x16x32_bf16 %1, v[116:119], v[72:75], %1\n\t" \
    "ds_read_b128 v[56:59], %6 offset:4096\n\t" \
    "ds_read_b128 v[60:63], %7 offset:0\n\t" \
    "v_mfma_f32_16x16x32_bf16 %2, v[112:115], v[76:79], %2\n\t" \
    "ds_read_b128 v[64:67], %7 offset:2048\n\t" \
    "ds_read_b128 v[68:71], %7 offset:4096\n\t" \
    "v_mfma_f32_16x16x32_bf16 %3, v[116:119], v[76:79], %3\n\t" \
    "ds_read_b64_tr_b16 v[96:97], %12 offset:16384\n\t" \
    "v_mfma_f32_16x16x32_bf16 %4, v[112:115], v[80:83], %4\n\t" \
    "ds_read_b64_tr_b16 v[98:99], %12 offset:16896\n\t" \
    "v_mfma_f32_16x16x32_bf16 %5, v[116:119], v[80:83], %5\n\t" \
    "ds_read_b64_tr_b16 v[100:101], %13 offset:16384\n\t" \
    "v_mfma_f32_16x16x32_bf16 %0, v[120:123], v[84:87], %0\n\t" \
    "ds_read_b64_tr_b16 v[102:103], %13 offset:16896\n\t" \
    "v_mfma_f32_16x16x32_bf16 %1, v[124:127], v[84:87], %1\n\t" \
    "ds_read_b64_tr_b16 v[104:105], %12 offset:20480\n\t" \
    "v_mfma_f32_16x16x32_bf16 %2, v[120:123], v[88:91], %2\n\t" \
    "ds_read_b64_tr_b16 v[106:107], %12 offset:20992\n\t" \
    "v_mfma_f32_16x16x32_bf16 %3, v[124:127], v[88:91], %3\n\t" \
    "ds_read_b64_tr_b16 v[108:109], %13 offset:20480\n\t" \
    "v_mfma_f32_16x16x32_bf16 %4, v[120:123], v[92:95], %4\n\t" \
    "ds_read_b64_tr_b16 v[110:111], %13 offset:20992\n\t" \
    "v_mfma_f32_16x16x32_bf16 %5, v[124:127], v[92:95], %5\n\t" \
    "s_waitcnt lgkmcnt(0)\n\t" \
    "v_mfma_f32_16x16x32_bf16 %0, v[96:99], v[48:51], %0\n\t" \
    "ds_read_b128 v[72:75], %6 offset:12288\n\t" \
    "ds_read_b128 v[76:79], %6 offset:14336\n\t" \
    "v_mfma_f32_16x16x32_bf16 %1, v[100:103], v[48:51], %1\n\t" \
    "ds_read_b128 v[80:83], %6 offset:16384\n\t" \
    "ds_read_b128 v[84:87], %7 offset:12288\n\t" \
    "v_mfma_f32_16x16x32_bf16 %2, v[96:99], v[52:55], %2\n\t" \
    "ds_read_b128 v[88:91], %7 offset:14336\n\t" \
    "ds_read_b128 v[92:95], %7 offset:16384\n\t" \
    "v_mfma_f32_16x16x32_bf16 %3, v[100:103], v[52:55], %3\n\t" \
    "ds_read_b64_tr_b16 v[112:113], %12 offset:24576\n\t" \
    "v_mfma_f32_16x16x32_bf16 %4, v[96:99], v[56:59], %4\n\t" \
    "ds_read_b64_tr_b16 v[114:115], %12 offset:25088\n\t" \
    "v_mfma_f32_16x16x32_bf16 %5, v[100:103], v[56:59], %5\n\t" \
    "ds_read_b64_tr_b16 v[116:117], %13 offset:24576\n\t" \
    "v_mfma_f32_16x16x32_bf16 %0, v[104:107], v[60:63], %0\n\t" \
    "ds_read_b64_tr_b16 v[118:119], %13 offset:25088\n\t" \
    "v_mfma_f32_16x16x32_bf16 %1, v[108:111], v[60:63], %1\n\t" \
    "ds_read_b64_tr_b16 v[120:121], %12 offset:28672\n\t" \
    "v_mfma_f32_16x16x32_bf16 %2, v[104:107], v[64:67], %2\n\t" \
    "ds_read_b64_tr_b16 v[122:123], %12 offset:29184\n\t" \
    "v_mfma_f32_16x16x32_bf16 %3, v[108:111], v[64:67], %3\n\t" \
    "ds_read_b64_tr_b16 v[124:125], %13 offset:28672\n\t" \
    "v_mfma_f32_16x16x32_bf16 %4, v[104:107], v[68:71], %4\n\t" \
    "ds_read_b64_tr_b16 v[126:127], %13 offset:29184\n\t" \
    "v_mfma_f32_16x16x32_bf16 %5, v[108:111], v[68:71], %5\n\t" \
    "s_waitcnt lgkmcnt(8)\n\t" \
    "s_barrier\n\t" \
    "s_waitcnt lgkmcnt(0)\n\t" \
    "v_mfma_f32_16x16x32_bf16 %0, v[112:115], v[72:75], %0\n\t" \
    "ds_read_b128 v[48:51], %6 offset:24576\n\t" \
    "ds_read_b128 v[52:55], %6 offset:26624\n\t" \
    "v_mfma_f32_16x16x32_bf16 %1, v[116:119], v[72:75], %1\n\t" \
    "ds_read_b128 v[56:59], %6 offset:28672\n\t" \
    "ds_read_b128 v[60:63], %7 offset:24576\n\t" \
    "v_mfma_f32_16x16x32_bf16 %2, v[112:115], v[76:79], %2\n\t" \
    "ds_read_b128 v[64:67], %7 offset:26624\n\t" \
    "ds_read_b128 v[68:71], %7 offset:28672\n\t" \
    "v_mfma_f32_16x16x32_bf16 %3, v[116:119], v[76:79], %3\n\t" \
    "ds_read_b64_tr_b16 v[96:97], %12 offset:32768\n\t" \
    "v_mfma_f32_16x16x32_bf16 %4, v[112:115], v[80:83], %4\n\t" \
    "ds_read_b64_tr_b16 v[98:99], %12 offset:33280\n\t" \
    "v_mfma_f32_16x16x32_bf16 %5, v[116:119], v[80:83], %5\n\t" \
    "ds_read_b64_tr_b16 v[100:101], %13 offset:32768\n\t" \
    "v_mfma_f32_16x16x32_bf16 %0, v[120:123], v[84:87], %0\n\t" \
    "ds_read_b64_tr_b16 v[102:103], %13 offset:33280\n\t" \
    "v_mfma_f32_16x16x32_bf16 %1, v[124:127], v[84:87], %1\n\t" \
    "ds_read_b64_tr_b16 v[104:105], %12 offset:36864\n\t" \
    "v_mfma_f32_16x16x32_bf16 %2, v[120:123], v[88:91], %2\n\t" \
    "ds_read_b64_tr_b16 v[106:107], %12 offset:37376\n\t" \
    "v_mfma_f32_16x16x32_bf16 %3, v[124:127], v[88:91], %3\n\t" \
    "ds_read_b64_tr_b16 v[108:109], %13 offset:36864\n\t" \
    "v_mfma_f32_16x16x32_bf16 %4, v[120:123], v[92:95], %4\n\t" \
    "ds_read_b64_tr_b16 v[110:111], %13 offset:37376\n\t" \
    "v_mfma_f32_16x16x32_bf16 %5, v[124:127], v[92:95], %5\n\t" \
    "s_waitcnt lgkmcnt(0)\n\t" \
    "v_mfma_f32_16x16x32_bf16 %0, v[96:99], v[48:51], %0\n\t" \
    "ds_read_b128 v[72:75], %8 offset:0\n\t" \
    "ds_read_b128 v[76:79], %8 offset:2048\n\t" \
    "v_mfma_f32_16x16x32_bf16 %1, v[100:103], v[48:51], %1\n\t" \
    "ds_read_b128 v[80:83], %8 offset:4096\n\t" \
    "ds_read_b128 v[84:87], %9 offset:0\n\t" \
    "v_mfma_f32_16x16x32_bf16 %2, v[96:99], v[52:55], %2\n\t" \
    "ds_read_b128 v[88:91], %9 offset:2048\n\t" \
    "ds_read_b128 v[92:95], %9 offset:4096\n\t" \
    "v_mfma_f32_16x16x32_bf16 %3, v[100:103], v[52:55], %3\n\t" \
    "ds_read_b64_tr_b16 v[112:113], %10 offset:0\n\t" \
    "v_mfma_f32_16x16x32_bf16 %4, v[96:99], v[56:59], %4\n\t" \
    "ds_read_b64_tr_b16 v[114:115], %10 offset:512\n\t" \
    "v_mfma_f32_16x16x32_bf16 %5, v[100:103], v[56:59], %5\n\t" \
    "ds_read_b64_tr_b16 v[116:117], %11 offset:0\n\t" \
    "v_mfma_f32_16x16x32_bf16 %0, v[104:107], v[60:63], %0\n\t" \
    "ds_read_b64_tr_b16 v[118:119], %11 offset:512\n\t" \
    "v_mfma_f32_16x16x32_bf16 %1, v[108:111], v[60:63], %1\n\t" \
    "ds_read_b64_tr_b16 v[120:121], %10 offset:4096\n\t" \
    "v_mfma_f32_16x16x32_bf16 %2, v[104:107], v[64:67], %2\n\t" \
    "ds_read_b64_tr_b16 v[122:123], %10 offset:4608\n\t" \
    "v_mfma_f32_16x16x32_bf16 %3, v[108:111], v[64:67], %3\n\t" \
    "ds_read_b64_tr_b16 v[124:125], %11 offset:4096\n\t" \
    "v_mfma_f32_16x16x32_bf16 %4, v[104:107], v[68:71], %4\n\t" \
    "ds_read_b64_tr_b16 v[126:127], %11 offset:4608\n\t" \
    "v_mfma_f32_16x16x32_bf16 %5, v[108:111], v[68:71], %5\n\t" \
    "s_waitcnt lgkmcnt(8)\n\t" \
    "s_barrier\n\t" \
    "s_waitcnt lgkmcnt(0)\n\t" \
    "v_mfma_f32_16x16x32_bf16 %0, v[112:115], v[72:75], %0\n\t" \
    "ds_read_b128 v[48:51], %8 offset:12288\n\t" \
    "ds_read_b128 v[52:55], %8 offset:14336\n\t" \
    "v_mfma_f32_16x16x32_bf16 %1, v[116:119], v[72:75], %1\n\t" \
    "ds_read_b128 v[56:59], %8 offset:16384\n\t" \
    "ds_read_b128 v[60:63], %9 offset:12288\n\t" \
    "v_mfma_f32_16x16x32_bf16 %2, v[112:115], v[76:79], %2\n\t" \
    "ds_read_b128 v[64:67], %9 offset:14336\n\t" \
    "ds_read_b128 v[68:71], %9 offset:16384\n\t" \
    "v_mfma_f32_16x16x32_bf16 %3, v[116:119], v[76:79], %3\n\t" \
    "ds_read_b64_tr_b16 v[96:97], %10 offset:8192\n\t" \
    "v_mfma_f32_16x16x32_bf16 %4, v[112:115], v[80:83], %4\n\t" \
    "ds_read_b64_tr_b16 v[98:99], %10 offset:8704\n\t" \
    "v_mfma_f32_16x16x32_bf16 %5, v[116:119], v[80:83], %5\n\t" \
    "ds_read_b64_tr_b16 v[100:101], %11 offset:8192\n\t" \
    "v_mfma_f32_16x16x32_bf16 %0, v[120:123], v[84:87], %0\n\t" \
    "ds_read_b64_tr_b16 v[102:103], %11 offset:8704\n\t" \
    "v_mfma_f32_16x16x32_bf16 %1, v[124:127], v[84:87], %1\n\t" \
    "ds_read_b64_tr_b16 v[104:105], %10 offset:12288\n\t" \
    "v_mfma_f32_16x16x32_bf16 %2, v[120:123], v[88:91], %2\n\t" \
    "ds_read_b64_tr_b16 v[106:107], %10 offset:12800\n\t" \
    "v_mfma_f32_16x16x32_bf16 %3, v[124:127], v[88:91], %3\n\t" \
    "ds_read_b64_tr_b16 v[108:109], %11 offset:12288\n\t" \
    "v_mfma_f32_16x16x32_bf16 %4, v[120:123], v[92:95], %4\n\t" \
    "ds_read_b64_tr_b16 v[110:111], %11 offset:12800\n\t" \
    "v_mfma_f32_16x16x32_bf16 %5, v[124:127], v[92:95], %5\n\t" \
    "s_waitcnt lgkmcnt(0)\n\t" \
    "v_mfma_f32_16x16x32_bf16 %0, v[96:99], v[48:51], %0\n\t" \
    "ds_read_b128 v[72:75], %8 offset:24576\n\t" \
    "ds_read_b128 v[76:79], %8 offset:26624\n\t" \
    "v_mfma_f32_16x16x32_bf16 %1, v[100:103], v[48:51], %1\n\t" \
    "ds_read_b128 v[80:83], %8 offset:28672\n\t" \
    "ds_read_b128 v[84:87], %9 offset:24576\n\t" \
    "v_mfma_f32_16x16x32_bf16 %2, v[96:99], v[52:55], %2\n\t" \
    "ds_read_b128 v[88:91], %9 offset:26624\n\t" \
    "ds_read_b128 v[92:95], %9 offset:28672\n\t" \
    "v_mfma_f32_16x16x32_bf16 %3, v[100:103], v[52:55], %3\n\t" \
    "ds_read_b64_tr_b16 v[112:113], %10 offset:16384\n\t" \
    "v_mfma_f32_16x16x32_bf16 %4, v[96:99], v[56:59], %4\n\t" \
    "ds_read_b64_tr_b16 v[114:115], %10 offset:16896\n\t" \
    "v_mfma_f32_16x16x32_bf16 %5, v[100:103], v[56:59], %5\n\t" \
    "ds_read_b64_tr_b16 v[116:117], %11 offset:16384\n\t" \
    "v_mfma_f32_16x16x32_bf16 %0, v[104:107], v[60:63], %0\n\t" \
    "ds_read_b64_tr_b16 v[118:119], %11 offset:16896\n\t" \
    "v_mfma_f32_16x16x32_bf16 %1, v[108:111], v[60:63], %1\n\t" \
    "ds_read_b64_tr_b16 v[120:121], %10 offset:20480\n\t" \
    "v_mfma_f32_16x16x32_bf16 %2, v[104:107], v[64:67], %2\n\t" \
    "ds_read_b64_tr_b16 v[122:123], %10 offset:20992\n\t" \
    "v_mfma_f32_16x16x32_bf16 %3, v[108:111], v[64:67], %3\n\t" \
    "ds_read_b64_tr_b16 v[124:125], %11 offset:20480\n\t" \
    "v_mfma_f32_16x16x32_bf16 %4, v[104:107], v[68:71], %4\n\t" \
    "ds_read_b64_tr_b16 v[126:127], %11 offset:20992\n\t" \
    "v_mfma_f32_16x16x32_bf16 %5, v[108:111], v[68:71], %5\n\t" \
    "s_waitcnt lgkmcnt(8)\n\t" \
    "s_barrier\n\t" \
    "s_waitcnt lgkmcnt(0)\n\t" \
    "v_mfma_f32_16x16x32_bf16 %0, v[112:115], v[72:75], %0\n\t" \
    "ds_read_b128 v[48:51], %6 offset:0\n\t" \
    "ds_read_b128 v[52:55], %6 offset:2048\n\t" \
    "v_mfma_f32_16x16x32_bf16 %1, v[116:119], v[72:75], %1\n\t" \
    "ds_read_b128 v[56:59], %6 offset:4096\n\t" \
    "ds_read_b128 v[60:63], %7 offset:0\n\t" \
    "v_mfma_f32_16x16x32_bf16 %2, v[112:115], v[76:79], %2\n\t" \
    "ds_read_b128 v[64:67], %7 offset:2048\n\t" \
    "ds_read_b128 v[68:71], %7 offset:4096\n\t" \
    "v_mfma_f32_16x16x32_bf16 %3, v[116:119], v[76:79], %3\n\t" \
    "ds_read_b64_tr_b16 v[96:97], %10 offset:24576\n\t" \
    "v_mfma_f32_16x16x32_bf16 %4, v[112:115], v[80:83], %4\n\t" \
    "ds_read_b64_tr_b16 v[98:99], %10 offset:25088\n\t" \
    "v_mfma_f32_16x16x32_bf16 %5, v[116:119], v[80:83], %5\n\t" \
    "ds_read_b64_tr_b16 v[100:101], %11 offset:24576\n\t" \
    "v_mfma_f32_16x16x32_bf16 %0, v[120:123], v[84:87], %0\n\t" \
    "ds_read_b64_tr_b16 v[102:103], %11 offset:25088\n\t" \
    "v_mfma_f32_16x16x32_bf16 %1, v[124:127], v[84:87], %1\n\t" \
    "ds_read_b64_tr_b16 v[104:105], %10 offset:28672\n\t" \
    "v_mfma_f32_16x16x32_bf16 %2, v[120:123], v[88:91], %2\n\t" \
    "ds_read_b64_tr_b16 v[106:107], %10 offset:29184\n\t" \
    "v_mfma_f32_16x16x32_bf16 %3, v[124:127], v[88:91], %3\n\t" \
    "ds_read_b64_tr_b16 v[108:109], %11 offset:28672\n\t" \
    "v_mfma_f32_16x16x32_bf16 %4, v[120:123], v[92:95], %4\n\t" \
    "ds_read_b64_tr_b16 v[110:111], %11 offset:29184\n\t" \
    "v_mfma_f32_16x16x32_bf16 %5, v[124:127], v[92:95], %5\n\t" \
    "s_waitcnt lgkmcnt(0)\n\t" \
    "v_mfma_f32_16x16x32_bf16 %0, v[96:99], v[48:51], %0\n\t" \
    "ds_read_b128 v[72:75], %6 offset:12288\n\t" \
    "ds_read_b128 v[76:79], %6 offset:14336\n\t" \
    "v_mfma_f32_16x16x32_bf16 %1, v[100:103], v[48:51], %1\n\t" \
    "ds_read_b128 v[80:83], %6 offset:16384\n\t" \
    "ds_read_b128 v[84:87], %7 offset:12288\n\t" \
    "v_mfma_f32_16x16x32_bf16 %2, v[96:99], v[52:55], %2\n\t" \
    "ds_read_b128 v[88:91], %7 offset:14336\n\t" \
    "ds_read_b128 v[92:95], %7 offset:16384\n\t" \
    "v_mfma_f32_16x16x32_bf16 %3, v[100:103], v[52:55], %3\n\t" \
    "ds_read_b64_tr_b16 v[112:113], %10 offset:32768\n\t" \
    "v_mfma_f32_16x16x32_bf16 %4, v[96:99], v[56:59], %4\n\t" \
    "ds_read_b64_tr_b16 v[114:115], %10 offset:33280\n\t" \
    "v_mfma_f32_16x16x32_bf16 %5, v[100:103], v[56:59], %5\n\t" \
    "ds_read_b64_tr_b16 v[116:117], %11 offset:32768\n\t" \
    "v_mfma_f32_16x16x32_bf16 %0, v[104:107], v[60:63], %0\n\t" \
    "ds_read_b64_tr_b16 v[118:119], %11 offset:33280\n\t" \
    "v_mfma_f32_16x16x32_bf16 %1, v[108:111], v[60:63], %1\n\t" \
    "ds_read_b64_tr_b16 v[120:121], %10 offset:36864\n\t" \
    "v_mfma_f32_16x16x32_bf16 %2, v[104:107], v[64:67], %2\n\t" \
    "ds_read_b64_tr_b16 v[122:123], %10 offset:37376\n\t" \
    "v_mfma_f32_16x16x32_bf16 %3, v[108:111], v[64:67], %3\n\t" \
    "ds_read_b64_tr_b16 v[124:125], %11 offset:36864\n\t" \
    "v_mfma_f32_16x16x32_bf16 %4, v[104:107], v[68:71], %4\n\t" \
    "ds_read_b64_tr_b16 v[126:127], %11 offset:37376\n\t" \
    "v_mfma_f32_16x16x32_bf16 %5, v[108:111], v[68:71], %5\n\t" \
    "s_waitcnt lgkmcnt(8)\n\t" \
    "s_barrier\n\t" \
    "s_waitcnt lgkmcnt(0)\n\t" \
    "v_mfma_f32_16x16x32_bf16 %0, v[112:115], v[72:75], %0\n\t" \
    "ds_read_b128 v[48:51], %6 offset:24576\n\t" \
    "ds_read_b128 v[52:55], %6 offset:26624\n\t" \
    "v_mfma_f32_16x16x32_bf16 %1, v[116:119], v[72:75], %1\n\t" \
    "ds_read_b128 v[56:59], %6 offset:28672\n\t" \
    "ds_read_b128 v[60:63], %7 offset:24576\n\t" \
    "v_mfma_f32_16x16x32_bf16 %2, v[112:115], v[76:79], %2\n\t" \
    "ds_read_b128 v[64:67], %7 offset:26624\n\t" \
    "ds_read_b128 v[68:71], %7 offset:28672\n\t" \
    "v_mfma_f32_16x16x32_bf16 %3, v[116:119], v[76:79], %3\n\t" \
    "ds_read_b64_tr_b16 v[96:97], %10 offset:40960\n\t" \
    "v_mfma_f32_16x16x32_bf16 %4, v[112:115], v[80:83], %4\n\t" \
    "ds_read_b64_tr_b16 v[98:99], %10 offset:41472\n\t" \
    "v_mfma_f32_16x16x32_bf16 %5, v[116:119], v[80:83], %5\n\t" \
    "ds_read_b64_tr_b16 v[100:101], %11 offset:40960\n\t" \
    "v_mfma_f32_16x16x32_bf16 %0, v[120:123], v[84:87], %0\n\t" \
    "ds_read_b64_tr_b16 v[102:103], %11 offset:41472\n\t" \
    "v_mfma_f32_16x16x32_bf16 %1, v[124:127], v[84:87], %1\n\t" \
    "ds_read_b64_tr_b16 v[104:105], %10 offset:45056\n\t" \
    "v_mfma_f32_16x16x32_bf16 %2, v[120:123], v[88:91], %2\n\t" \
    "ds_read_b64_tr_b16 v[106:107], %10 offset:45568\n\t" \
    "v_mfma_f32_16x16x32_bf16 %3, v[124:127], v[88:91], %3\n\t" \
    "ds_read_b64_tr_b16 v[108:109], %11 offset:45056\n\t" \
    "v_mfma_f32_16x16x32_bf16 %4, v[120:123], v[92:95], %4\n\t" \
    "ds_read_b64_tr_b16 v[110:111], %11 offset:45568\n\t" \
    "v_mfma_f32_16x16x32_bf16 %5, v[124:127], v[92:95], %5\n\t" \
    "s_waitcnt lgkmcnt(0)\n\t" \
    "v_mfma_f32_16x16x32_bf16 %0, v[96:99], v[48:51], %0\n\t" \
    "v_mfma_f32_16x16x32_bf16 %1, v[100:103], v[48:51], %1\n\t" \
    "v_mfma_f32_16x16x32_bf16 %2, v[96:99], v[52:55], %2\n\t" \
    "v_mfma_f32_16x16x32_bf16 %3, v[100:103], v[52:55], %3\n\t" \
    "v_mfma_f32_16x16x32_bf16 %4, v[96:99], v[56:59], %4\n\t" \
    "v_mfma_f32_16x16x32_bf16 %5, v[100:103], v[56:59], %5\n\t" \
    "v_mfma_f32_16x16x32_bf16 %0, v[104:107], v[60:63], %0\n\t" \
    "v_mfma_f32_16x16x32_bf16 %1, v[108:111], v[60:63], %1\n\t" \
    "v_mfma_f32_16x16x32_bf16 %2, v[104:107], v[64:67], %2\n\t" \
    "v_mfma_f32_16x16x32_bf16 %3, v[108:111], v[64:67], %3\n\t" \
    "v_mfma_f32_16x16x32_bf16 %4, v[104:107], v[68:71], %4\n\t" \
    "v_mfma_f32_16x16x32_bf16 %5, v[108:111], v[68:71], %5\n\t" \
    "s_nop 7\n\t" \
    "s_nop 1\n\t"
#define P2_LOAD3 \
    "s_add_u32 m0, %4, 0\n\t" \
    "s_nop 0\n\t" \
    "buffer_load_dwordx4 %6, %2, %0 offen sc1 lds\n\t" \
    "buffer_load_dwordx4 %7, %2, %0 offen offset:1024 sc1 lds\n\t" \
    "buffer_load_dwordx4 %8, %2, %0 offen offset:2048 sc1 lds\n\t" \
    "s_add_u32 %0, %0, 128\n\t" \
    "s_cmp_eq_u32 %0, %11\n\t" \
    "s_cselect_b32 %0, %12, %0\n\t" \
    "s_add_u32 m0, %4, 12288\n\t" \
    "s_nop 0\n\t" \
    "buffer_load_dwordx4 %6, %2, %0 offen sc1 lds\n\t" \
    "buffer_load_dwordx4 %7, %2, %0 offen offset:1024 sc1 lds\n\t" \
    "buffer_load_dwordx4 %8, %2, %0 offen offset:2048 sc1 lds\n\t" \
    "s_add_u32 %0, %0, 128\n\t" \
    "s_cmp_eq_u32 %0, %11\n\t" \
    "s_cselect_b32 %0, %12, %0\n\t" \
    "s_add_u32 m0, %4, 24576\n\t" \
    "s_nop 0\n\t" \
    "buffer_load_dwordx4 %6, %2, %0 offen sc1 lds\n\t" \
    "buffer_load_dwordx4 %7, %2, %0 offen offset:1024 sc1 lds\n\t" \
    "buffer_load_dwordx4 %8, %2, %0 offen offset:2048 sc1 lds\n\t" \
    "s_add_u32 %0, %0, 128\n\t" \
    "s_cmp_eq_u32 %0, %11\n\t" \
    "s_cselect_b32 %0, %12, %0\n\t" \
    "s_add_u32 m0, %4, 36864\n\t" \
    "s_nop 0\n\t" \
    "buffer_load_dwordx4 %6, %2, %0 offen sc1 lds\n\t" \
    "buffer_load_dwordx4 %7, %2, %0 offen offset:1024 sc1 lds\n\t" \
    "buffer_load_dwordx4 %8, %2, %0 offen offset:2048 sc1 lds\n\t" \
    "s_add_u32 %0, %0, 128\n\t" \
    "s_cmp_eq_u32 %0, %11\n\t" \
    "s_cselect_b32 %0, %12, %0\n\t" \
    "s_waitcnt vmcnt(6)\n\t" \
    "s_barrier\n\t" \
    "s_add_u32 m0, %4, 49152\n\t" \
    "s_nop 0\n\t" \
    "buffer_load_dwordx4 %6, %2, %0 offen sc1 lds\n\t" \
    "buffer_load_dwordx4 %7, %2, %0 offen offset:1024 sc1 lds\n\t" \
    "buffer_load_dwordx4 %8, %2, %0 offen offset:2048 sc1 lds\n\t" \
    "s_add_u32 %0, %0, 128\n\t" \
    "s_cmp_eq_u32 %0, %11\n\t" \
    "s_cselect_b32 %0, %12, %0\n\t" \
    "s_add_u32 m0, %4, 61440\n\t" \
    "s_nop 0\n\t" \
    "buffer_load_dwordx4 %6, %2, %0 offen sc1 lds\n\t" \
    "buffer_load_dwordx4 %7, %2, %0 offen offset:1024 sc1 lds\n\t" \
    "buffer_load_dwordx4 %8, %2, %0 offen offset:2048 sc1 lds\n\t" \
    "s_add_u32 %0, %0, 128\n\t" \
    "s_cmp_eq_u32 %0, %11\n\t" \
    "s_cselect_b32 %0, %12, %0\n\t" \
    "s_waitcnt vmcnt(6)\n\t" \
    "s_barrier\n\t" \
    "s_add_u32 m0, %4, 0\n\t" \
    "s_nop 0\n\t" \
    "buffer_load_dwordx4 %6, %2, %0 offen sc1 lds\n\t" \
    "buffer_load_dwordx4 %7, %2, %0 offen offset:1024 sc1 lds\n\t" \
    "buffer_load_dwordx4 %8, %2, %0 offen offset:2048 sc1 lds\n\t" \
    "s_add_u32 %0, %0, 128\n\t" \
    "s_cmp_eq_u32 %0, %11\n\t" \
    "s_cselect_b32 %0, %12, %0\n\t" \
    "s_add_u32 m0, %4, 12288\n\t" \
    "s_nop 0\n\t" \
    "buffer_load_dwordx4 %6, %2, %0 offen sc1 lds\n\t" \
    "buffer_load_dwordx4 %7, %2, %0 offen offset:1024 sc1 lds\n\t" \
    "buffer_load_dwordx4 %8, %2, %0 offen offset:2048 sc1 lds\n\t" \
    "s_add_u32 %0, %0, 128\n\t" \
    "s_cmp_eq_u32 %0, %11\n\t" \
    "s_cselect_b32 %0, %12, %0\n\t" \
    "s_waitcnt vmcnt(6)\n\t" \
    "s_barrier\n\t" \
    "s_add_u32 m0, %4, 24576\n\t" \
    "s_nop 0\n\t" \
    "buffer_load_dwordx4 %6, %2, %0 offen sc1 lds\n\t" \
    "buffer_load_dwordx4 %7, %2, %0 offen offset:1024 sc1 lds\n\t" \
    "buffer_load_dwordx4 %8, %2, %0 offen offset:2048 sc1 lds\n\t" \
    "s_add_u32 %0, %0, 128\n\t" \
    "s_cmp_eq_u32 %0, %11\n\t" \
    "s_cselect_b32 %0, %12, %0\n\t" \
    "s_add_u32 m0, %4, 36864\n\t" \
    "s_nop 0\n\t" \
    "buffer_load_dwordx4 %6, %2, %0 offen sc1 lds\n\t" \
    "buffer_load_dwordx4 %7, %2, %0 offen offset:1024 sc1 lds\n\t" \
    "buffer_load_dwordx4 %8, %2, %0 offen offset:2048 sc1 lds\n\t" \
    "s_add_u32 %0, %0, 128\n\t" \
    "s_cmp_eq_u32 %0, %11\n\t" \
    "s_cselect_b32 %0, %12, %0\n\t" \
    "s_waitcnt vmcnt(6)\n\t" \
    "s_barrier\n\t" \
    "s_add_u32 m0, %4, 49152\n\t" \
    "s_nop 0\n\t" \
    "buffer_load_dwordx4 %6, %2, %0 offen sc1 lds\n\t" \
    "buffer_load_dwordx4 %7, %2, %0 offen offset:1024 sc1 lds\n\t" \
    "buffer_load_dwordx4 %8, %2, %0 offen offset:2048 sc1 lds\n\t" \
    "s_add_u32 %0, %0, 128\n\t" \
    "s_cmp_eq_u32 %0, %11\n\t" \
    "s_cselect_b32 %0, %12, %0\n\t" \
    "s_add_u32 m0, %4, 61440\n\t" \
    "s_nop 0\n\t" \
    "buffer_load_dwordx4 %6, %2, %0 offen sc1 lds\n\t" \
    "buffer_load_dwordx4 %7, %2, %0 offen offset:1024 sc1 lds\n\t" \
    "buffer_load_dwordx4 %8, %2, %0 offen offset:2048 sc1 lds\n\t" \
    "s_add_u32 %0, %0, 128\n\t" \
    "s_cmp_eq_u32 %0, %11\n\t" \
    "s_cselect_b32 %0, %12, %0\n\t" \
    "s_waitcnt vmcnt(6)\n\t" \
    "s_barrier\n\t" \
    "s_add_u32 m0, %4, 0\n\t" \
    "s_nop 0\n\t" \
    "buffer_load_dwordx4 %6, %2, %0 offen sc1 lds\n\t" \
    "buffer_load_dwordx4 %7, %2, %0 offen offset:1024 sc1 lds\n\t" \
    "buffer_load_dwordx4 %8, %2, %0 offen offset:2048 sc1 lds\n\t" \
    "s_add_u32 %0, %0, 128\n\t" \
    "s_cmp_eq_u32 %0, %11\n\t" \
    "s_cselect_b32 %0, %12, %0\n\t" \
    "s_add_u32 m0, %4, 12288\n\t" \
    "s_nop 0\n\t" \
    "buffer_load_dwordx4 %6, %2, %0 offen sc1 lds\n\t" \
    "buffer_load_dwordx4 %7, %2, %0 offen offset:1024 sc1 lds\n\t" \
    "buffer_load_dwordx4 %8, %2, %0 offen offset:2048 sc1 lds\n\t" \
    "s_add_u32 %0, %0, 128\n\t" \
    "s_cmp_eq_u32 %0, %11\n\t" \
    "s_cselect_b32 %0, %12, %0\n\t" \
    "s_waitcnt vmcnt(6)\n\t" \
    "s_barrier\n\t" \
    "s_add_u32 m0, %4, 24576\n\t" \
    "s_nop 0\n\t" \
    "buffer_load_dwordx4 %6, %2, %0 offen sc1 lds\n\t" \
    "buffer_load_dwordx4 %7, %2, %0 offen offset:1024 sc1 lds\n\t" \
    "buffer_load_dwordx4 %8, %2, %0 offen offset:2048 sc1 lds\n\t" \
    "s_add_u32 %0, %0, 128\n\t" \
    "s_cmp_eq_u32 %0, %11\n\t" \
    "s_cselect_b32 %0, %12, %0\n\t" \
    "s_add_u32 m0, %4, 36864\n\t" \
    "s_nop 0\n\t" \
    "buffer_load_dwordx4 %6, %2, %0 offen sc1 lds\n\t" \
    "buffer_load_dwordx4 %7, %2, %0 offen offset:1024 sc1 lds\n\t" \
    "buffer_load_dwordx4 %8, %2, %0 offen offset:2048 sc1 lds\n\t" \
    "s_add_u32 %0, %0, 128\n\t" \
    "s_cmp_eq_u32 %0, %11\n\t" \
    "s_cselect_b32 %0, %12, %0\n\t" \
    "s_waitcnt vmcnt(6)\n\t" \
    "s_barrier\n\t" \
    "s_add_u32 m0, %4, 49152\n\t" \
    "s_nop 0\n\t" \
    "buffer_load_dwordx4 %6, %2, %0 offen sc1 lds\n\t" \
    "buffer_load_dwordx4 %7, %2, %0 offen offset:1024 sc1 lds\n\t" \
    "buffer_load_dwordx4 %8, %2, %0 offen offset:2048 sc1 lds\n\t" \
    "s_add_u32 %0, %0, 128\n\t" \
    "s_cmp_eq_u32 %0, %11\n\t" \
    "s_cselect_b32 %0, %12, %0\n\t" \
    "s_add_u32 m0, %4, 61440\n\t" \
    "s_nop 0\n\t" \
    "buffer_load_dwordx4 %6, %2, %0 offen sc1 lds\n\t" \
    "buffer_load_dwordx4 %7, %2, %0 offen offset:1024 sc1 lds\n\t" \
    "buffer_load_dwordx4 %8, %2, %0 offen offset:2048 sc1 lds\n\t" \
    "s_add_u32 %0, %0, 128\n\t" \
    "s_cmp_eq_u32 %0, %11\n\t" \
    "s_cselect_b32 %0, %12, %0\n\t" \
    "s_add_u32 m0, %5, 0\n\t" \
    "s_nop 0\n\t" \
    "buffer_load_dwordx4 %9, %3, %1 offen lds\n\t" \
    "buffer_load_dwordx4 %10, %3, %1 offen offset:1024 lds\n\t" \
    "s_add_u32 %1, %1, 81920\n\t" \
    "s_cmp_eq_u32 %1, %13\n\t" \
    "s_cselect_b32 %1, %14, %1\n\t" \
    "s_waitcnt vmcnt(8)\n\t" \
    "s_barrier\n\t" \
    "s_add_u32 m0, %4, 0\n\t" \
    "s_nop 0\n\t" \
    "buffer_load_dwordx4 %6, %2, %0 offen sc1 lds\n\t" \
    "buffer_load_dwordx4 %7, %2, %0 offen offset:1024 sc1 lds\n\t" \
    "buffer_load_dwordx4 %8, %2, %0 offen offset:2048 sc1 lds\n\t" \
    "s_add_u32 %0, %0, 128\n\t" \
    "s_cmp_eq_u32 %0, %11\n\t" \
    "s_cselect_b32 %0, %12, %0\n\t" \
    "s_add_u32 m0, %4, 12288\n\t" \
    "s_nop 0\n\t" \
    "buffer_load_dwordx4 %6, %2, %0 offen sc1 lds\n\t" \
    "buffer_load_dwordx4 %7, %2, %0 offen offset:1024 sc1 lds\n\t" \
    "buffer_load_dwordx4 %8, %2, %0 offen offset:2048 sc1 lds\n\t" \
    "s_add_u32 %0, %0, 128\n\t" \
    "s_cmp_eq_u32 %0, %11\n\t" \
    "s_cselect_b32 %0, %12, %0\n\t" \
    "s_add_u32 m0, %5, 8192\n\t" \
    "s_nop 0\n\t" \
    "buffer_load_dwordx4 %9, %3, %1 offen lds\n\t" \
    "buffer_load_dwordx4 %10, %3, %1 offen offset:1024 lds\n\t" \
    "s_add_u32 %1, %1, 81920\n\t" \
    "s_cmp_eq_u32 %1, %13\n\t" \
    "s_cselect_b32 %1, %14, %1\n\t" \
    "s_add_u32 m0, %5, 16384\n\t" \
    "s_nop 0\n\t" \
    "buffer_load_dwordx4 %9, %3, %1 offen lds\n\t" \
    "buffer_load_dwordx4 %10, %3, %1 offen offset:1024 lds\n\t" \
    "s_add_u32 %1, %1, 81920\n\t" \
    "s_cmp_eq_u32 %1, %13\n\t" \
    "s_cselect_b32 %1, %14, %1\n\t" \
    "s_waitcnt vmcnt(12)\n\t" \
    "s_barrier\n\t" \
    "s_add_u32 m0, %4, 24576\n\t" \
    "s_nop 0\n\t" \
    "buffer_load_dwordx4 %6, %2, %0 offen sc1 lds\n\t" \
    "buffer_load_dwordx4 %7, %2, %0 offen offset:1024 sc1 lds\n\t" \
    "buffer_load_dwordx4 %8, %2, %0 offen offset:2048 sc1 lds\n\t" \
    "s_add_u32 %0, %0, 128\n\t" \
    "s_cmp_eq_u32 %0, %11\n\t" \
    "s_cselect_b32 %0, %12, %0\n\t" \
    "s_add_u32 m0, %4, 36864\n\t" \
    "s_nop 0\n\t" \
    "buffer_load_dwordx4 %6, %2, %0 offen sc1 lds\n\t" \
    "buffer_load_dwordx4 %7, %2, %0 offen offset:1024 sc1 lds\n\t" \
    "buffer_load_dwordx4 %8, %2, %0 offen offset:2048 sc1 lds\n\t" \
    "s_add_u32 %0, %0, 128\n\t" \
    "s_cmp_eq_u32 %0, %11\n\t" \
    "s_cselect_b32 %0, %12, %0\n\t" \
    "s_add_u32 m0, %5, 24576\n\t" \
    "s_nop 0\n\t" \
    "buffer_load_dwordx4 %9, %3, %1 offen lds\n\t" \
    "buffer_load_dwordx4 %10, %3, %1 offen offset:1024 lds\n\t" \
    "s_add_u32 %1, %1, 81920\n\t" \
    "s_cmp_eq_u32 %1, %13\n\t" \
    "s_cselect_b32 %1, %14, %1\n\t" \
    "s_add_u32 m0, %5, 32768\n\t" \
    "s_nop 0\n\t" \
    "buffer_load_dwordx4 %9, %3, %1 offen lds\n\t" \
    "buffer_load_dwordx4 %10, %3, %1 offen offset:1024 lds\n\t" \
    "s_add_u32 %1, %1, 81920\n\t" \
    "s_cmp_eq_u32 %1, %13\n\t" \
    "s_cselect_b32 %1, %14, %1\n\t" \
    "s_waitcnt vmcnt(14)\n\t" \
    "s_barrier\n\t" \
    "s_add_u32 m0, %4, 49152\n\t" \
    "s_nop 0\n\t" \
    "buffer_load_dwordx4 %6, %2, %0 offen sc1 lds\n\t" \
    "buffer_load_dwordx4 %7, %2, %0 offen offset:1024 sc1 lds\n\t" \
    "buffer_load_dwordx4 %8, %2, %0 offen offset:2048 sc1 lds\n\t" \
    "s_add_u32 %0, %0, 128\n\t" \
    "s_cmp_eq_u32 %0, %11\n\t" \
    "s_cselect_b32 %0, %12, %0\n\t" \
    "s_add_u32 m0, %4, 61440\n\t" \
    "s_nop 0\n\t" \
    "buffer_load_dwordx4 %6, %2, %0 offen sc1 lds\n\t" \
    "buffer_load_dwordx4 %7, %2, %0 offen offset:1024 sc1 lds\n\t" \
    "buffer_load_dwordx4 %8, %2, %0 offen offset:2048 sc1 lds\n\t" \
    "s_add_u32 %0, %0, 128\n\t" \
    "s_cmp_eq_u32 %0, %11\n\t" \
    "s_cselect_b32 %0, %12, %0\n\t" \
    "s_add_u32 m0, %5, 40960\n\t" \
    "s_nop 0\n\t" \
    "buffer_load_dwordx4 %9, %3, %1 offen lds\n\t" \
    "buffer_load_dwordx4 %10, %3, %1 offen offset:1024 lds\n\t" \
    "s_add_u32 %1, %1, 81920\n\t" \
    "s_cmp_eq_u32 %1, %13\n\t" \
    "s_cselect_b32 %1, %14, %1\n\t" \
    "s_waitcnt vmcnt(12)\n\t" \
    "s_barrier\n\t" \
    "s_add_u32 m0, %4, 0\n\t" \
    "s_nop 0\n\t" \
    "buffer_load_dwordx4 %6, %2, %0 offen sc1 lds\n\t" \
    "buffer_load_dwordx4 %7, %2, %0 offen offset:1024 sc1 lds\n\t" \
    "buffer_load_dwordx4 %8, %2, %0 offen offset:2048 sc1 lds\n\t" \
    "s_add_u32 %0, %0, 128\n\t" \
    "s_cmp_eq_u32 %0, %11\n\t" \
    "s_cselect_b32 %0, %12, %0\n\t" \
    "s_add_u32 m0, %4, 12288\n\t" \
    "s_nop 0\n\t" \
    "buffer_load_dwordx4 %6, %2, %0 offen sc1 lds\n\t" \
    "buffer_load_dwordx4 %7, %2, %0 offen offset:1024 sc1 lds\n\t" \
    "buffer_load_dwordx4 %8, %2, %0 offen offset:2048 sc1 lds\n\t" \
    "s_add_u32 %0, %0, 128\n\t" \
    "s_cmp_eq_u32 %0, %11\n\t" \
    "s_cselect_b32 %0, %12, %0\n\t" \
    "s_waitcnt vmcnt(8)\n\t" \
    "s_barrier\n\t" \
    "s_add_u32 m0, %4, 24576\n\t" \
    "s_nop 0\n\t" \
    "buffer_load_dwordx4 %6, %2, %0 offen sc1 lds\n\t" \
    "buffer_load_dwordx4 %7, %2, %0 offen offset:1024 sc1 lds\n\t" \
    "buffer_load_dwordx4 %8, %2, %0 offen offset:2048 sc1 lds\n\t" \
    "s_add_u32 %0, %0, 128\n\t" \
    "s_cmp_eq_u32 %0, %11\n\t" \
    "s_cselect_b32 %0, %12, %0\n\t" \
    "s_waitcnt vmcnt(3)\n\t" \
    "s_barrier\n\t" \
    "s_waitcnt vmcnt(0)\n\t" \
    "s_barrier\n\t"
#define P2_LOAD2 \
    "s_add_u32 m0, %4, 0\n\t" \
    "s_nop 0\n\t" \
    "buffer_load_dwordx4 %6, %2, %0 offen sc1 lds\n\t" \
    "buffer_load_dwordx4 %7, %2, %0 offen offset:1024 sc1 lds\n\t" \
    "s_add_u32 %0, %0, 128\n\t" \
    "s_cmp_eq_u32 %0, %11\n\t" \
    "s_cselect_b32 %0, %12, %0\n\t" \
    "s_add_u32 m0, %4, 12288\n\t" \
    "s_nop 0\n\t" \
    "buffer_load_dwordx4 %6, %2, %0 offen sc1 lds\n\t" \
    "buffer_load_dwordx4 %7, %2, %0 offen offset:1024 sc1 lds\n\t" \
    "s_add_u32 %0, %0, 128\n\t" \
    "s_cmp_eq_u32 %0, %11\n\t" \
    "s_cselect_b32 %0, %12, %0\n\t" \
    "s_add_u32 m0, %4, 24576\n\t" \
    "s_nop 0\n\t" \
    "buffer_load_dwordx4 %6, %2, %0 offen sc1 lds\n\t" \
    "buffer_load_dwordx4 %7, %2, %0 offen offset:1024 sc1 lds\n\t" \
    "s_add_u32 %0, %0, 128\n\t" \
    "s_cmp_eq_u32 %0, %11\n\t" \
    "s_cselect_b32 %0, %12, %0\n\t" \
    "s_add_u32 m0, %4, 36864\n\t" \
    "s_nop 0\n\t" \
    "buffer_load_dwordx4 %6, %2, %0 offen sc1 lds\n\t" \
    "buffer_load_dwordx4 %7, %2, %0 offen offset:1024 sc1 lds\n\t" \
    "s_add_u32 %0, %0, 128\n\t" \
    "s_cmp_eq_u32 %0, %11\n\t" \
    "s_cselect_b32 %0, %12, %0\n\t" \
    "s_waitcnt vmcnt(4)\n\t" \
    "s_barrier\n\t" \
    "s_add_u32 m0, %4, 49152\n\t" \
    "s_nop 0\n\t" \
    "buffer_load_dwordx4 %6, %2, %0 offen sc1 lds\n\t" \
    "buffer_load_dwordx4 %7, %2, %0 offen offset:1024 sc1 lds\n\t" \
    "s_add_u32 %0, %0, 128\n\t" \
    "s_cmp_eq_u32 %0, %11\n\t" \
    "s_cselect_b32 %0, %12, %0\n\t" \
    "s_add_u32 m0, %4, 61440\n\t" \
    "s_nop 0\n\t" \
    "buffer_load_dwordx4 %6, %2, %0 offen sc1 lds\n\t" \
    "buffer_load_dwordx4 %7, %2, %0 offen offset:1024 sc1 lds\n\t" \
    "s_add_u32 %0, %0, 128\n\t" \
    "s_cmp_eq_u32 %0, %11\n\t" \
    "s_cselect_b32 %0, %12, %0\n\t" \
    "s_waitcnt vmcnt(4)\n\t" \
    "s_barrier\n\t" \
    "s_add_u32 m0, %4, 0\n\t" \
    "s_nop 0\n\t" \
    "buffer_load_dwordx4 %6, %2, %0 offen sc1 lds\n\t" \
    "buffer_load_dwordx4 %7, %2, %0 offen offset:1024 sc1 lds\n\t" \
    "s_add_u32 %0, %0, 128\n\t" \
    "s_cmp_eq_u32 %0, %11\n\t" \
    "s_cselect_b32 %0, %12, %0\n\t" \
    "s_add_u32 m0, %4, 12288\n\t" \
    "s_nop 0\n\t" \
    "buffer_load_dwordx4 %6, %2, %0 offen sc1 lds\n\t" \
    "buffer_load_dwordx4 %7, %2, %0 offen offset:1024 sc1 lds\n\t" \
    "s_add_u32 %0, %0, 128\n\t" \
    "s_cmp_eq_u32 %0, %11\n\t" \
    "s_cselect_b32 %0, %12, %0\n\t" \
    "s_waitcnt vmcnt(4)\n\t" \
    "s_barrier\n\t" \
    "s_add_u32 m0, %4, 24576\n\t" \
    "s_nop 0\n\t" \
    "buffer_load_dwordx4 %6, %2, %0 offen sc1 lds\n\t" \
    "buffer_load_dwordx4 %7, %2, %0 offen offset:1024 sc1 lds\n\t" \
    "s_add_u32 %0, %0, 128\n\t" \
    "s_cmp_eq_u32 %0, %11\n\t" \
    "s_cselect_b32 %0, %12, %0\n\t" \
    "s_add_u32 m0, %4, 36864\n\t" \
    "s_nop 0\n\t" \
    "buffer_load_dwordx4 %6, %2, %0 offen sc1 lds\n\t" \
    "buffer_load_dwordx4 %7, %2, %0 offen offset:1024 sc1 lds\n\t" \
    "s_add_u32 %0, %0, 128\n\t" \
    "s_cmp_eq_u32 %0, %11\n\t" \
    "s_cselect_b32 %0, %12, %0\n\t" \
    "s_waitcnt vmcnt(4)\n\t" \
    "s_barrier\n\t" \
    "s_add_u32 m0, %4, 49152\n\t" \
    "s_nop 0\n\t" \
    "buffer_load_dwordx4 %6, %2, %0 offen sc1 lds\n\t" \
    "buffer_load_dwordx4 %7, %2, %0 offen offset:1024 sc1 lds\n\t" \
    "s_add_u32 %0, %0, 128\n\t" \
    "s_cmp_eq_u32 %0, %11\n\t" \
    "s_cselect_b32 %0, %12, %0\n\t" \
    "s_add_u32 m0, %4, 61440\n\t" \
    "s_nop 0\n\t" \
    "buffer_load_dwordx4 %6, %2, %0 offen sc1 lds\n\t" \
    "buffer_load_dwordx4 %7, %2, %0 offen offset:1024 sc1 lds\n\t" \
    "s_add_u32 %0, %0, 128\n\t" \
    "s_cmp_eq_u32 %0, %11\n\t" \
    "s_cselect_b32 %0, %12, %0\n\t" \
    "s_waitcnt vmcnt(4)\n\t" \
    "s_barrier\n\t" \
    "s_add_u32 m0, %4, 0\n\t" \
    "s_nop 0\n\t" \
    "buffer_load_dwordx4 %6, %2, %0 offen sc1 lds\n\t" \
    "buffer_load_dwordx4 %7, %2, %0 offen offset:1024 sc1 lds\n\t" \
    "s_add_u32 %0, %0, 128\n\t" \
    "s_cmp_eq_u32 %0, %11\n\t" \
    "s_cselect_b32 %0, %12, %0\n\t" \
    "s_add_u32 m0, %4, 12288\n\t" \
    "s_nop 0\n\t" \
    "buffer_load_dwordx4 %6, %2, %0 offen sc1 lds\n\t" \
    "buffer_load_dwordx4 %7, %2, %0 offen offset:1024 sc1 lds\n\t" \
    "s_add_u32 %0, %0, 128\n\t" \
    "s_cmp_eq_u32 %0, %11\n\t" \
    "s_cselect_b32 %0, %12, %0\n\t" \
    "s_waitcnt vmcnt(4)\n\t" \
    "s_barrier\n\t" \
    "s_add_u32 m0, %4, 24576\n\t" \
    "s_nop 0\n\t" \
    "buffer_load_dwordx4 %6, %2, %0 offen sc1 lds\n\t" \
    "buffer_load_dwordx4 %7, %2, %0 offen offset:1024 sc1 lds\n\t" \
    "s_add_u32 %0, %0, 128\n\t" \
    "s_cmp_eq_u32 %0, %11\n\t" \
    "s_cselect_b32 %0, %12, %0\n\t" \
    "s_add_u32 m0, %4, 36864\n\t" \
    "s_nop 0\n\t" \
    "buffer_load_dwordx4 %6, %2, %0 offen sc1 lds\n\t" \
    "buffer_load_dwordx4 %7, %2, %0 offen offset:1024 sc1 lds\n\t" \
    "s_add_u32 %0, %0, 128\n\t" \
    "s_cmp_eq_u32 %0, %11\n\t" \
    "s_cselect_b32 %0, %12, %0\n\t" \
    "s_waitcnt vmcnt(4)\n\t" \
    "s_barrier\n\t" \
    "s_add_u32 m0, %4, 49152\n\t" \
    "s_nop 0\n\t" \
    "buffer_load_dwordx4 %6, %2, %0 offen sc1 lds\n\t" \
    "buffer_load_dwordx4 %7, %2, %0 offen offset:1024 sc1 lds\n\t" \
    "s_add_u32 %0, %0, 128\n\t" \
    "s_cmp_eq_u32 %0, %11\n\t" \
    "s_cselect_b32 %0, %12, %0\n\t" \
    "s_add_u32 m0, %4, 61440\n\t" \
    "s_nop 0\n\t" \
    "buffer_load_dwordx4 %6, %2, %0 offen sc1 lds\n\t" \
    "buffer_load_dwordx4 %7, %2, %0 offen offset:1024 sc1 lds\n\t" \
    "s_add_u32 %0, %0, 128\n\t" \
    "s_cmp_eq_u32 %0, %11\n\t" \
    "s_cselect_b32 %0, %12, %0\n\t" \
    "s_add_u32 m0, %5, 0\n\t" \
    "s_nop 0\n\t" \
    "buffer_load_dwordx4 %9, %3, %1 offen lds\n\t" \
    "buffer_load_dwordx4 %10, %3, %1 offen offset:1024 lds\n\t" \
    "s_add_u32 %1, %1, 81920\n\t" \
    "s_cmp_eq_u32 %1, %13\n\t" \
    "s_cselect_b32 %1, %14, %1\n\t" \
    "s_waitcnt vmcnt(6)\n\t" \
    "s_barrier\n\t" \
    "s_add_u32 m0, %4, 0\n\t" \
    "s_nop 0\n\t" \
    "buffer_load_dwordx4 %6, %2, %0 offen sc1 lds\n\t" \
    "buffer_load_dwordx4 %7, %2, %0 offen offset:1024 sc1 lds\n\t" \
    "s_add_u32 %0, %0, 128\n\t" \
    "s_cmp_eq_u32 %0, %11\n\t" \
    "s_cselect_b32 %0, %12, %0\n\t" \
    "s_add_u32 m0, %4, 12288\n\t" \
    "s_nop 0\n\t" \
    "buffer_load_dwordx4 %6, %2, %0 offen sc1 lds\n\t" \
    "buffer_load_dwordx4 %7, %2, %0 offen offset:1024 sc1 lds\n\t" \
    "s_add_u32 %0, %0, 128\n\t" \
    "s_cmp_eq_u32 %0, %11\n\t" \
    "s_cselect_b32 %0, %12, %0\n\t" \
    "s_add_u32 m0, %5, 8192\n\t" \
    "s_nop 0\n\t" \
    "buffer_load_dwordx4 %9, %3, %1 offen lds\n\t" \
    "buffer_load_dwordx4 %10, %3, %1 offen offset:1024 lds\n\t" \
    "s_add_u32 %1, %1, 81920\n\t" \
    "s_cmp_eq_u32 %1, %13\n\t" \
    "s_cselect_b32 %1, %14, %1\n\t" \
    "s_add_u32 m0, %5, 16384\n\t" \
    "s_nop 0\n\t" \
    "buffer_load_dwordx4 %9, %3, %1 offen lds\n\t" \
    "buffer_load_dwordx4 %10, %3, %1 offen offset:1024 lds\n\t" \
    "s_add_u32 %1, %1, 81920\n\t" \
    "s_cmp_eq_u32 %1, %13\n\t" \
    "s_cselect_b32 %1, %14, %1\n\t" \
    "s_waitcnt vmcnt(10)\n\t" \
    "s_barrier\n\t" \
    "s_add_u32 m0, %4, 24576\n\t" \
    "s_nop 0\n\t" \
    "buffer_load_dwordx4 %6, %2, %0 offen sc1 lds\n\t" \
    "buffer_load_dwordx4 %7, %2, %0 offen offset:1024 sc1 lds\n\t" \
    "s_add_u32 %0, %0, 128\n\t" \
    "s_cmp_eq_u32 %0, %11\n\t" \
    "s_cselect_b32 %0, %12, %0\n\t" \
    "s_add_u32 m0, %4, 36864\n\t" \
    "s_nop 0\n\t" \
    "buffer_load_dwordx4 %6, %2, %0 offen sc1 lds\n\t" \
    "buffer_load_dwordx4 %7, %2, %0 offen offset:1024 sc1 lds\n\t" \
    "s_add_u32 %0, %0, 128\n\t" \
    "s_cmp_eq_u32 %0, %11\n\t" \
    "s_cselect_b32 %0, %12, %0\n\t" \
    "s_add_u32 m0, %5, 24576\n\t" \
    "s_nop 0\n\t" \
    "buffer_load_dwordx4 %9, %3, %1 offen lds\n\t" \
    "buffer_load_dwordx4 %10, %3, %1 offen offset:1024 lds\n\t" \
    "s_add_u32 %1, %1, 81920\n\t" \
    "s_cmp_eq_u32 %1, %13\n\t" \
    "s_cselect_b32 %1, %14, %1\n\t" \
    "s_add_u32 m0, %5, 32768\n\t" \
    "s_nop 0\n\t" \
    "buffer_load_dwordx4 %9, %3, %1 offen lds\n\t" \
    "buffer_load_dwordx4 %10, %3, %1 offen offset:1024 lds\n\t" \
    "s_add_u32 %1, %1, 81920\n\t" \
    "s_cmp_eq_u32 %1, %13\n\t" \
    "s_cselect_b32 %1, %14, %1\n\t" \
    "s_waitcnt vmcnt(12)\n\t" \
    "s_barrier\n\t" \
    "s_add_u32 m0, %4, 49152\n\t" \
    "s_nop 0\n\t" \
    "buffer_load_dwordx4 %6, %2, %0 offen sc1 lds\n\t" \
    "buffer_load_dwordx4 %7, %2, %0 offen offset:1024 sc1 lds\n\t" \
    "s_add_u32 %0, %0, 128\n\t" \
    "s_cmp_eq_u32 %0, %11\n\t" \
    "s_cselect_b32 %0, %12, %0\n\t" \
    "s_add_u32 m0, %4, 61440\n\t" \
    "s_nop 0\n\t" \
    "buffer_load_dwordx4 %6, %2, %0 offen sc1 lds\n\t" \
    "buffer_load_dwordx4 %7, %2, %0 offen offset:1024 sc1 lds\n\t" \
    "s_add_u32 %0, %0, 128\n\t" \
    "s_cmp_eq_u32 %0, %11\n\t" \
    "s_cselect_b32 %0, %12, %0\n\t" \
    "s_add_u32 m0, %5, 40960\n\t" \
    "s_nop 0\n\t" \
    "buffer_load_dwordx4 %9, %3, %1 offen lds\n\t" \
    "buffer_load_dwordx4 %10, %3, %1 offen offset:1024 lds\n\t" \
    "s_add_u32 %1, %1, 81920\n\t" \
    "s_cmp_eq_u32 %1, %13\n\t" \
    "s_cselect_b32 %1, %14, %1\n\t" \
    "s_waitcnt vmcnt(10)\n\t" \
    "s_barrier\n\t" \
    "s_add_u32 m0, %4, 0\n\t" \
    "s_nop 0\n\t" \
    "buffer_load_dwordx4 %6, %2, %0 offen sc1 lds\n\t" \
    "buffer_load_dwordx4 %7, %2, %0 offen offset:1024 sc1 lds\n\t" \
    "s_add_u32 %0, %0, 128\n\t" \
    "s_cmp_eq_u32 %0, %11\n\t" \
    "s_cselect_b32 %0, %12, %0\n\t" \
    "s_add_u32 m0, %4, 12288\n\t" \
    "s_nop 0\n\t" \
    "buffer_load_dwordx4 %6, %2, %0 offen sc1 lds\n\t" \
    "buffer_load_dwordx4 %7, %2, %0 offen offset:1024 sc1 lds\n\t" \
    "s_add_u32 %0, %0, 128\n\t" \
    "s_cmp_eq_u32 %0, %11\n\t" \
    "s_cselect_b32 %0, %12, %0\n\t" \
    "s_waitcnt vmcnt(6)\n\t" \
    "s_barrier\n\t" \
    "s_add_u32 m0, %4, 24576\n\t" \
    "s_nop 0\n\t" \
    "buffer_load_dwordx4 %6, %2, %0 offen sc1 lds\n\t" \
    "buffer_load_dwordx4 %7, %2, %0 offen offset:1024 sc1 lds\n\t" \
    "s_add_u32 %0, %0, 128\n\t" \
    "s_cmp_eq_u32 %0, %11\n\t" \
    "s_cselect_b32 %0, %12, %0\n\t" \
    "s_waitcnt vmcnt(2)\n\t" \
    "s_barrier\n\t" \
    "s_waitcnt vmcnt(0)\n\t" \
    "s_barrier\n\t"
#define CL10(a) "v" #a "0", "v" #a "1", "v" #a "2", "v" #a "3", "v" #a "4", "v" #a "5", "v" #a "6", "v" #a "7", "v" #a "8", "v" #a "9"
#define CLOB_HI CL10(16), CL10(17), CL10(18), CL10(19), CL10(20), CL10(21), CL10(22), CL10(23), CL10(24), "v250", "v251", "v252", "v253", "v254", "v255"
#define CLOB1 "v0", "v1", "v2", "v3", "v4", "v5", "v6", "v7", "v8", "v9", CL10(1), CL10(2), CL10(3), CL10(4), CL10(5), CL10(6), CL10(7), CL10(8), CL10(9), CL10(10), CL10(11), \
    "v120", "v121", "v122", "v123", "v124", "v125", "v126", "v127", CLOB_HI
#define CLOB2 "v48", "v49", CL10(5), CL10(6), CL10(7), CL10(8), CL10(9), CL10(10), CL10(11), CL10(12), CL10(13), CL10(14), CL10(15), CLOB_HI

template <int NH> DI void p2_stream(unsigned h0, unsigned w0, int rot, u32x4 rh, u32x4 rw, unsigned lh, unsigned lw, unsigned v0, unsigned v1, unsigned v2, unsigned d0, unsigned d1) {
    const int kw = rot + 21 >= 27 ? rot + 21 - 27 : rot + 21;
    unsigned sh = h0 + rot * 128, sw = w0 + kw * 64 * XROW;
    const unsigned he = h0 + 27 * 128, we = w0 + 27 * 64 * XROW;
    if constexpr (NH == 3)
        asm volatile(P2_LOAD3 : "+s"(sh), "+s"(sw) : "s"(rh), "s"(rw), "s"(lh), "s"(lw), "v"(v0), "v"(v1), "v"(v2), "v"(d0), "v"(d1), "s"(he), "s"(h0), "s"(we), "s"(w0)
                     : "memory", "m0", "scc");
    else
        asm volatile(P2_LOAD2 : "+s"(sh), "+s"(sw) : "s"(rh), "s"(rw), "s"(lh), "s"(lw), "v"(v0), "v"(v1), "v"(v2), "v"(d0), "v"(d1), "s"(he), "s"(h0), "s"(we), "s"(w0)
                     : "memory", "m0", "scc");
}

__global__ __launch_bounds__(512, 2) void geglu_mlp(const void* __restrict__ x, const void* __restrict__ wg, const void* __restrict__ wu,
                                                    const void* __restrict__ wd, void* __restrict__ out, char* __restrict__ ws) {
    extern __shared__ __attribute__((aligned(16))) char smem_[];
    LAS char* smem = (LAS char*)smem_;
    const unsigned lds0 = (unsigned)(size_t)smem;
    const int lane = threadIdx.x & 63, w = rfl(threadIdx.x >> 6);
    const int xcd = blockIdx.x & 7, j = ((blockIdx.x >> 3) + 28) & 31;
    const int fs = (xcd & 3) * 7 + (j >> 2), pm = (xcd >> 2) * 4 + (j & 3);
    const bool prod = j < 28 && fs < 27, cons = j < 30;
    const int mt = j / 10, nt = j - mt * 10, ntok = mt == 2 ? 64 : 96, rot = (nt % 3) * 9;
    GAS u64* flags = (GAS u64*)(ws + WS_FLAGS);
    GAS u64* arrive = flags + 8 * 32 * 16;
    GAS u64* epoch = arrive + 256;

    if (w < 4) {
        const u64 e0 = __hip_atomic_load(epoch, __ATOMIC_RELAXED, __HIP_MEMORY_SCOPE_AGENT);
        const u64 tag = (((u64)rfl((unsigned)(e0 >> 32)) << 32) | rfl((unsigned)e0)) * LCG_A + LCG_C;
        if (threadIdx.x == 0) {
            __hip_atomic_store(arrive + blockIdx.x, tag, __ATOMIC_RELAXED, __HIP_MEMORY_SCOPE_AGENT);
            *(LAS unsigned*)(smem + MISC) = 0u; *(LAS unsigned*)(smem + MISC + 4) = 0u;
        }
        const int wm = w >> 1, wn = w & 1;
        if (prod) {
            const int q4 = lane >> 4, tq = (lane & 15) >> 2, tp = lane & 3;
            const unsigned tsw = (((tq >> 1) & 1) | ((q4 & 1) << 1)) << 1;
            const unsigned ax = lds0 + (wm * 128 + (lane & 15)) * 128 + (q4 ^ (lane & 6)) * 16;
            const unsigned aw = lds0 + (q4 >> 1) * 4096 + (8 * (q4 & 1) + tq) * 128 + (((wn * 4 + (tp >> 1)) ^ tsw) * 16) + 8 * (tp & 1);
            const unsigned ho = (pm >> 1) * HPAGE + ((pm & 1) * 256 + wm * 128 + (lane & 15)) * WROW + (fs * 64 + wn * 32 + (q4 & 1) * 16 + (q4 >> 1) * 8) * 2;
            const f32x2 c1 = {-2.302208198f, -2.302208198f};
            const u64 c2 = 0xbdd2d3e8ull;
            const u32x4 rh = mksrd(ws, 4 * HPAGE);
            unsigned cnt, ro = 0;
            WAIT_L(0);
            asm volatile(P1_INIT ::: CLOB1);
            asm volatile(P1_LOOP : "=&s"(cnt)
                         : "v"(ax), "v"(ax ^ 64), "v"(ax + P1_SLOT), "v"((ax ^ 64) + P1_SLOT), "v"(ax + 2 * P1_SLOT), "v"((ax ^ 64) + 2 * P1_SLOT),
                           "v"(aw), "v"(aw ^ 32), "v"(aw + P1_SLOT), "v"((aw ^ 32) + P1_SLOT), "v"(aw + 2 * P1_SLOT), "v"((aw ^ 32) + 2 * P1_SLOT)
                         : "memory", "scc", CLOB1);
            asm volatile(P1_TAIL : "+s"(ro) : "v"(ho), "v"(c1), "s"(c2), "s"(rh) : "memory", "scc", CLOB1);
        }
        if (cons) {
            const int tw = ntok >> 1;
            const int l2 = lane_id(), g2 = l2 >> 4, r2 = (l2 & 15) >> 2, p2 = l2 & 3;
            const unsigned sw2 = (((r2 >> 1) & 1) | ((g2 & 1) << 1)) << 1;
            const unsigned ah = lds0 + H_BASE + (wm * tw + (l2 & 15)) * 128 + (g2 ^ (l2 & 6)) * 16;
            const unsigned aw = lds0 + (8 * g2 + r2) * 128 + (((wn * 4 + (p2 >> 1)) ^ sw2) * 16) + 8 * (p2 & 1);
            WAIT_L(0);
            BAR();
            asm volatile(P2_WLOAD :: "v"(aw + H_BASE), "v"((aw ^ 32) + H_BASE), "v"(aw + 6 * WD_SLOT), "v"((aw ^ 32) + 6 * WD_SLOT) : "memory", CLOB2);
            if (prod) {
                WAIT_V(0);
                unsigned old = 0;
                if (l2 == 0) old = __hip_atomic_fetch_add((LAS unsigned*)(smem + MISC), 1u, __ATOMIC_RELAXED, __HIP_MEMORY_SCOPE_WORKGROUP);
                if (rfl(old) == 3 && l2 == 0) __hip_atomic_store(flags + (pm * 32 + fs) * 16, tag, __ATOMIC_RELAXED, __HIP_MEMORY_SCOPE_AGENT);
            }
            f32x4 a00 = {0.f, 0.f, 0.f, 0.f}, a01 = a00, a10 = a00, a11 = a00, a20 = a00, a21 = a00;
            asm volatile("" : "+v"(a00), "+v"(a01), "+v"(a10), "+v"(a11), "+v"(a20), "+v"(a21));
            WAIT_L(0);
            BAR();
            asm volatile(P2_MFMA : "+v"(a00), "+v"(a01), "+v"(a10), "+v"(a11), "+v"(a20), "+v"(a21)
                         : "v"(ah), "v"(ah ^ 64), "v"(ah + 3 * H_SLOT), "v"((ah ^ 64) + 3 * H_SLOT), "v"(aw), "v"(aw ^ 32), "v"(aw + 6 * WD_SLOT), "v"((aw ^ 32) + 6 * WD_SLOT)
                         : "memory", CLOB2);
            const __amdgpu_buffer_rsrc_t ro = __builtin_amdgcn_make_buffer_rsrc(out, 0, M_ * D_ * 2, 0x00020000);
            const int lo = lane_id();
            const unsigned oo = (xcd * 256 + mt * 96 + wm * tw + (lo & 15)) * XROW + (nt * 64 + wn * 32 + ((lo >> 4) & 1) * 16 + (lo >> 5) * 8) * 2;
            auto put = [&](const f32x4& p, const f32x4& q, int m) {
                const auto s0 = __builtin_amdgcn_permlane16_swap(pk(p[0], p[1]), pk(q[0], q[1]), false, false);
                const auto s1 = __builtin_amdgcn_permlane16_swap(pk(p[2], p[3]), pk(q[2], q[3]), false, false);
                const u32x4 v = {s0[0], s1[0], s0[1], s1[1]};
                __builtin_amdgcn_raw_buffer_store_b128(v, ro, oo + m * 16 * XROW, 0, 16);
            };
            put(a00, a01, 0); put(a10, a11, 1);
            if (ntok == 96) put(a20, a21, 2);
        }
    } else {
        const int lw = w - 4;
        unsigned pf0 = 0, pf1 = 0, pf2 = 0, pf3 = 0;
        if (!prod) {
            if (lw == 3) {
                u64 pc; asm volatile("s_getpc_b64 %0" : "=s"(pc));
                const char* a = (const char*)(pc & ~255ull) + lane * 128;
                asm volatile("global_load_dword %0, %2, off\n\tglobal_load_dword %1, %3, off" : "+v"(pf0), "+v"(pf1) : "v"(a), "v"(a + 8192) : "memory");
                if (lane < CODE_LINES - 128) asm volatile("global_load_dword %0, %1, off" : "+v"(pf3) : "v"(a + 16384) : "memory");
            }
            if (lw == 2) {
                auto touch = [&](const void* base, unsigned size) {
                    const unsigned o = lane * TLB_STEP;
                    if (o < size + TLB_STEP) asm volatile("global_load_dword %0, %1, off" : "+v"(pf2) : "v"((const char*)base + (o < size ? o : size - 4)) : "memory");
                };
                touch(wg, F_ * D_ * 2); touch(wu, F_ * D_ * 2); touch(x, M_ * D_ * 2); touch(wd, F_ * D_ * 2); touch(out, M_ * D_ * 2);
                if (lane < 4) asm volatile("global_load_dword %0, %1, off" : "+v"(pf2) : "v"((const char*)ws + (size_t)lane * HPAGE + HPAGE - 128) : "memory");
            }
        }
        u64 e0;
        asm volatile("global_load_dwordx2 %0, %1, off sc1" : "=&v"(e0) : "v"(epoch) : "memory");
        const unsigned r8 = lane >> 3, c8 = lane & 7;
        const unsigned swx = (c8 ^ (r8 & 6)) * 16;
        const unsigned sww0 = (c8 ^ (((r8 >> 1) & 1) << 1)) * 16;
        const unsigned sww1 = (c8 ^ ((((r8 >> 1) & 1) | 2) << 1)) * 16;
        constexpr unsigned DX = 8 * XROW - 1024, DW = 8 * WROW - 1024;
        const u32x4 rwd = mksrd(wd, F_ * D_ * 2);
        const unsigned vd0 = r8 * XROW + sww0, vd1 = r8 * XROW + sww1 + DX;
        auto wd_off = [&](int k) { int tr = k + rot; if (tr >= 27) tr -= 27; return (unsigned)((tr * 64 + lw * 16) * XROW + nt * 128); };
        auto wd_tiles = [&](int i0, int i1) {
#pragma unroll 1
            for (int i = i0; i < i1; ++i) {
                const int k = i < 9 ? i + 10 : i < 11 ? i - 1 : i - 11;
                dma2(lds0 + i * WD_SLOT + lw * 2048, rwd, wd_off(k), vd0, vd1);
            }
        };
        if (prod) {
            const u32x4 rx = mksrd(x, M_ * D_ * 2), rg = mksrd(wg, D_ * F_ * 2), ru = mksrd((const char*)wu - 2048, D_ * F_ * 2 + 2048);
            const unsigned vx0 = r8 * XROW + swx, vx1 = vx0 + DX, vx2 = vx0 + 2 * DX, vx3 = vx0 + 3 * DX;
            const unsigned vg0 = r8 * WROW + sww0, vg1 = r8 * WROW + sww1 + DW;
            unsigned sx = (pm * 256 + lw * 64) * XROW, sw = lw * 16 * WROW + fs * 128, sl = 2 * P1_SLOT;
            auto stage = [&]() {
                const unsigned b = lds0 + sl;
                dma22(b + 32768 + lw * 4096, rg, ru, sw, vg0, vg1);
                dma4(b + lw * 8192, rx, sx, vx0, vx1, vx2, vx3);
                dma4(b + lw * 8192 + 4096, rx, sx + 32 * XROW, vx0, vx1, vx2, vx3);
                sx += 128; sw += 64 * WROW; sl = sl == 2 * P1_SLOT ? 0 : sl + P1_SLOT;
            };
            stage(); stage();
            WAIT_V(12); BAR();
            stage();
            WAIT_V(12); BAR();
#pragma unroll 1
            for (int t = 1; t < 8; ++t) { stage(); WAIT_V(12); BAR(); }
            wd_tiles(0, 6);
            WAIT_V(12); BAR();
            wd_tiles(6, 12);
            BAR();
            wd_tiles(12, 19);
        } else {
            const int sp = (j >= 28 ? xcd * 4 + (j - 28) : 32 + (xcd >> 2) * 4 + (j & 3)) * 4 + lw;
            const u32x4 sx_ = mksrd(x, M_ * D_ * 2), sg_ = mksrd(wg, D_ * F_ * 2), su_ = mksrd(wu, D_ * F_ * 2);
            unsigned dummy = 0;
#define TOUCH(r, v, s) asm volatile("buffer_load_dword %0, %1, %2, %3 offen" : "+v"(dummy) : "v"(v), "s"(r), "s"((unsigned)(s)) : "memory")
#pragma unroll 1
            for (int g = sp; g < 9 * 86; g += 160) {
                const int kt = g / 86 + 1, r = g % 86;
                if (r < 32) TOUCH(sx_, lane * XROW, r * 64 * XROW + kt * 128);
                else if (r < 59) TOUCH(sg_, lane * WROW, kt * 64 * WROW + (r - 32) * 128);
                else TOUCH(su_, lane * WROW, kt * 64 * WROW + (r - 59) * 128);
            }
#pragma unroll 1
            for (int g = sp; g < 270; g += 160) { const int kt = g / 10, sl = g - kt * 10; TOUCH(rwd, lane * XROW, kt * 64 * XROW + sl * 128); }
            if (cons) wd_tiles(0, 19);
            WAIT_V(0);
            asm volatile("" :: "v"(dummy), "v"(pf0), "v"(pf1), "v"(pf2), "v"(pf3));
        }
        WAIT_V(0);
        asm volatile("" : "+v"(e0));
        const u64 tag = e0 * LCG_A + LCG_C;
        if (cons) {
            BAR();
            BAR();
            dma2(lds0 + 9 * WD_SLOT + lw * 2048, rwd, wd_off(19), vd0, vd1);
            dma2(lds0 + 10 * WD_SLOT + lw * 2048, rwd, wd_off(20), vd0, vd1);
            WAIT_V(0);
            if (lw == 2) {
                GAS u64* pf = flags + (xcd * 32 + (lane < 27 ? lane : 0)) * 16;
                const u64 t0 = __builtin_amdgcn_s_memrealtime();
                while (!__all(__hip_atomic_load(pf, __ATOMIC_RELAXED, __HIP_MEMORY_SCOPE_AGENT) == tag) && __builtin_amdgcn_s_memrealtime() - t0 < 5000000ull) __builtin_amdgcn_s_sleep(1);
                *(volatile LAS unsigned*)(smem + MISC + 4) = 1u;
            } else {
                while (*(volatile LAS unsigned*)(smem + MISC + 4) == 0u) __builtin_amdgcn_s_sleep(1);
            }
            const int nh = ntok >> 5;
            const unsigned h0 = (xcd >> 1) * HPAGE + ((xcd & 1) * 256 + mt * 96 + lw * nh * 8) * WROW, w0 = lw * 16 * XROW + nt * 128;
            const unsigned v0 = r8 * WROW + swx;
            if (nh == 3) p2_stream<3>(h0, w0, rot, mksrd(ws, 4 * HPAGE), rwd, lds0 + H_BASE + lw * 3072, lds0 + lw * 2048, v0, v0 + DW, v0 + 2 * DW, vd0, vd1);
            else p2_stream<2>(h0, w0, rot, mksrd(ws, 4 * HPAGE), rwd, lds0 + H_BASE + lw * 2048, lds0 + lw * 2048, v0, v0 + DW, v0 + 2 * DW, vd0, vd1);
        }
        if (blockIdx.x == 0 && lw == 0) {
            const u64 t0 = __builtin_amdgcn_s_memrealtime();
            for (;;) {
                bool ok = true;
#pragma unroll
                for (int i = 0; i < 4; ++i) ok &= __hip_atomic_load(arrive + i * 64 + lane, __ATOMIC_RELAXED, __HIP_MEMORY_SCOPE_AGENT) == tag;
                if (__all(ok) || __builtin_amdgcn_s_memrealtime() - t0 > 5000000ull) break;
                __builtin_amdgcn_s_sleep(1);
            }
            if (lane == 0) __hip_atomic_store(epoch, tag, __ATOMIC_RELAXED, __HIP_MEMORY_SCOPE_AGENT);
        }
    }
}

extern "C" void kernel_launch(void* const* d_in, const int* in_sizes, int n_in, void* d_out, int out_size, void* d_ws, size_t ws_size, hipStream_t stream) {
    (void)in_sizes; (void)n_in; (void)out_size; (void)ws_size;
    (void)hipFuncSetAttribute((const void*)geglu_mlp, hipFuncAttributeMaxDynamicSharedMemorySize, LDS_BYTES);
    geglu_mlp<<<256, 512, LDS_BYTES, stream>>>(d_in[0], d_in[1], d_in[2], d_in[3], d_out, (char*)d_ws);
}
```
